```python
import torch

def _softmax(x, axis=-1):
    return torch.softmax(x, dim=axis)
def _gelu(x):
    return torch.nn.functional.gelu(x, approximate="tanh")
def _silu(x):
    return torch.nn.functional.silu(x)
def _sigmoid(x):
    return torch.sigmoid(x)
def _layernorm(x, axis=-1, eps=1e-5):
    m = x.mean(dim=axis, keepdim=True)
    v = x.var(dim=axis, keepdim=True, unbiased=False)
    return (x - m) / torch.sqrt(v + eps)
def _scatter_add(tgt, idx, src, axis=0):
    return tgt.clone().index_add_(axis, idx, src)
def _segment_sum(src, seg, n):
    out = torch.zeros((n,) + src.shape[1:], dtype=src.dtype)
    return out.index_add_(0, seg, src)
def _pad(x, pw):
    flat = tuple(v for pair in reversed(pw) for v in pair)
    return torch.nn.functional.pad(x, flat)

kernel_name = 'recurrent_scan__gqa_block_cached__swiglu_ffn__decode'

def setup_inputs():

    _w = (lambda a: (lambda t: t.to(torch.bfloat16) if t.is_floating_point() else t)(torch.as_tensor(a)))
    def _A_setup():
        B, Sq, D = 2, 1, 1024
        Hq, Hkv, Sk, Dh = 32, 4, 32768, 128
        assert Hq % Hkv == 0, (Hq, Hkv)
        return {
            "x": _w((lambda *s: torch.randn(*s, dtype=torch.bfloat16) * 0.05)(B, Sq, D)),
            "k_cache": _w((lambda *s: torch.randn(*s, dtype=torch.bfloat16) * 1.6)(B, Hkv, Sk, Dh)),
            "v_cache": _w((lambda *s: torch.randn(*s, dtype=torch.bfloat16) * 0.2)(B, Hkv, Sk, Dh)),
            "wq": _w((lambda *s: torch.randn(*s, dtype=torch.bfloat16) * 1.6)(D, Hq * Dh) * (D ** -0.5)),
            "wo": _w((lambda *s: torch.randn(*s, dtype=torch.bfloat16) * 0.2)(Hq * Dh, D) * ((Hq * Dh) ** -0.5)),
        }
    _A_in = _A_setup()

    def _B_setup():
        B, S, D, F = 2, 1, 1024, 3584
        return {
            "x": _w((lambda *s: torch.randn(*s, dtype=torch.bfloat16) * 0.05)(B, S, D)),
            "w_gate": _w((lambda *s: torch.randn(*s, dtype=torch.bfloat16) * 0.1)(D, F)),
            "w_up": _w((lambda *s: torch.randn(*s, dtype=torch.bfloat16) * 0.1)(D, F)),
            "w_down": _w((lambda *s: torch.randn(*s, dtype=torch.bfloat16) * 0.1)(F, D)),
        }
    _B_in = _B_setup()

    return {
        "A_x": _A_in["x"],
        "A_k_cache": _A_in["k_cache"],
        "A_v_cache": _A_in["v_cache"],
        "A_wq": _A_in["wq"],
        "A_wo": _A_in["wo"],
        "B_x": _B_in["x"],
        "B_w_gate": _B_in["w_gate"],
        "B_w_up": _B_in["w_up"],
        "B_w_down": _B_in["w_down"],
    }

def reference(A_x, A_k_cache, A_v_cache, A_wq, A_wo, B_x, B_w_gate, B_w_up, B_w_down):
    def _A_ref(x, k_cache, v_cache, wq, wo):
        B, Sq, D = x.shape
        Hq, Dh = 32, 128
        Hkv = k_cache.shape[1]
        rep = Hq // Hkv
        q = (lambda x, *d: x.permute(*d))((x @ wq).reshape(B, Sq, Hq, Dh), 0, 2, 1, 3)
        k = (lambda x, n, axis: torch.repeat_interleave(x, n, dim=axis))(k_cache, rep, axis=1)
        v = (lambda x, n, axis: torch.repeat_interleave(x, n, dim=axis))(v_cache, rep, axis=1)
        scores = torch.einsum("bhqd,bhkd->bhqk", q, k) * (Dh ** -0.5)
        p = _softmax(scores, axis=-1)
        o = torch.einsum("bhqk,bhkd->bhqd", p, v)
        return (lambda x, *d: x.permute(*d))(o, 0, 2, 1, 3).reshape(B, Sq, Hq * Dh) @ wo

    def _B_ref(x, w_gate, w_up, w_down):
        g = _silu(x @ w_gate)
        u = x @ w_up
        return (g * u) @ w_down

    x = A_x
    xn = _layernorm(x, axis=-1)
    s = _A_ref(xn, A_k_cache, A_v_cache, A_wq, A_wo)
    g = _silu(_B_ref(xn, B_w_gate, B_w_up, B_w_down))
    return x + s * g
```

```cpp
#include <hip/hip_runtime.h>
#include <hip/hip_bf16.h>
#include <stdint.h>

#define GAS __attribute__((address_space(1)))
#define LAS __attribute__((address_space(3)))
typedef unsigned v4u __attribute__((ext_vector_type(4)));
typedef unsigned v2u __attribute__((ext_vector_type(2)));
typedef float f32x4 __attribute__((ext_vector_type(4)));
typedef short bf16x8 __attribute__((ext_vector_type(8)));
typedef short bf16x4 __attribute__((ext_vector_type(4)));
typedef unsigned long long u64;

namespace {

constexpr int NWG = 256, NTHR = 512;
constexpr int D = 1024, HD = 4096, FF = 3584, SK = 32768;
constexpr int AUX_SC1 = 16;
constexpr unsigned SENT = 0xFFFFFFFFu;
constexpr unsigned SPIN_CAP = 1u << 22;

constexpr int WS_E = 0;
constexpr int WS_QG = 4096;
constexpr int WS_GU = WS_QG + 262144;
constexpr int WS_P1 = WS_GU + 262144;
constexpr int WS_LSE = WS_P1 + 1048576;
constexpr int WS_S2 = WS_LSE + 8192;
constexpr int WS_F2 = WS_S2 + 524288;
constexpr int WS_END = WS_F2 + 524288;
constexpr int WS_ALL = WS_END;

constexpr int L_RED = 0;
constexpr int L_Q = 12288;
constexpr int L_ML = L_Q + 16 * 272;
constexpr int L_O = L_ML + 512;
constexpr int L_S = L_O + 32768;
constexpr int L_F = L_S + 16384;
constexpr int L_USED = L_F + 16384;
constexpr int L_BYTES = 84 * 1024;
static_assert(L_USED <= L_BYTES, "lds");

struct Params {
  const unsigned short* x;
  const unsigned short* kc;
  const unsigned short* vc;
  const unsigned short* wq;
  const unsigned short* wo;
  const unsigned short* wg;
  const unsigned short* wu;
  const unsigned short* wd;
  unsigned short* out;
  unsigned char* ws;
};

__device__ __forceinline__ float bflo(unsigned u) { return __builtin_bit_cast(float, u << 16); }
__device__ __forceinline__ float bfhi(unsigned u) { return __builtin_bit_cast(float, u & 0xFFFF0000u); }
__device__ __forceinline__ unsigned f2u(float f) { return __builtin_bit_cast(unsigned, f); }
__device__ __forceinline__ float u2f(unsigned u) { return __builtin_bit_cast(float, u); }
typedef __bf16 bf16p __attribute__((ext_vector_type(2)));
typedef float f32x2 __attribute__((ext_vector_type(2)));
__device__ __forceinline__ unsigned cvtpk(float lo, float hi) {
  return __builtin_bit_cast(unsigned, __builtin_convertvector(f32x2{lo, hi}, bf16p));
}
template <int CTRL> __device__ __forceinline__ float dpp(float x) {
  return __builtin_bit_cast(float, __builtin_amdgcn_mov_dpp(__builtin_bit_cast(int, x), CTRL, 0xf, 0xf, true));
}
constexpr int XOR1 = 0xB1, XOR2 = 0x4E, XOR7 = 0x141, XOR8 = 0x128, ROR4 = 0x124, XOR15 = 0x140;
__device__ __forceinline__ float x16_sum(float x) {
  auto s = __builtin_amdgcn_permlane16_swap(f2u(x), f2u(x), false, false);
  return u2f(s[0]) + u2f(s[1]);
}
__device__ __forceinline__ float x32_sum(float x) {
  auto s = __builtin_amdgcn_permlane32_swap(f2u(x), f2u(x), false, false);
  return u2f(s[0]) + u2f(s[1]);
}
__device__ __forceinline__ float x16_max(float x) {
  auto s = __builtin_amdgcn_permlane16_swap(f2u(x), f2u(x), false, false);
  return fmaxf(u2f(s[0]), u2f(s[1]));
}
__device__ __forceinline__ float x32_max(float x) {
  auto s = __builtin_amdgcn_permlane32_swap(f2u(x), f2u(x), false, false);
  return fmaxf(u2f(s[0]), u2f(s[1]));
}
__device__ __forceinline__ float row16_sum(float x) {
  x += dpp<XOR1>(x);
  x += dpp<XOR2>(x);
  x += dpp<XOR7>(x);
  x += dpp<XOR8>(x);
  return x;
}
__device__ __forceinline__ float row16_max(float x) {
  x = fmaxf(x, dpp<XOR1>(x));
  x = fmaxf(x, dpp<XOR2>(x));
  x = fmaxf(x, dpp<XOR7>(x));
  x = fmaxf(x, dpp<XOR8>(x));
  return x;
}
__device__ __forceinline__ float wave_sum(float x) { return x32_sum(x16_sum(row16_sum(x))); }
__device__ __forceinline__ float tr16(float a, float b) {
  auto s = __builtin_amdgcn_permlane16_swap(f2u(a), f2u(b), false, false);
  return u2f(s[0]) + u2f(s[1]);
}
__device__ __forceinline__ float tr32(float a, float b) {
  auto s = __builtin_amdgcn_permlane32_swap(f2u(a), f2u(b), false, false);
  return u2f(s[0]) + u2f(s[1]);
}
__device__ __forceinline__ v4u ldnt(const GAS v4u* p) { return __builtin_nontemporal_load(p); }
__device__ __forceinline__ v2u ldnt2(const GAS v2u* p) { return __builtin_nontemporal_load(p); }
__device__ __forceinline__ unsigned clean(float f) {
  const unsigned u = f2u(f);
  return u == SENT ? 0x7FC00000u : u;
}
__device__ __forceinline__ v4u clean4(f32x4 v) { return v4u{clean(v.x), clean(v.y), clean(v.z), clean(v.w)}; }
__device__ __forceinline__ bool full4(v4u v) { return v.x != SENT && v.y != SENT && v.z != SENT && v.w != SENT; }
__device__ __forceinline__ float rdl(float v, int lane) { return u2f(__builtin_amdgcn_readlane(f2u(v), lane)); }
__device__ __forceinline__ float silu(float v) { return v / (1.0f + __expf(-v)); }

#define SB() __builtin_amdgcn_sched_barrier(0)
#define WG_BARRIER()                                        \
  do {                                                      \
    asm volatile("s_waitcnt lgkmcnt(0)" ::: "memory");      \
    __builtin_amdgcn_s_barrier();                           \
    asm volatile("" ::: "memory");                          \
  } while (0)
#define STAMP(i)

__device__ __forceinline__ f32x4 gemv64(const v4u (&w)[4], const float (&x0)[4], const float (&x1)[4]) {
  float a0[8], a1[8];
#pragma unroll
  for (int c = 0; c < 8; ++c) { a0[c] = 0.f; a1[c] = 0.f; }
#pragma unroll
  for (int i = 0; i < 4; ++i) {
#pragma unroll
    for (int k = 0; k < 4; ++k) {
      const float lo = bflo(w[i][k]), hi = bfhi(w[i][k]);
      a0[2 * k] = fmaf(lo, x0[i], a0[2 * k]);
      a0[2 * k + 1] = fmaf(hi, x0[i], a0[2 * k + 1]);
      a1[2 * k] = fmaf(lo, x1[i], a1[2 * k]);
      a1[2 * k + 1] = fmaf(hi, x1[i], a1[2 * k + 1]);
    }
  }
  float t[8];
#pragma unroll
  for (int c = 0; c < 8; ++c) t[c] = tr16(a0[c], a1[c]);
  f32x4 r;
#pragma unroll
  for (int c = 0; c < 4; ++c) {
    float v = tr32(t[c], t[c + 4]);
    v += dpp<XOR8>(v);
    r[c] = v;
  }
  return r;
}

__device__ __forceinline__ void gemv256(const v2u (&w)[8], const float (&c0)[8], const float (&c1)[8], f32x4& s0, f32x4& s1) {
  s0 = f32x4{0.f, 0.f, 0.f, 0.f};
  s1 = f32x4{0.f, 0.f, 0.f, 0.f};
#pragma unroll
  for (int k = 0; k < 8; ++k) {
    const f32x4 wf = {bflo(w[k].x), bfhi(w[k].x), bflo(w[k].y), bfhi(w[k].y)};
#pragma unroll
    for (int c = 0; c < 4; ++c) {
      s0[c] = fmaf(wf[c], c0[k], s0[c]);
      s1[c] = fmaf(wf[c], c1[k], s1[c]);
    }
  }
}

__global__ void __launch_bounds__(NTHR, 2) fused_decode(Params p) {
  __shared__ __attribute__((aligned(16))) unsigned char smem[L_BYTES];
  LAS unsigned char* L = (LAS unsigned char*)smem;
  const int tid = threadIdx.x, lane = tid & 63, w = __builtin_amdgcn_readfirstlane(tid >> 6);
  const int bid = blockIdx.x;
  const int rg = bid >> 2, cg = bid & 3;
  const int hkv = bid >> 6, bb = (bid >> 5) & 1, sp = bid & 31;
  const bool has_ffn = rg < 56;
  const __amdgpu_buffer_rsrc_t rs = __builtin_amdgcn_make_buffer_rsrc((void*)p.ws, 0, WS_ALL, 0x00020000);
  STAMP(0);

  {
    const v4u s4 = {SENT, SENT, SENT, SENT};
    int off;
    if (tid < 256) off = WS_P1 + bid * 4096 + tid * 16;
    else if (tid < 384) off = WS_S2 + (cg * 64 + rg) * 2048 + (tid - 256) * 16;
    else off = WS_F2 + (cg * 64 + rg) * 2048 + (tid - 384) * 16;
    __builtin_amdgcn_raw_buffer_store_b128(s4, rs, off, 0, AUX_SC1);
    if (tid < 64) __builtin_amdgcn_raw_buffer_store_b128(s4, rs, WS_GU + bid * 1024 + tid * 16, 0, AUX_SC1);
    else if (tid < 66) __builtin_amdgcn_raw_buffer_store_b128(s4, rs, WS_LSE + bid * 32 + (tid - 64) * 16, 0, AUX_SC1);
  }
  SB();
  const v2u ev = __builtin_amdgcn_raw_buffer_load_b64(rs, WS_E, 0, AUX_SC1);
  const GAS v4u* xg = (const GAS v4u*)p.x;
  const v4u xa0 = xg[lane * 2], xa1 = xg[lane * 2 + 1], xb0 = xg[128 + lane * 2], xb1 = xg[128 + lane * 2 + 1];
  const int r8 = lane >> 3, c8 = lane & 7;
  const int krow = cg * 256 + w * 32 + r8 * 4;
  const v2u xr0 = *(const GAS v2u*)(p.x + krow), xr1 = *(const GAS v2u*)(p.x + D + krow);
  v4u wqr[4], wgr[4], wur[4];
#pragma unroll
  for (int i = 0; i < 4; ++i) wqr[i] = ldnt((const GAS v4u*)(p.wq + (size_t)(krow + i) * HD + rg * 64 + c8 * 8));
  const int rgf = has_ffn ? rg : 0;
#pragma unroll
  for (int i = 0; i < 4; ++i) wgr[i] = ldnt((const GAS v4u*)(p.wg + (size_t)(krow + i) * FF + rgf * 64 + c8 * 8));
  SB();

  float x0[4], x1[4];
  {
    float va[16], vb[16];
#pragma unroll
    for (int k = 0; k < 4; ++k) {
      va[2 * k] = bflo(xa0[k]); va[2 * k + 1] = bfhi(xa0[k]); va[8 + 2 * k] = bflo(xa1[k]); va[8 + 2 * k + 1] = bfhi(xa1[k]);
      vb[2 * k] = bflo(xb0[k]); vb[2 * k + 1] = bfhi(xb0[k]); vb[8 + 2 * k] = bflo(xb1[k]); vb[8 + 2 * k + 1] = bfhi(xb1[k]);
    }
    float sa = 0.f, sb = 0.f;
#pragma unroll
    for (int k = 0; k < 16; ++k) { sa += va[k]; sb += vb[k]; }
    const float ma = wave_sum(sa) * (1.0f / D), mb = wave_sum(sb) * (1.0f / D);
    float qa = 0.f, qb = 0.f;
#pragma unroll
    for (int k = 0; k < 16; ++k) { const float da = va[k] - ma, db = vb[k] - mb; qa = fmaf(da, da, qa); qb = fmaf(db, db, qb); }
    const float ra = 1.0f / sqrtf(wave_sum(qa) * (1.0f / D) + 1e-5f), rb = 1.0f / sqrtf(wave_sum(qb) * (1.0f / D) + 1e-5f);
    x0[0] = (bflo(xr0.x) - ma) * ra; x0[1] = (bfhi(xr0.x) - ma) * ra; x0[2] = (bflo(xr0.y) - ma) * ra; x0[3] = (bfhi(xr0.y) - ma) * ra;
    x1[0] = (bflo(xr1.x) - mb) * rb; x1[1] = (bfhi(xr1.x) - mb) * rb; x1[2] = (bflo(xr1.y) - mb) * rb; x1[3] = (bfhi(xr1.y) - mb) * rb;
  }
  const u64 e1 = ((((u64)__builtin_amdgcn_readfirstlane(ev.y)) << 32) | (u64)(unsigned)__builtin_amdgcn_readfirstlane(ev.x)) + 1ull;
  const unsigned tag_hi = (unsigned)(e1 >> 8), tag_lo = (unsigned)(e1 & 0xFFull);
  const int rho = lane >> 4;
  const int redoff = ((rho & 1) * 64 + c8 * 8 + (rho >> 1) * 4) * 4;

  {
    const f32x4 r = gemv64(wqr, x0, x1);
    if ((lane & 8) == 0) *(LAS f32x4*)(L + L_RED + w * 512 + redoff) = r;
  }
#pragma unroll
  for (int i = 0; i < 4; ++i) wur[i] = ldnt((const GAS v4u*)(p.wu + (size_t)(krow + i) * FF + rgf * 64 + c8 * 8));
  SB();
  WG_BARRIER();
  if (tid < 128) {
    float s = 0.f;
#pragma unroll
    for (int ww = 0; ww < 8; ++ww) s += *(LAS float*)(L + L_RED + ww * 512 + tid * 4);
    const unsigned v24 = (f2u(s) + 0x80u) >> 8;
    const v2u gr = {(tag_lo << 24) | v24, tag_hi};
    const int b = tid >> 6, c = tid & 63;
    __builtin_amdgcn_raw_buffer_store_b64(gr, rs, WS_QG + (((hkv * 2 + b) * 4 + cg) * 1024 + (rg & 15) * 64 + c) * 8, 0, AUX_SC1);
  }
  STAMP(1);
  SB();

  v2u wdr[8], wor[8];
#pragma unroll
  for (int k = 0; k < 8; ++k) wdr[k] = ldnt2((const GAS v2u*)(p.wd + (size_t)(rgf * 64 + w * 8 + k) * D + cg * 256 + lane * 4));
  SB();
  {
    const f32x4 r = gemv64(wgr, x0, x1);
    if ((lane & 8) == 0) *(LAS f32x4*)(L + L_RED + 4096 + w * 512 + redoff) = r;
  }
  const int dg = (rg & 1) * 8 + w;
  {
    const int rbase = (rg >> 1) * 128 + (dg >> 2) * 32 + (dg & 3) * 2;
#pragma unroll
    for (int k = 0; k < 8; ++k) wor[k] = ldnt2((const GAS v2u*)(p.wo + (size_t)(rbase + (k & 3) * 8 + (k >> 2)) * D + cg * 256 + lane * 4));
  }
  SB();
  {
    const f32x4 r = gemv64(wur, x0, x1);
    if ((lane & 8) == 0) *(LAS f32x4*)(L + L_RED + 8192 + w * 512 + redoff) = r;
  }
  WG_BARRIER();
  if (tid < 64 && has_ffn) {
    f32x4 s = {0.f, 0.f, 0.f, 0.f};
#pragma unroll
    for (int ww = 0; ww < 8; ++ww) s += *(LAS f32x4*)(L + L_RED + 4096 + (tid >> 5) * 4096 + ww * 512 + (tid & 31) * 16);
    __builtin_amdgcn_raw_buffer_store_b128(clean4(s), rs, WS_GU + bid * 1024 + tid * 16, 0, AUX_SC1);
  }
  STAMP(2);
  SB();

  const int i16 = lane & 15, g4 = lane >> 4;
  const size_t kvbase = ((size_t)(bb * 4 + hkv) * SK + (size_t)sp * 1024 + w * 16) * 128;
  const GAS v4u* kp = (const GAS v4u*)(p.kc + kvbase) + (4 * (i16 & 3) + (i16 >> 2)) * 16 + g4;
  const GAS v4u* vp = (const GAS v4u*)(p.vc + kvbase) + lane;
  v4u kb[2][4], vb[2][4];
  {
    const int qoff = WS_QG + ((hkv * 2 + bb) * 4096 + w * 128 + lane * 2) * 8;
    v4u qg[4];
#pragma unroll
    for (int ks = 0; ks < 4; ++ks) qg[ks] = __builtin_amdgcn_raw_buffer_load_b128(rs, qoff + ks * 8192, 0, AUX_SC1);
    SB();
#pragma unroll
    for (int j = 0; j < 4; ++j) kb[0][j] = ldnt(kp + j * 4);
#pragma unroll
    for (int j = 0; j < 4; ++j) vb[0][j] = ldnt(vp + j * 64);
    SB();
    for (unsigned spins = 0; spins < SPIN_CAP; ++spins) {
      bool ok = true;
#pragma unroll
      for (int ks = 0; ks < 4; ++ks)
        ok = ok && qg[ks].y == tag_hi && (qg[ks].x >> 24) == tag_lo && qg[ks].w == tag_hi && (qg[ks].z >> 24) == tag_lo;
      if (__all(ok)) break;
      __builtin_amdgcn_s_sleep(4);
#pragma unroll
      for (int ks = 0; ks < 4; ++ks) qg[ks] = __builtin_amdgcn_raw_buffer_load_b128(rs, qoff + ks * 8192, 0, AUX_SC1);
    }
    float q0 = 0.f, q1 = 0.f;
#pragma unroll
    for (int ks = 0; ks < 4; ++ks) { q0 += u2f(qg[ks].x << 8); q1 += u2f(qg[ks].z << 8); }
    const unsigned hi = cvtpk(q0, q1);
    const unsigned lo = cvtpk(q0 - bflo(hi), q1 - bfhi(hi));
    *(LAS unsigned*)(L + L_Q + w * 272 + lane * 4) = hi;
    *(LAS unsigned*)(L + L_Q + (w + 8) * 272 + lane * 4) = lo;
  }
  WG_BARRIER();
  v4u qf[4];
#pragma unroll
  for (int kk = 0; kk < 4; ++kk) qf[kk] = *(LAS v4u*)(L + L_Q + i16 * 272 + kk * 64 + g4 * 16);
  STAMP(3);

  constexpr float C2 = 0.088388347648318440f * 1.4426950408889634f;
  float m = -INFINITY, l = 0.f;
  f32x4 o[8];
#pragma unroll
  for (int e = 0; e < 8; ++e) o[e] = f32x4{0.f, 0.f, 0.f, 0.f};
  float fsum[8];
#pragma unroll
  for (int u = 0; u < 8; ++u) {
    v4u(&kc)[4] = kb[u & 1];
    v4u(&vc)[4] = vb[u & 1];
    f32x4 s = {0.f, 0.f, 0.f, 0.f};
    asm volatile("" : "+v"(kc[0]), "+v"(kc[1]), "+v"(kc[2]), "+v"(kc[3]));
    if (u + 1 < 8) {
#pragma unroll
      for (int j = 0; j < 4; ++j) kb[(u + 1) & 1][j] = ldnt(kp + (u + 1) * 2048 + j * 4);
    }
    SB();
#pragma unroll
    for (int kk = 0; kk < 4; ++kk)
      s = __builtin_amdgcn_mfma_f32_16x16x32_bf16(__builtin_bit_cast(bf16x8, kc[kk]), __builtin_bit_cast(bf16x8, qf[kk]), s, 0, 0, 0);
#pragma unroll
    for (int r = 0; r < 4; ++r) s[r] = (s[r] + dpp<XOR8>(s[r])) * C2;
    float mx = fmaxf(fmaxf(s[0], s[1]), fmaxf(s[2], s[3]));
    mx = x32_max(x16_max(mx));
    const float mn = fmaxf(m, mx);
    const float alpha = __builtin_amdgcn_exp2f(m - mn);
    m = mn;
    const float p0 = __builtin_amdgcn_exp2f(s[0] - mn), p1 = __builtin_amdgcn_exp2f(s[1] - mn);
    const float p2 = __builtin_amdgcn_exp2f(s[2] - mn), p3 = __builtin_amdgcn_exp2f(s[3] - mn);
    l = fmaf(l, alpha, (p0 + p1) + (p2 + p3));
    const v2u pb = {cvtpk(p0, p1), cvtpk(p2, p3)};
#pragma unroll
    for (int e = 0; e < 8; ++e) o[e] *= alpha;
    SB();
    asm volatile("" : "+v"(vc[0]), "+v"(vc[1]), "+v"(vc[2]), "+v"(vc[3]));
    if (u + 1 < 8) {
#pragma unroll
      for (int j = 0; j < 4; ++j) vb[(u + 1) & 1][j] = ldnt(vp + (u + 1) * 2048 + j * 64);
    }
    SB();
#pragma unroll
    for (int e = 0; e < 8; ++e) {
      const unsigned sel = (e & 1) ? 0x07060302u : 0x05040100u;
      const v2u a = {__builtin_amdgcn_perm(vc[1][e >> 1], vc[0][e >> 1], sel), __builtin_amdgcn_perm(vc[3][e >> 1], vc[2][e >> 1], sel)};
      o[e] = __builtin_amdgcn_mfma_f32_16x16x16bf16_1k(__builtin_bit_cast(bf16x4, a), __builtin_bit_cast(bf16x4, pb), o[e], 0, 0, 0);
    }
    SB();

    if (u == 3) {
      if (w == 7) {
        for (unsigned spins = 0; spins < SPIN_CAP; ++spins) {
          bool ok = true;
#pragma unroll
          for (int j = 0; j < 4; ++j) {
            const int pb2 = lane * 4 + j;
            const v2u gq = __builtin_amdgcn_raw_buffer_load_b64(rs, WS_QG + ((((pb2 >> 6) * 2) * 4 + j) * 1024 + (lane & 15) * 64) * 8, 0, AUX_SC1);
            ok = ok && gq.y == tag_hi && (gq.x >> 24) == tag_lo;
          }
          if (__all(ok)) break;
          __builtin_amdgcn_s_sleep(4);
        }
      }
      if (has_ffn) {
        const int l5 = lane & 31;
        const int goff = WS_GU + (rg * 4 + (l5 >> 3)) * 1024 + ((l5 >> 1) & 3) * 256 + (w * 8 + (l5 & 1) * 4) * 4;
        v4u gu = __builtin_amdgcn_raw_buffer_load_b128(rs, goff, 0, AUX_SC1);
        for (unsigned spins = 0; spins < SPIN_CAP; ++spins) {
          if (__all(full4(gu))) break;
          __builtin_amdgcn_s_sleep(4);
          gu = __builtin_amdgcn_raw_buffer_load_b128(rs, goff, 0, AUX_SC1);
        }
        float h0[8], h1[8];
#pragma unroll
        for (int c = 0; c < 4; ++c) {
          float v = u2f(gu[c]);
          v += dpp<XOR8>(v);
          v = x16_sum(v);
          const float uu = dpp<0x104>(v);
          const float h = silu(v) * uu;
          h0[c] = rdl(h, 0); h0[4 + c] = rdl(h, 1); h1[c] = rdl(h, 2); h1[4 + c] = rdl(h, 3);
        }
        f32x4 s0, s1;
        gemv256(wdr, h0, h1, s0, s1);
        *(LAS f32x4*)(L + L_F + w * 2048 + lane * 16) = s0;
        *(LAS f32x4*)(L + L_F + w * 2048 + 1024 + lane * 16) = s1;
      }
      WG_BARRIER();
      if (tid < 128 && has_ffn) {
        f32x4 s4 = {0.f, 0.f, 0.f, 0.f};
#pragma unroll
        for (int ww = 0; ww < 8; ++ww) s4 += *(LAS f32x4*)(L + L_F + ww * 2048 + tid * 16);
        __builtin_amdgcn_raw_buffer_store_b128(clean4(s4), rs, WS_F2 + ((cg * 64 + (tid & 63)) * 64 + rg) * 32 + (tid >> 6) * 16, 0, AUX_SC1);
      }
      if (bid == 0 && tid == 0) {
        const v2u ne = {(unsigned)e1, (unsigned)(e1 >> 32)};
        __builtin_amdgcn_raw_buffer_store_b64(ne, rs, WS_E, 0, AUX_SC1);
      }
      STAMP(4);
      SB();
    }
  }
  STAMP(5);

  l = x32_sum(x16_sum(l));
  if (i16 < 8) {
#pragma unroll
    for (int e = 0; e < 8; ++e) *(LAS f32x4*)(L + L_O + ((w * 8 + e) * 32 + g4 * 8 + i16) * 16) = o[e];
    if (g4 == 0) *(LAS v2u*)(L + L_ML + (w * 8 + i16) * 8) = v2u{f2u(m), f2u(l)};
  }
  WG_BARRIER();
  const int prod = bb * 32 + sp;
  if (tid < 256) {
    const int j = tid & 7, e = tid >> 5, g = (tid >> 3) & 3;
    float mw[8], lw[8];
#pragma unroll
    for (int ww = 0; ww < 8; ++ww) { const v2u t = *(LAS v2u*)(L + L_ML + (ww * 8 + j) * 8); mw[ww] = u2f(t.x); lw[ww] = u2f(t.y); }
    float M = mw[0];
#pragma unroll
    for (int ww = 1; ww < 8; ++ww) M = fmaxf(M, mw[ww]);
    float Ls = 0.f;
    f32x4 acc = {0.f, 0.f, 0.f, 0.f};
#pragma unroll
    for (int ww = 0; ww < 8; ++ww) {
      const float wt = __builtin_amdgcn_exp2f(mw[ww] - M);
      Ls = fmaf(wt, lw[ww], Ls);
      acc += *(LAS f32x4*)(L + L_O + ww * 4096 + tid * 16) * wt;
    }
    const float inv = 1.0f / Ls;
    acc *= inv;
    const int head = hkv * 8 + j;
    __builtin_amdgcn_raw_buffer_store_b128(clean4(acc), rs, WS_P1 + ((head * 16 + g * 4 + (e >> 1)) * 64 + prod) * 32 + (e & 1) * 16, 0, AUX_SC1);
    if (tid < 8) __builtin_amdgcn_raw_buffer_store_b32(clean(M + __builtin_amdgcn_logf(Ls)), rs, WS_LSE + (head * 64 + prod) * 4, 0, AUX_SC1);
  }
  STAMP(6);
  if (w == 7) {
    const int foff = WS_F2 + ((cg * 64 + rg) * 64 + (lane < 56 ? lane : 0)) * 32;
    v4u fa = __builtin_amdgcn_raw_buffer_load_b128(rs, foff, 0, AUX_SC1), fb = __builtin_amdgcn_raw_buffer_load_b128(rs, foff + 16, 0, AUX_SC1);
    for (unsigned spins = 0; spins < SPIN_CAP; ++spins) {
      if (__all(full4(fa) && full4(fb))) break;
      __builtin_amdgcn_s_sleep(4);
      fa = __builtin_amdgcn_raw_buffer_load_b128(rs, foff, 0, AUX_SC1);
      fb = __builtin_amdgcn_raw_buffer_load_b128(rs, foff + 16, 0, AUX_SC1);
    }
#pragma unroll
    for (int c = 0; c < 4; ++c) {
      fsum[c] = wave_sum(lane < 56 ? u2f(fa[c]) : 0.f);
      fsum[4 + c] = wave_sum(lane < 56 ? u2f(fb[c]) : 0.f);
    }
  }

  {
    const int head = rg >> 1;
    const int loff = WS_LSE + (head * 64 + lane) * 4;
    const int poff = WS_P1 + ((head * 16 + dg) * 64 + lane) * 32;
    unsigned lse = SENT;
    v4u pa = {SENT, SENT, SENT, SENT}, pb = {SENT, SENT, SENT, SENT};
    bool hl = false, hd = false;
    for (unsigned spins = 0; spins < SPIN_CAP; ++spins) {
      const bool few = __builtin_popcountll(__ballot(!hd)) <= 8;
      const bool want = !hd && (hl || few);
      if (!hl) lse = __builtin_amdgcn_raw_buffer_load_b32(rs, loff, 0, AUX_SC1);
      if (want) {
        pa = __builtin_amdgcn_raw_buffer_load_b128(rs, poff, 0, AUX_SC1);
        pb = __builtin_amdgcn_raw_buffer_load_b128(rs, poff + 16, 0, AUX_SC1);
      }
      hl = lse != SENT;
      hd = full4(pa) && full4(pb);
      if (__all(hl && hd)) break;
      __builtin_amdgcn_s_sleep(2);
    }
    const float ls = u2f(lse);
    const float Mx = x16_max(row16_max(ls));
    const float wt = __builtin_amdgcn_exp2f(ls - Mx);
    const float W = x16_sum(row16_sum(wt));
    const float wn = wt / W;
    float c0[8], c1[8];
#pragma unroll
    for (int k = 0; k < 8; ++k) {
      float v = u2f(k < 4 ? pa[k & 3] : pb[k & 3]) * wn;
      v = x16_sum(row16_sum(v));
      c0[k] = rdl(v, 0);
      c1[k] = rdl(v, 32);
    }
    f32x4 s0, s1;
    gemv256(wor, c0, c1, s0, s1);
    *(LAS f32x4*)(L + L_S + w * 2048 + lane * 16) = s0;
    *(LAS f32x4*)(L + L_S + w * 2048 + 1024 + lane * 16) = s1;
  }
  STAMP(7);
  WG_BARRIER();
  if (tid < 128) {
    f32x4 s4 = {0.f, 0.f, 0.f, 0.f};
#pragma unroll
    for (int ww = 0; ww < 8; ++ww) s4 += *(LAS f32x4*)(L + L_S + ww * 2048 + tid * 16);
    __builtin_amdgcn_raw_buffer_store_b128(clean4(s4), rs, WS_S2 + ((cg * 64 + (tid & 63)) * 64 + rg) * 32 + (tid >> 6) * 16, 0, AUX_SC1);
  }
  STAMP(8);

  if (w == 7) {
    const int soff = WS_S2 + ((cg * 64 + rg) * 64 + lane) * 32;
    v4u sa = {SENT, SENT, SENT, SENT}, sb = {SENT, SENT, SENT, SENT};
    bool hd = false;
    const int col = cg * 256 + rg * 4;
    const v2u xo0 = *(const GAS v2u*)(p.x + col), xo1 = *(const GAS v2u*)(p.x + D + col);
    for (unsigned spins = 0; spins < SPIN_CAP; ++spins) {
      if (!hd) {
        sa = __builtin_amdgcn_raw_buffer_load_b128(rs, soff, 0, AUX_SC1);
        sb = __builtin_amdgcn_raw_buffer_load_b128(rs, soff + 16, 0, AUX_SC1);
      }
      hd = full4(sa) && full4(sb);
      if (__all(hd)) break;
      __builtin_amdgcn_s_sleep(2);
    }
    float r[8];
#pragma unroll
    for (int c = 0; c < 4; ++c) { r[c] = wave_sum(u2f(sa[c])); r[4 + c] = wave_sum(u2f(sb[c])); }
    const float xv[8] = {bflo(xo0.x), bfhi(xo0.x), bflo(xo0.y), bfhi(xo0.y), bflo(xo1.x), bfhi(xo1.x), bflo(xo1.y), bfhi(xo1.y)};
#pragma unroll
    for (int c = 0; c < 8; ++c) r[c] = fmaf(r[c], silu(fsum[c]), xv[c]);
    if (lane == 0) {
      *(GAS v2u*)(p.out + col) = v2u{cvtpk(r[0], r[1]), cvtpk(r[2], r[3])};
      *(GAS v2u*)(p.out + D + col) = v2u{cvtpk(r[4], r[5]), cvtpk(r[6], r[7])};
    }
  }
}

}

extern "C" void kernel_launch(void* const* d_in, const int* in_sizes, int n_in, void* d_out, int out_size, void* d_ws,
                              size_t ws_size, hipStream_t stream) {
  Params p;
  p.x = (const unsigned short*)d_in[0];
  p.kc = (const unsigned short*)d_in[1];
  p.vc = (const unsigned short*)d_in[2];
  p.wq = (const unsigned short*)d_in[3];
  p.wo = (const unsigned short*)d_in[4];
  p.wg = (const unsigned short*)d_in[6];
  p.wu = (const unsigned short*)d_in[7];
  p.wd = (const unsigned short*)d_in[8];
  p.out = (unsigned short*)d_out;
  p.ws = (unsigned char*)d_ws;
  hipLaunchKernelGGL(fused_decode, dim3(NWG), dim3(NTHR), 0, stream, p);
}
```

```cpp
#define KNT 0
#define VNT 1
#define WNT 0
#include <hip/hip_runtime.h>
#include <hip/hip_bf16.h>
#include <stdint.h>

#define GAS __attribute__((address_space(1)))
#define LAS __attribute__((address_space(3)))
typedef unsigned v4u __attribute__((ext_vector_type(4)));
typedef unsigned v2u __attribute__((ext_vector_type(2)));
typedef float f32x4 __attribute__((ext_vector_type(4)));
typedef short bf16x8 __attribute__((ext_vector_type(8)));
typedef short bf16x4 __attribute__((ext_vector_type(4)));
typedef unsigned long long u64;

namespace {

constexpr int NWG = 256, NTHR = 512;
constexpr int D = 1024, HD = 4096, FF = 3584, SK = 32768;
constexpr int AUX_SC1 = 16;
constexpr unsigned SENT = 0xFFFFFFFFu;
constexpr unsigned SPIN_CAP = 1u << 22;

constexpr int WS_E = 0;
constexpr int WS_QG = 4096;
constexpr int WS_GU = WS_QG + 262144;
constexpr int WS_P1 = WS_GU + 262144;
constexpr int WS_LSE = WS_P1 + 1048576;
constexpr int WS_S2 = WS_LSE + 8192;
constexpr int WS_F2 = WS_S2 + 524288;
constexpr int WS_END = WS_F2 + 524288;
constexpr int WS_ALL = WS_END;

constexpr int L_RED = 0;
constexpr int L_Q = 12288;
constexpr int L_ML = L_Q + 16 * 272;
constexpr int L_O = L_ML + 512;
constexpr int L_S = L_O + 32768;
constexpr int L_F = L_S + 16384;
constexpr int L_USED = L_F + 16384;
constexpr int L_BYTES = 84 * 1024;
static_assert(L_USED <= L_BYTES, "lds");

struct Params {
  const unsigned short* x;
  const unsigned short* kc;
  const unsigned short* vc;
  const unsigned short* wq;
  const unsigned short* wo;
  const unsigned short* wg;
  const unsigned short* wu;
  const unsigned short* wd;
  unsigned short* out;
  unsigned char* ws;
};

__device__ __forceinline__ float bflo(unsigned u) { return __builtin_bit_cast(float, u << 16); }
__device__ __forceinline__ float bfhi(unsigned u) { return __builtin_bit_cast(float, u & 0xFFFF0000u); }
__device__ __forceinline__ unsigned f2u(float f) { return __builtin_bit_cast(unsigned, f); }
__device__ __forceinline__ float u2f(unsigned u) { return __builtin_bit_cast(float, u); }
typedef __bf16 bf16p __attribute__((ext_vector_type(2)));
typedef float f32x2 __attribute__((ext_vector_type(2)));
__device__ __forceinline__ unsigned cvtpk(float lo, float hi) {
  return __builtin_bit_cast(unsigned, __builtin_convertvector(f32x2{lo, hi}, bf16p));
}
template <int CTRL> __device__ __forceinline__ float dpp(float x) {
  return __builtin_bit_cast(float, __builtin_amdgcn_mov_dpp(__builtin_bit_cast(int, x), CTRL, 0xf, 0xf, true));
}
constexpr int XOR1 = 0xB1, XOR2 = 0x4E, XOR7 = 0x141, XOR8 = 0x128, ROR4 = 0x124, XOR15 = 0x140;
__device__ __forceinline__ float x16_sum(float x) {
  auto s = __builtin_amdgcn_permlane16_swap(f2u(x), f2u(x), false, false);
  return u2f(s[0]) + u2f(s[1]);
}
__device__ __forceinline__ float x32_sum(float x) {
  auto s = __builtin_amdgcn_permlane32_swap(f2u(x), f2u(x), false, false);
  return u2f(s[0]) + u2f(s[1]);
}
__device__ __forceinline__ float x16_max(float x) {
  auto s = __builtin_amdgcn_permlane16_swap(f2u(x), f2u(x), false, false);
  return fmaxf(u2f(s[0]), u2f(s[1]));
}
__device__ __forceinline__ float x32_max(float x) {
  auto s = __builtin_amdgcn_permlane32_swap(f2u(x), f2u(x), false, false);
  return fmaxf(u2f(s[0]), u2f(s[1]));
}
__device__ __forceinline__ float row16_sum(float x) {
  x += dpp<XOR1>(x);
  x += dpp<XOR2>(x);
  x += dpp<XOR7>(x);
  x += dpp<XOR8>(x);
  return x;
}
__device__ __forceinline__ float row16_max(float x) {
  x = fmaxf(x, dpp<XOR1>(x));
  x = fmaxf(x, dpp<XOR2>(x));
  x = fmaxf(x, dpp<XOR7>(x));
  x = fmaxf(x, dpp<XOR8>(x));
  return x;
}
__device__ __forceinline__ float wave_sum(float x) { return x32_sum(x16_sum(row16_sum(x))); }
__device__ __forceinline__ float tr16(float a, float b) {
  auto s = __builtin_amdgcn_permlane16_swap(f2u(a), f2u(b), false, false);
  return u2f(s[0]) + u2f(s[1]);
}
__device__ __forceinline__ float tr32(float a, float b) {
  auto s = __builtin_amdgcn_permlane32_swap(f2u(a), f2u(b), false, false);
  return u2f(s[0]) + u2f(s[1]);
}
template <int NT> __device__ __forceinline__ v4u ldx(const GAS v4u* p) { if (NT) return __builtin_nontemporal_load(p); else return *p; }
__device__ __forceinline__ v4u ldnt(const GAS v4u* p) { return ldx<WNT>(p); }
__device__ __forceinline__ v2u ldnt2(const GAS v2u* p) { if (WNT) return __builtin_nontemporal_load(p); else return *p; }
__device__ __forceinline__ unsigned clean(float f) {
  const unsigned u = f2u(f);
  return u == SENT ? 0x7FC00000u : u;
}
__device__ __forceinline__ v4u clean4(f32x4 v) { return v4u{clean(v.x), clean(v.y), clean(v.z), clean(v.w)}; }
__device__ __forceinline__ bool full4(v4u v) { return v.x != SENT && v.y != SENT && v.z != SENT && v.w != SENT; }
__device__ __forceinline__ float rdl(float v, int lane) { return u2f(__builtin_amdgcn_readlane(f2u(v), lane)); }
__device__ __forceinline__ float silu(float v) { return v / (1.0f + __expf(-v)); }

#define SB() __builtin_amdgcn_sched_barrier(0)
#define WG_BARRIER()                                        \
  do {                                                      \
    asm volatile("s_waitcnt lgkmcnt(0)" ::: "memory");      \
    __builtin_amdgcn_s_barrier();                           \
    asm volatile("" ::: "memory");                          \
  } while (0)
#define STAMP(i)

__device__ __forceinline__ f32x4 gemv64(const v4u (&w)[4], const float (&x0)[4], const float (&x1)[4]) {
  float a0[8], a1[8];
#pragma unroll
  for (int c = 0; c < 8; ++c) { a0[c] = 0.f; a1[c] = 0.f; }
#pragma unroll
  for (int i = 0; i < 4; ++i) {
#pragma unroll
    for (int k = 0; k < 4; ++k) {
      const float lo = bflo(w[i][k]), hi = bfhi(w[i][k]);
      a0[2 * k] = fmaf(lo, x0[i], a0[2 * k]);
      a0[2 * k + 1] = fmaf(hi, x0[i], a0[2 * k + 1]);
      a1[2 * k] = fmaf(lo, x1[i], a1[2 * k]);
      a1[2 * k + 1] = fmaf(hi, x1[i], a1[2 * k + 1]);
    }
  }
  float t[8];
#pragma unroll
  for (int c = 0; c < 8; ++c) t[c] = tr16(a0[c], a1[c]);
  f32x4 r;
#pragma unroll
  for (int c = 0; c < 4; ++c) {
    float v = tr32(t[c], t[c + 4]);
    v += dpp<XOR8>(v);
    r[c] = v;
  }
  return r;
}

__device__ __forceinline__ void gemv256(const v2u (&w)[8], const float (&c0)[8], const float (&c1)[8], f32x4& s0, f32x4& s1) {
  s0 = f32x4{0.f, 0.f, 0.f, 0.f};
  s1 = f32x4{0.f, 0.f, 0.f, 0.f};
#pragma unroll
  for (int k = 0; k < 8; ++k) {
    const f32x4 wf = {bflo(w[k].x), bfhi(w[k].x), bflo(w[k].y), bfhi(w[k].y)};
#pragma unroll
    for (int c = 0; c < 4; ++c) {
      s0[c] = fmaf(wf[c], c0[k], s0[c]);
      s1[c] = fmaf(wf[c], c1[k], s1[c]);
    }
  }
}

__global__ void __launch_bounds__(NTHR, 2) fused_decode(Params p) {
  __shared__ __attribute__((aligned(16))) unsigned char smem[L_BYTES];
  LAS unsigned char* L = (LAS unsigned char*)smem;
  const int tid = threadIdx.x, lane = tid & 63, w = __builtin_amdgcn_readfirstlane(tid >> 6);
  const int bid = blockIdx.x;
  const int rg = bid >> 2, cg = bid & 3;
  const int hkv = bid >> 6, bb = (bid >> 5) & 1, sp = bid & 31;
  const bool has_ffn = rg < 56;
  const __amdgpu_buffer_rsrc_t rs = __builtin_amdgcn_make_buffer_rsrc((void*)p.ws, 0, WS_ALL, 0x00020000);
  STAMP(0);

  {
    const v4u s4 = {SENT, SENT, SENT, SENT};
    int off;
    if (tid < 256) off = WS_P1 + bid * 4096 + tid * 16;
    else if (tid < 384) off = WS_S2 + (cg * 64 + rg) * 2048 + (tid - 256) * 16;
    else off = WS_F2 + (cg * 64 + rg) * 2048 + (tid - 384) * 16;
    __builtin_amdgcn_raw_buffer_store_b128(s4, rs, off, 0, AUX_SC1);
    if (tid < 64) __builtin_amdgcn_raw_buffer_store_b128(s4, rs, WS_GU + bid * 1024 + tid * 16, 0, AUX_SC1);
    else if (tid < 66) __builtin_amdgcn_raw_buffer_store_b128(s4, rs, WS_LSE + bid * 32 + (tid - 64) * 16, 0, AUX_SC1);
  }
  SB();
  const v2u ev = __builtin_amdgcn_raw_buffer_load_b64(rs, WS_E, 0, AUX_SC1);
  const GAS v4u* xg = (const GAS v4u*)p.x;
  const v4u xa0 = xg[lane * 2], xa1 = xg[lane * 2 + 1], xb0 = xg[128 + lane * 2], xb1 = xg[128 + lane * 2 + 1];
  const int r8 = lane >> 3, c8 = lane & 7;
  const int krow = cg * 256 + w * 32 + r8 * 4;
  const v2u xr0 = *(const GAS v2u*)(p.x + krow), xr1 = *(const GAS v2u*)(p.x + D + krow);
  v4u wqr[4], wgr[4], wur[4];
#pragma unroll
  for (int i = 0; i < 4; ++i) wqr[i] = ldnt((const GAS v4u*)(p.wq + (size_t)(krow + i) * HD + rg * 64 + c8 * 8));
  const int rgf = has_ffn ? rg : 0;
#pragma unroll
  for (int i = 0; i < 4; ++i) wgr[i] = ldnt((const GAS v4u*)(p.wg + (size_t)(krow + i) * FF + rgf * 64 + c8 * 8));
  SB();

  float x0[4], x1[4];
  {
    float va[16], vb[16];
#pragma unroll
    for (int k = 0; k < 4; ++k) {
      va[2 * k] = bflo(xa0[k]); va[2 * k + 1] = bfhi(xa0[k]); va[8 + 2 * k] = bflo(xa1[k]); va[8 + 2 * k + 1] = bfhi(xa1[k]);
      vb[2 * k] = bflo(xb0[k]); vb[2 * k + 1] = bfhi(xb0[k]); vb[8 + 2 * k] = bflo(xb1[k]); vb[8 + 2 * k + 1] = bfhi(xb1[k]);
    }
    float sa = 0.f, sb = 0.f;
#pragma unroll
    for (int k = 0; k < 16; ++k) { sa += va[k]; sb += vb[k]; }
    const float ma = wave_sum(sa) * (1.0f / D), mb = wave_sum(sb) * (1.0f / D);
    float qa = 0.f, qb = 0.f;
#pragma unroll
    for (int k = 0; k < 16; ++k) { const float da = va[k] - ma, db = vb[k] - mb; qa = fmaf(da, da, qa); qb = fmaf(db, db, qb); }
    const float ra = 1.0f / sqrtf(wave_sum(qa) * (1.0f / D) + 1e-5f), rb = 1.0f / sqrtf(wave_sum(qb) * (1.0f / D) + 1e-5f);
    x0[0] = (bflo(xr0.x) - ma) * ra; x0[1] = (bfhi(xr0.x) - ma) * ra; x0[2] = (bflo(xr0.y) - ma) * ra; x0[3] = (bfhi(xr0.y) - ma) * ra;
    x1[0] = (bflo(xr1.x) - mb) * rb; x1[1] = (bfhi(xr1.x) - mb) * rb; x1[2] = (bflo(xr1.y) - mb) * rb; x1[3] = (bfhi(xr1.y) - mb) * rb;
  }
  const u64 e1 = ((((u64)__builtin_amdgcn_readfirstlane(ev.y)) << 32) | (u64)(unsigned)__builtin_amdgcn_readfirstlane(ev.x)) + 1ull;
  const unsigned tag_hi = (unsigned)(e1 >> 8), tag_lo = (unsigned)(e1 & 0xFFull);
  const int rho = lane >> 4;
  const int redoff = ((rho & 1) * 64 + c8 * 8 + (rho >> 1) * 4) * 4;

  {
    const f32x4 r = gemv64(wqr, x0, x1);
    if ((lane & 8) == 0) *(LAS f32x4*)(L + L_RED + w * 512 + redoff) = r;
  }
#pragma unroll
  for (int i = 0; i < 4; ++i) wur[i] = ldnt((const GAS v4u*)(p.wu + (size_t)(krow + i) * FF + rgf * 64 + c8 * 8));
  SB();
  WG_BARRIER();
  if (tid < 128) {
    float s = 0.f;
#pragma unroll
    for (int ww = 0; ww < 8; ++ww) s += *(LAS float*)(L + L_RED + ww * 512 + tid * 4);
    const unsigned v24 = (f2u(s) + 0x80u) >> 8;
    const v2u gr = {(tag_lo << 24) | v24, tag_hi};
    const int b = tid >> 6, c = tid & 63;
    __builtin_amdgcn_raw_buffer_store_b64(gr, rs, WS_QG + (((hkv * 2 + b) * 4 + cg) * 1024 + (rg & 15) * 64 + c) * 8, 0, AUX_SC1);
  }
  STAMP(1);
  SB();

  v2u wdr[8], wor[8];
#pragma unroll
  for (int k = 0; k < 8; ++k) wdr[k] = ldnt2((const GAS v2u*)(p.wd + (size_t)(rgf * 64 + w * 8 + k) * D + cg * 256 + lane * 4));
  SB();
  {
    const f32x4 r = gemv64(wgr, x0, x1);
    if ((lane & 8) == 0) *(LAS f32x4*)(L + L_RED + 4096 + w * 512 + redoff) = r;
  }
  const int dg = (rg & 1) * 8 + w;
  {
    const int rbase = (rg >> 1) * 128 + (dg >> 2) * 32 + (dg & 3) * 2;
#pragma unroll
    for (int k = 0; k < 8; ++k) wor[k] = ldnt2((const GAS v2u*)(p.wo + (size_t)(rbase + (k & 3) * 8 + (k >> 2)) * D + cg * 256 + lane * 4));
  }
  SB();
  {
    const f32x4 r = gemv64(wur, x0, x1);
    if ((lane & 8) == 0) *(LAS f32x4*)(L + L_RED + 8192 + w * 512 + redoff) = r;
  }
  WG_BARRIER();
  if (tid < 64 && has_ffn) {
    f32x4 s = {0.f, 0.f, 0.f, 0.f};
#pragma unroll
    for (int ww = 0; ww < 8; ++ww) s += *(LAS f32x4*)(L + L_RED + 4096 + (tid >> 5) * 4096 + ww * 512 + (tid & 31) * 16);
    __builtin_amdgcn_raw_buffer_store_b128(clean4(s), rs, WS_GU + bid * 1024 + tid * 16, 0, AUX_SC1);
  }
  STAMP(2);
  SB();

  const int i16 = lane & 15, g4 = lane >> 4;
  const size_t kvbase = ((size_t)(bb * 4 + hkv) * SK + (size_t)sp * 1024 + w * 16) * 128;
  const GAS v4u* kp = (const GAS v4u*)(p.kc + kvbase) + (4 * (i16 & 3) + (i16 >> 2)) * 16 + g4;
  const GAS v4u* vp = (const GAS v4u*)(p.vc + kvbase) + lane;
  v4u kb[2][4], vb[2][4];
  {
    const int qoff = WS_QG + ((hkv * 2 + bb) * 4096 + w * 128 + lane * 2) * 8;
    v4u qg[4];
#pragma unroll
    for (int ks = 0; ks < 4; ++ks) qg[ks] = __builtin_amdgcn_raw_buffer_load_b128(rs, qoff + ks * 8192, 0, AUX_SC1);
    SB();
#pragma unroll
    for (int j = 0; j < 4; ++j) kb[0][j] = ldx<KNT>(kp + j * 4);
#pragma unroll
    for (int j = 0; j < 4; ++j) vb[0][j] = ldx<VNT>(vp + j * 64);
    SB();
    for (unsigned spins = 0; spins < SPIN_CAP; ++spins) {
      bool ok = true;
#pragma unroll
      for (int ks = 0; ks < 4; ++ks)
        ok = ok && qg[ks].y == tag_hi && (qg[ks].x >> 24) == tag_lo && qg[ks].w == tag_hi && (qg[ks].z >> 24) == tag_lo;
      if (__all(ok)) break;
      __builtin_amdgcn_s_sleep(4);
#pragma unroll
      for (int ks = 0; ks < 4; ++ks) qg[ks] = __builtin_amdgcn_raw_buffer_load_b128(rs, qoff + ks * 8192, 0, AUX_SC1);
    }
    float q0 = 0.f, q1 = 0.f;
#pragma unroll
    for (int ks = 0; ks < 4; ++ks) { q0 += u2f(qg[ks].x << 8); q1 += u2f(qg[ks].z << 8); }
    const unsigned hi = cvtpk(q0, q1);
    const unsigned lo = cvtpk(q0 - bflo(hi), q1 - bfhi(hi));
    *(LAS unsigned*)(L + L_Q + w * 272 + lane * 4) = hi;
    *(LAS unsigned*)(L + L_Q + (w + 8) * 272 + lane * 4) = lo;
  }
  WG_BARRIER();
  v4u qf[4];
#pragma unroll
  for (int kk = 0; kk < 4; ++kk) qf[kk] = *(LAS v4u*)(L + L_Q + i16 * 272 + kk * 64 + g4 * 16);
  STAMP(3);

  constexpr float C2 = 0.088388347648318440f * 1.4426950408889634f;
  float m = -INFINITY, l = 0.f;
  f32x4 o[8];
#pragma unroll
  for (int e = 0; e < 8; ++e) o[e] = f32x4{0.f, 0.f, 0.f, 0.f};
  float fsum[8];
#pragma unroll
  for (int u = 0; u < 8; ++u) {
    v4u(&kc)[4] = kb[u & 1];
    v4u(&vc)[4] = vb[u & 1];
    f32x4 s = {0.f, 0.f, 0.f, 0.f};
    asm volatile("" : "+v"(kc[0]), "+v"(kc[1]), "+v"(kc[2]), "+v"(kc[3]));
    if (u + 1 < 8) {
#pragma unroll
      for (int j = 0; j < 4; ++j) kb[(u + 1) & 1][j] = ldx<KNT>(kp + (u + 1) * 2048 + j * 4);
    }
    SB();
#pragma unroll
    for (int kk = 0; kk < 4; ++kk)
      s = __builtin_amdgcn_mfma_f32_16x16x32_bf16(__builtin_bit_cast(bf16x8, kc[kk]), __builtin_bit_cast(bf16x8, qf[kk]), s, 0, 0, 0);
#pragma unroll
    for (int r = 0; r < 4; ++r) s[r] = (s[r] + dpp<XOR8>(s[r])) * C2;
    float mx = fmaxf(fmaxf(s[0], s[1]), fmaxf(s[2], s[3]));
    mx = x32_max(x16_max(mx));
    const float mn = fmaxf(m, mx);
    const float alpha = __builtin_amdgcn_exp2f(m - mn);
    m = mn;
    const float p0 = __builtin_amdgcn_exp2f(s[0] - mn), p1 = __builtin_amdgcn_exp2f(s[1] - mn);
    const float p2 = __builtin_amdgcn_exp2f(s[2] - mn), p3 = __builtin_amdgcn_exp2f(s[3] - mn);
    l = fmaf(l, alpha, (p0 + p1) + (p2 + p3));
    const v2u pb = {cvtpk(p0, p1), cvtpk(p2, p3)};
#pragma unroll
    for (int e = 0; e < 8; ++e) o[e] *= alpha;
    SB();
    asm volatile("" : "+v"(vc[0]), "+v"(vc[1]), "+v"(vc[2]), "+v"(vc[3]));
    if (u + 1 < 8) {
#pragma unroll
      for (int j = 0; j < 4; ++j) vb[(u + 1) & 1][j] = ldx<VNT>(vp + (u + 1) * 2048 + j * 64);
    }
    SB();
#pragma unroll
    for (int e = 0; e < 8; ++e) {
      const unsigned sel = (e & 1) ? 0x07060302u : 0x05040100u;
      const v2u a = {__builtin_amdgcn_perm(vc[1][e >> 1], vc[0][e >> 1], sel), __builtin_amdgcn_perm(vc[3][e >> 1], vc[2][e >> 1], sel)};
      o[e] = __builtin_amdgcn_mfma_f32_16x16x16bf16_1k(__builtin_bit_cast(bf16x4, a), __builtin_bit_cast(bf16x4, pb), o[e], 0, 0, 0);
    }
    SB();

    if (u == 3) {
      if (w == 7) {
        for (unsigned spins = 0; spins < SPIN_CAP; ++spins) {
          bool ok = true;
#pragma unroll
          for (int j = 0; j < 4; ++j) {
            const int pb2 = lane * 4 + j;
            const v2u gq = __builtin_amdgcn_raw_buffer_load_b64(rs, WS_QG + ((((pb2 >> 6) * 2) * 4 + j) * 1024 + (lane & 15) * 64) * 8, 0, AUX_SC1);
            ok = ok && gq.y == tag_hi && (gq.x >> 24) == tag_lo;
          }
          if (__all(ok)) break;
          __builtin_amdgcn_s_sleep(4);
        }
      }
      if (has_ffn) {
        const int l5 = lane & 31;
        const int goff = WS_GU + (rg * 4 + (l5 >> 3)) * 1024 + ((l5 >> 1) & 3) * 256 + (w * 8 + (l5 & 1) * 4) * 4;
        v4u gu = __builtin_amdgcn_raw_buffer_load_b128(rs, goff, 0, AUX_SC1);
        for (unsigned spins = 0; spins < SPIN_CAP; ++spins) {
          if (__all(full4(gu))) break;
          __builtin_amdgcn_s_sleep(4);
          gu = __builtin_amdgcn_raw_buffer_load_b128(rs, goff, 0, AUX_SC1);
        }
        float h0[8], h1[8];
#pragma unroll
        for (int c = 0; c < 4; ++c) {
          float v = u2f(gu[c]);
          v += dpp<XOR8>(v);
          v = x16_sum(v);
          const float uu = dpp<0x104>(v);
          const float h = silu(v) * uu;
          h0[c] = rdl(h, 0); h0[4 + c] = rdl(h, 1); h1[c] = rdl(h, 2); h1[4 + c] = rdl(h, 3);
        }
        f32x4 s0, s1;
        gemv256(wdr, h0, h1, s0, s1);
        *(LAS f32x4*)(L + L_F + w * 2048 + lane * 16) = s0;
        *(LAS f32x4*)(L + L_F + w * 2048 + 1024 + lane * 16) = s1;
      }
      WG_BARRIER();
      if (tid < 128 && has_ffn) {
        f32x4 s4 = {0.f, 0.f, 0.f, 0.f};
#pragma unroll
        for (int ww = 0; ww < 8; ++ww) s4 += *(LAS f32x4*)(L + L_F + ww * 2048 + tid * 16);
        __builtin_amdgcn_raw_buffer_store_b128(clean4(s4), rs, WS_F2 + ((cg * 64 + (tid & 63)) * 64 + rg) * 32 + (tid >> 6) * 16, 0, AUX_SC1);
      }
      if (bid == 0 && tid == 0) {
        const v2u ne = {(unsigned)e1, (unsigned)(e1 >> 32)};
        __builtin_amdgcn_raw_buffer_store_b64(ne, rs, WS_E, 0, AUX_SC1);
      }
      STAMP(4);
      SB();
    }
  }
  STAMP(5);

  l = x32_sum(x16_sum(l));
  if (i16 < 8) {
#pragma unroll
    for (int e = 0; e < 8; ++e) *(LAS f32x4*)(L + L_O + ((w * 8 + e) * 32 + g4 * 8 + i16) * 16) = o[e];
    if (g4 == 0) *(LAS v2u*)(L + L_ML + (w * 8 + i16) * 8) = v2u{f2u(m), f2u(l)};
  }
  WG_BARRIER();
  const int prod = bb * 32 + sp;
  if (tid < 256) {
    const int j = tid & 7, e = tid >> 5, g = (tid >> 3) & 3;
    float mw[8], lw[8];
#pragma unroll
    for (int ww = 0; ww < 8; ++ww) { const v2u t = *(LAS v2u*)(L + L_ML + (ww * 8 + j) * 8); mw[ww] = u2f(t.x); lw[ww] = u2f(t.y); }
    float M = mw[0];
#pragma unroll
    for (int ww = 1; ww < 8; ++ww) M = fmaxf(M, mw[ww]);
    float Ls = 0.f;
    f32x4 acc = {0.f, 0.f, 0.f, 0.f};
#pragma unroll
    for (int ww = 0; ww < 8; ++ww) {
      const float wt = __builtin_amdgcn_exp2f(mw[ww] - M);
      Ls = fmaf(wt, lw[ww], Ls);
      acc += *(LAS f32x4*)(L + L_O + ww * 4096 + tid * 16) * wt;
    }
    const float inv = 1.0f / Ls;
    acc *= inv;
    const int head = hkv * 8 + j;
    __builtin_amdgcn_raw_buffer_store_b128(clean4(acc), rs, WS_P1 + ((head * 16 + g * 4 + (e >> 1)) * 64 + prod) * 32 + (e & 1) * 16, 0, AUX_SC1);
    if (tid < 8) __builtin_amdgcn_raw_buffer_store_b32(clean(M + __builtin_amdgcn_logf(Ls)), rs, WS_LSE + (head * 64 + prod) * 4, 0, AUX_SC1);
  }
  STAMP(6);
  if (w == 7) {
    const int foff = WS_F2 + ((cg * 64 + rg) * 64 + (lane < 56 ? lane : 0)) * 32;
    v4u fa = __builtin_amdgcn_raw_buffer_load_b128(rs, foff, 0, AUX_SC1), fb = __builtin_amdgcn_raw_buffer_load_b128(rs, foff + 16, 0, AUX_SC1);
    for (unsigned spins = 0; spins < SPIN_CAP; ++spins) {
      if (__all(full4(fa) && full4(fb))) break;
      __builtin_amdgcn_s_sleep(4);
      fa = __builtin_amdgcn_raw_buffer_load_b128(rs, foff, 0, AUX_SC1);
      fb = __builtin_amdgcn_raw_buffer_load_b128(rs, foff + 16, 0, AUX_SC1);
    }
#pragma unroll
    for (int c = 0; c < 4; ++c) {
      fsum[c] = wave_sum(lane < 56 ? u2f(fa[c]) : 0.f);
      fsum[4 + c] = wave_sum(lane < 56 ? u2f(fb[c]) : 0.f);
    }
  }

  {
    const int head = rg >> 1;
    const int loff = WS_LSE + (head * 64 + lane) * 4;
    const int poff = WS_P1 + ((head * 16 + dg) * 64 + lane) * 32;
    unsigned lse = SENT;
    v4u pa = {SENT, SENT, SENT, SENT}, pb = {SENT, SENT, SENT, SENT};
    bool hl = false, hd = false;
    for (unsigned spins = 0; spins < SPIN_CAP; ++spins) {
      const bool few = __builtin_popcountll(__ballot(!hd)) <= 8;
      const bool want = !hd && (hl || few);
      if (!hl) lse = __builtin_amdgcn_raw_buffer_load_b32(rs, loff, 0, AUX_SC1);
      if (want) {
        pa = __builtin_amdgcn_raw_buffer_load_b128(rs, poff, 0, AUX_SC1);
        pb = __builtin_amdgcn_raw_buffer_load_b128(rs, poff + 16, 0, AUX_SC1);
      }
      hl = lse != SENT;
      hd = full4(pa) && full4(pb);
      if (__all(hl && hd)) break;
      __builtin_amdgcn_s_sleep(2);
    }
    const float ls = u2f(lse);
    const float Mx = x16_max(row16_max(ls));
    const float wt = __builtin_amdgcn_exp2f(ls - Mx);
    const float W = x16_sum(row16_sum(wt));
    const float wn = wt / W;
    float c0[8], c1[8];
#pragma unroll
    for (int k = 0; k < 8; ++k) {
      float v = u2f(k < 4 ? pa[k & 3] : pb[k & 3]) * wn;
      v = x16_sum(row16_sum(v));
      c0[k] = rdl(v, 0);
      c1[k] = rdl(v, 32);
    }
    f32x4 s0, s1;
    gemv256(wor, c0, c1, s0, s1);
    *(LAS f32x4*)(L + L_S + w * 2048 + lane * 16) = s0;
    *(LAS f32x4*)(L + L_S + w * 2048 + 1024 + lane * 16) = s1;
  }
  STAMP(7);
  WG_BARRIER();
  if (tid < 128) {
    f32x4 s4 = {0.f, 0.f, 0.f, 0.f};
#pragma unroll
    for (int ww = 0; ww < 8; ++ww) s4 += *(LAS f32x4*)(L + L_S + ww * 2048 + tid * 16);
    __builtin_amdgcn_raw_buffer_store_b128(clean4(s4), rs, WS_S2 + ((cg * 64 + (tid & 63)) * 64 + rg) * 32 + (tid >> 6) * 16, 0, AUX_SC1);
  }
  STAMP(8);

  if (w == 7) {
    const int soff = WS_S2 + ((cg * 64 + rg) * 64 + lane) * 32;
    v4u sa = {SENT, SENT, SENT, SENT}, sb = {SENT, SENT, SENT, SENT};
    bool hd = false;
    const int col = cg * 256 + rg * 4;
    const v2u xo0 = *(const GAS v2u*)(p.x + col), xo1 = *(const GAS v2u*)(p.x + D + col);
    for (unsigned spins = 0; spins < SPIN_CAP; ++spins) {
      if (!hd) {
        sa = __builtin_amdgcn_raw_buffer_load_b128(rs, soff, 0, AUX_SC1);
        sb = __builtin_amdgcn_raw_buffer_load_b128(rs, soff + 16, 0, AUX_SC1);
      }
      hd = full4(sa) && full4(sb);
      if (__all(hd)) break;
      __builtin_amdgcn_s_sleep(2);
    }
    float r[8];
#pragma unroll
    for (int c = 0; c < 4; ++c) { r[c] = wave_sum(u2f(sa[c])); r[4 + c] = wave_sum(u2f(sb[c])); }
    const float xv[8] = {bflo(xo0.x), bfhi(xo0.x), bflo(xo0.y), bfhi(xo0.y), bflo(xo1.x), bfhi(xo1.x), bflo(xo1.y), bfhi(xo1.y)};
#pragma unroll
    for (int c = 0; c < 8; ++c) r[c] = fmaf(r[c], silu(fsum[c]), xv[c]);
    if (lane == 0) {
      *(GAS v2u*)(p.out + col) = v2u{cvtpk(r[0], r[1]), cvtpk(r[2], r[3])};
      *(GAS v2u*)(p.out + D + col) = v2u{cvtpk(r[4], r[5]), cvtpk(r[6], r[7])};
    }
  }
}

}

extern "C" void kernel_launch(void* const* d_in, const int* in_sizes, int n_in, void* d_out, int out_size, void* d_ws,
                              size_t ws_size, hipStream_t stream) {
  Params p;
  p.x = (const unsigned short*)d_in[0];
  p.kc = (const unsigned short*)d_in[1];
  p.vc = (const unsigned short*)d_in[2];
  p.wq = (const unsigned short*)d_in[3];
  p.wo = (const unsigned short*)d_in[4];
  p.wg = (const unsigned short*)d_in[6];
  p.wu = (const unsigned short*)d_in[7];
  p.wd = (const unsigned short*)d_in[8];
  p.out = (unsigned short*)d_out;
  p.ws = (unsigned char*)d_ws;
  hipLaunchKernelGGL(fused_decode, dim3(NWG), dim3(NTHR), 0, stream, p);
}
```

```cpp
#define KPL 3
#define VPL 2
#include <hip/hip_runtime.h>
#include <hip/hip_bf16.h>
#include <stdint.h>

#define GAS __attribute__((address_space(1)))
#define LAS __attribute__((address_space(3)))
typedef unsigned v4u __attribute__((ext_vector_type(4)));
typedef unsigned v2u __attribute__((ext_vector_type(2)));
typedef float f32x4 __attribute__((ext_vector_type(4)));
typedef short bf16x8 __attribute__((ext_vector_type(8)));
typedef short bf16x4 __attribute__((ext_vector_type(4)));
typedef unsigned long long u64;

namespace {

constexpr int NWG = 256, NTHR = 512;
constexpr int D = 1024, HD = 4096, FF = 3584, SK = 32768;
constexpr int AUX_SC1 = 16;
constexpr unsigned SENT = 0xFFFFFFFFu;
constexpr unsigned SPIN_CAP = 1u << 22;

constexpr int WS_E = 0;
constexpr int WS_QG = 4096;
constexpr int WS_GU = WS_QG + 262144;
constexpr int WS_P1 = WS_GU + 262144;
constexpr int WS_LSE = WS_P1 + 1048576;
constexpr int WS_S2 = WS_LSE + 8192;
constexpr int WS_F2 = WS_S2 + 524288;
constexpr int WS_END = WS_F2 + 524288;
constexpr int WS_ALL = WS_END;

constexpr int L_RED = 0;
constexpr int L_Q = 12288;
constexpr int L_ML = L_Q + 16 * 272;
constexpr int L_O = L_ML + 512;
constexpr int L_S = L_O + 32768;
constexpr int L_F = L_S + 16384;
constexpr int L_USED = L_F + 16384;
constexpr int L_BYTES = 84 * 1024;
static_assert(L_USED <= L_BYTES, "lds");

struct Params {
  const unsigned short* x;
  const unsigned short* kc;
  const unsigned short* vc;
  const unsigned short* wq;
  const unsigned short* wo;
  const unsigned short* wg;
  const unsigned short* wu;
  const unsigned short* wd;
  unsigned short* out;
  unsigned char* ws;
};

__device__ __forceinline__ float bflo(unsigned u) { return __builtin_bit_cast(float, u << 16); }
__device__ __forceinline__ float bfhi(unsigned u) { return __builtin_bit_cast(float, u & 0xFFFF0000u); }
__device__ __forceinline__ unsigned f2u(float f) { return __builtin_bit_cast(unsigned, f); }
__device__ __forceinline__ float u2f(unsigned u) { return __builtin_bit_cast(float, u); }
typedef __bf16 bf16p __attribute__((ext_vector_type(2)));
typedef float f32x2 __attribute__((ext_vector_type(2)));
__device__ __forceinline__ unsigned cvtpk(float lo, float hi) {
  return __builtin_bit_cast(unsigned, __builtin_convertvector(f32x2{lo, hi}, bf16p));
}
template <int CTRL> __device__ __forceinline__ float dpp(float x) {
  return __builtin_bit_cast(float, __builtin_amdgcn_mov_dpp(__builtin_bit_cast(int, x), CTRL, 0xf, 0xf, true));
}
constexpr int XOR1 = 0xB1, XOR2 = 0x4E, XOR7 = 0x141, XOR8 = 0x128, ROR4 = 0x124, XOR15 = 0x140;
__device__ __forceinline__ float x16_sum(float x) {
  auto s = __builtin_amdgcn_permlane16_swap(f2u(x), f2u(x), false, false);
  return u2f(s[0]) + u2f(s[1]);
}
__device__ __forceinline__ float x32_sum(float x) {
  auto s = __builtin_amdgcn_permlane32_swap(f2u(x), f2u(x), false, false);
  return u2f(s[0]) + u2f(s[1]);
}
__device__ __forceinline__ float x16_max(float x) {
  auto s = __builtin_amdgcn_permlane16_swap(f2u(x), f2u(x), false, false);
  return fmaxf(u2f(s[0]), u2f(s[1]));
}
__device__ __forceinline__ float x32_max(float x) {
  auto s = __builtin_amdgcn_permlane32_swap(f2u(x), f2u(x), false, false);
  return fmaxf(u2f(s[0]), u2f(s[1]));
}
__device__ __forceinline__ float row16_sum(float x) {
  x += dpp<XOR1>(x);
  x += dpp<XOR2>(x);
  x += dpp<XOR7>(x);
  x += dpp<XOR8>(x);
  return x;
}
__device__ __forceinline__ float row16_max(float x) {
  x = fmaxf(x, dpp<XOR1>(x));
  x = fmaxf(x, dpp<XOR2>(x));
  x = fmaxf(x, dpp<XOR7>(x));
  x = fmaxf(x, dpp<XOR8>(x));
  return x;
}
__device__ __forceinline__ float wave_sum(float x) { return x32_sum(x16_sum(row16_sum(x))); }
__device__ __forceinline__ float tr16(float a, float b) {
  auto s = __builtin_amdgcn_permlane16_swap(f2u(a), f2u(b), false, false);
  return u2f(s[0]) + u2f(s[1]);
}
__device__ __forceinline__ float tr32(float a, float b) {
  auto s = __builtin_amdgcn_permlane32_swap(f2u(a), f2u(b), false, false);
  return u2f(s[0]) + u2f(s[1]);
}
__device__ __forceinline__ v4u ldnt(const GAS v4u* p) { return __builtin_nontemporal_load(p); }
#define LD4(dst, ptr, stride, plain, rsrc, base) do { const int o_ = (int)((const GAS unsigned char*)(ptr) - (const GAS unsigned char*)(base)); if (plain) { _Pragma("unroll") for (int j = 0; j < 4; ++j) dst[j] = __builtin_amdgcn_raw_buffer_load_b128(rsrc, o_ + j * (stride) * 16, 0, 0); } else { _Pragma("unroll") for (int j = 0; j < 4; ++j) dst[j] = __builtin_amdgcn_raw_buffer_load_b128(rsrc, o_ + j * (stride) * 16, 0, 2); } } while (0)
__device__ __forceinline__ v2u ldnt2(const GAS v2u* p) { return __builtin_nontemporal_load(p); }
__device__ __forceinline__ unsigned clean(float f) {
  const unsigned u = f2u(f);
  return u == SENT ? 0x7FC00000u : u;
}
__device__ __forceinline__ v4u clean4(f32x4 v) { return v4u{clean(v.x), clean(v.y), clean(v.z), clean(v.w)}; }
__device__ __forceinline__ bool full4(v4u v) { return v.x != SENT && v.y != SENT && v.z != SENT && v.w != SENT; }
__device__ __forceinline__ float rdl(float v, int lane) { return u2f(__builtin_amdgcn_readlane(f2u(v), lane)); }
__device__ __forceinline__ float silu(float v) { return v / (1.0f + __expf(-v)); }

#define SB() __builtin_amdgcn_sched_barrier(0)
#define WG_BARRIER()                                        \
  do {                                                      \
    asm volatile("s_waitcnt lgkmcnt(0)" ::: "memory");      \
    __builtin_amdgcn_s_barrier();                           \
    asm volatile("" ::: "memory");                          \
  } while (0)
#define STAMP(i)

__device__ __forceinline__ f32x4 gemv64(const v4u (&w)[4], const float (&x0)[4], const float (&x1)[4]) {
  float a0[8], a1[8];
#pragma unroll
  for (int c = 0; c < 8; ++c) { a0[c] = 0.f; a1[c] = 0.f; }
#pragma unroll
  for (int i = 0; i < 4; ++i) {
#pragma unroll
    for (int k = 0; k < 4; ++k) {
      const float lo = bflo(w[i][k]), hi = bfhi(w[i][k]);
      a0[2 * k] = fmaf(lo, x0[i], a0[2 * k]);
      a0[2 * k + 1] = fmaf(hi, x0[i], a0[2 * k + 1]);
      a1[2 * k] = fmaf(lo, x1[i], a1[2 * k]);
      a1[2 * k + 1] = fmaf(hi, x1[i], a1[2 * k + 1]);
    }
  }
  float t[8];
#pragma unroll
  for (int c = 0; c < 8; ++c) t[c] = tr16(a0[c], a1[c]);
  f32x4 r;
#pragma unroll
  for (int c = 0; c < 4; ++c) {
    float v = tr32(t[c], t[c + 4]);
    v += dpp<XOR8>(v);
    r[c] = v;
  }
  return r;
}

__device__ __forceinline__ void gemv256(const v2u (&w)[8], const float (&c0)[8], const float (&c1)[8], f32x4& s0, f32x4& s1) {
  s0 = f32x4{0.f, 0.f, 0.f, 0.f};
  s1 = f32x4{0.f, 0.f, 0.f, 0.f};
#pragma unroll
  for (int k = 0; k < 8; ++k) {
    const f32x4 wf = {bflo(w[k].x), bfhi(w[k].x), bflo(w[k].y), bfhi(w[k].y)};
#pragma unroll
    for (int c = 0; c < 4; ++c) {
      s0[c] = fmaf(wf[c], c0[k], s0[c]);
      s1[c] = fmaf(wf[c], c1[k], s1[c]);
    }
  }
}

__global__ void __launch_bounds__(NTHR, 2) fused_decode(Params p) {
  __shared__ __attribute__((aligned(16))) unsigned char smem[L_BYTES];
  LAS unsigned char* L = (LAS unsigned char*)smem;
  const int tid = threadIdx.x, lane = tid & 63, w = __builtin_amdgcn_readfirstlane(tid >> 6);
  const int bid = blockIdx.x;
  const int rg = bid >> 2, cg = bid & 3;
  const int hkv = bid >> 6, bb = (bid >> 5) & 1, sp = bid & 31;
  const bool has_ffn = rg < 56;
  const __amdgpu_buffer_rsrc_t rs = __builtin_amdgcn_make_buffer_rsrc((void*)p.ws, 0, WS_ALL, 0x00020000);
  STAMP(0);

  {
    const v4u s4 = {SENT, SENT, SENT, SENT};
    int off;
    if (tid < 256) off = WS_P1 + bid * 4096 + tid * 16;
    else if (tid < 384) off = WS_S2 + (cg * 64 + rg) * 2048 + (tid - 256) * 16;
    else off = WS_F2 + (cg * 64 + rg) * 2048 + (tid - 384) * 16;
    __builtin_amdgcn_raw_buffer_store_b128(s4, rs, off, 0, AUX_SC1);
    if (tid < 64) __builtin_amdgcn_raw_buffer_store_b128(s4, rs, WS_GU + bid * 1024 + tid * 16, 0, AUX_SC1);
    else if (tid < 66) __builtin_amdgcn_raw_buffer_store_b128(s4, rs, WS_LSE + bid * 32 + (tid - 64) * 16, 0, AUX_SC1);
  }
  SB();
  const v2u ev = __builtin_amdgcn_raw_buffer_load_b64(rs, WS_E, 0, AUX_SC1);
  const GAS v4u* xg = (const GAS v4u*)p.x;
  const v4u xa0 = xg[lane * 2], xa1 = xg[lane * 2 + 1], xb0 = xg[128 + lane * 2], xb1 = xg[128 + lane * 2 + 1];
  const int r8 = lane >> 3, c8 = lane & 7;
  const int krow = cg * 256 + w * 32 + r8 * 4;
  const v2u xr0 = *(const GAS v2u*)(p.x + krow), xr1 = *(const GAS v2u*)(p.x + D + krow);
  v4u wqr[4], wgr[4], wur[4];
#pragma unroll
  for (int i = 0; i < 4; ++i) wqr[i] = ldnt((const GAS v4u*)(p.wq + (size_t)(krow + i) * HD + rg * 64 + c8 * 8));
  const int rgf = has_ffn ? rg : 0;
#pragma unroll
  for (int i = 0; i < 4; ++i) wgr[i] = ldnt((const GAS v4u*)(p.wg + (size_t)(krow + i) * FF + rgf * 64 + c8 * 8));
  SB();

  float x0[4], x1[4];
  {
    float va[16], vb[16];
#pragma unroll
    for (int k = 0; k < 4; ++k) {
      va[2 * k] = bflo(xa0[k]); va[2 * k + 1] = bfhi(xa0[k]); va[8 + 2 * k] = bflo(xa1[k]); va[8 + 2 * k + 1] = bfhi(xa1[k]);
      vb[2 * k] = bflo(xb0[k]); vb[2 * k + 1] = bfhi(xb0[k]); vb[8 + 2 * k] = bflo(xb1[k]); vb[8 + 2 * k + 1] = bfhi(xb1[k]);
    }
    float sa = 0.f, sb = 0.f;
#pragma unroll
    for (int k = 0; k < 16; ++k) { sa += va[k]; sb += vb[k]; }
    const float ma = wave_sum(sa) * (1.0f / D), mb = wave_sum(sb) * (1.0f / D);
    float qa = 0.f, qb = 0.f;
#pragma unroll
    for (int k = 0; k < 16; ++k) { const float da = va[k] - ma, db = vb[k] - mb; qa = fmaf(da, da, qa); qb = fmaf(db, db, qb); }
    const float ra = 1.0f / sqrtf(wave_sum(qa) * (1.0f / D) + 1e-5f), rb = 1.0f / sqrtf(wave_sum(qb) * (1.0f / D) + 1e-5f);
    x0[0] = (bflo(xr0.x) - ma) * ra; x0[1] = (bfhi(xr0.x) - ma) * ra; x0[2] = (bflo(xr0.y) - ma) * ra; x0[3] = (bfhi(xr0.y) - ma) * ra;
    x1[0] = (bflo(xr1.x) - mb) * rb; x1[1] = (bfhi(xr1.x) - mb) * rb; x1[2] = (bflo(xr1.y) - mb) * rb; x1[3] = (bfhi(xr1.y) - mb) * rb;
  }
  const u64 e1 = ((((u64)__builtin_amdgcn_readfirstlane(ev.y)) << 32) | (u64)(unsigned)__builtin_amdgcn_readfirstlane(ev.x)) + 1ull;
  const unsigned tag_hi = (unsigned)(e1 >> 8), tag_lo = (unsigned)(e1 & 0xFFull);
  const int rho = lane >> 4;
  const int redoff = ((rho & 1) * 64 + c8 * 8 + (rho >> 1) * 4) * 4;

  {
    const f32x4 r = gemv64(wqr, x0, x1);
    if ((lane & 8) == 0) *(LAS f32x4*)(L + L_RED + w * 512 + redoff) = r;
  }
#pragma unroll
  for (int i = 0; i < 4; ++i) wur[i] = ldnt((const GAS v4u*)(p.wu + (size_t)(krow + i) * FF + rgf * 64 + c8 * 8));
  SB();
  WG_BARRIER();
  if (tid < 128) {
    float s = 0.f;
#pragma unroll
    for (int ww = 0; ww < 8; ++ww) s += *(LAS float*)(L + L_RED + ww * 512 + tid * 4);
    const unsigned v24 = (f2u(s) + 0x80u) >> 8;
    const v2u gr = {(tag_lo << 24) | v24, tag_hi};
    const int b = tid >> 6, c = tid & 63;
    __builtin_amdgcn_raw_buffer_store_b64(gr, rs, WS_QG + (((hkv * 2 + b) * 4 + cg) * 1024 + (rg & 15) * 64 + c) * 8, 0, AUX_SC1);
  }
  STAMP(1);
  SB();

  v2u wdr[8], wor[8];
#pragma unroll
  for (int k = 0; k < 8; ++k) wdr[k] = ldnt2((const GAS v2u*)(p.wd + (size_t)(rgf * 64 + w * 8 + k) * D + cg * 256 + lane * 4));
  SB();
  {
    const f32x4 r = gemv64(wgr, x0, x1);
    if ((lane & 8) == 0) *(LAS f32x4*)(L + L_RED + 4096 + w * 512 + redoff) = r;
  }
  const int dg = (rg & 1) * 8 + w;
  {
    const int rbase = (rg >> 1) * 128 + (dg >> 2) * 32 + (dg & 3) * 2;
#pragma unroll
    for (int k = 0; k < 8; ++k) wor[k] = ldnt2((const GAS v2u*)(p.wo + (size_t)(rbase + (k & 3) * 8 + (k >> 2)) * D + cg * 256 + lane * 4));
  }
  SB();
  {
    const f32x4 r = gemv64(wur, x0, x1);
    if ((lane & 8) == 0) *(LAS f32x4*)(L + L_RED + 8192 + w * 512 + redoff) = r;
  }
  WG_BARRIER();
  if (tid < 64 && has_ffn) {
    f32x4 s = {0.f, 0.f, 0.f, 0.f};
#pragma unroll
    for (int ww = 0; ww < 8; ++ww) s += *(LAS f32x4*)(L + L_RED + 4096 + (tid >> 5) * 4096 + ww * 512 + (tid & 31) * 16);
    __builtin_amdgcn_raw_buffer_store_b128(clean4(s), rs, WS_GU + bid * 1024 + tid * 16, 0, AUX_SC1);
  }
  STAMP(2);
  SB();

  const int i16 = lane & 15, g4 = lane >> 4;
  const size_t kvbase = ((size_t)(bb * 4 + hkv) * SK + (size_t)sp * 1024 + w * 16) * 128;
  const GAS v4u* kp = (const GAS v4u*)(p.kc + kvbase) + (4 * (i16 & 3) + (i16 >> 2)) * 16 + g4;
  const GAS v4u* vp = (const GAS v4u*)(p.vc + kvbase) + lane;
  v4u kb[2][4], vb[2][4];
  const __amdgpu_buffer_rsrc_t rk = __builtin_amdgcn_make_buffer_rsrc((void*)p.kc, 0, 64 << 20, 0x00020000);
  const __amdgpu_buffer_rsrc_t rv = __builtin_amdgcn_make_buffer_rsrc((void*)p.vc, 0, 64 << 20, 0x00020000);
  const bool kpl = (KPL >> bb) & 1, vpl = (VPL >> bb) & 1;
  {
    const int qoff = WS_QG + ((hkv * 2 + bb) * 4096 + w * 128 + lane * 2) * 8;
    v4u qg[4];
#pragma unroll
    for (int ks = 0; ks < 4; ++ks) qg[ks] = __builtin_amdgcn_raw_buffer_load_b128(rs, qoff + ks * 8192, 0, AUX_SC1);
    SB();
    LD4(kb[0], kp, 4, kpl, rk, p.kc);
    LD4(vb[0], vp, 64, vpl, rv, p.vc);
    SB();
    for (unsigned spins = 0; spins < SPIN_CAP; ++spins) {
      bool ok = true;
#pragma unroll
      for (int ks = 0; ks < 4; ++ks)
        ok = ok && qg[ks].y == tag_hi && (qg[ks].x >> 24) == tag_lo && qg[ks].w == tag_hi && (qg[ks].z >> 24) == tag_lo;
      if (__all(ok)) break;
      __builtin_amdgcn_s_sleep(4);
#pragma unroll
      for (int ks = 0; ks < 4; ++ks) qg[ks] = __builtin_amdgcn_raw_buffer_load_b128(rs, qoff + ks * 8192, 0, AUX_SC1);
    }
    float q0 = 0.f, q1 = 0.f;
#pragma unroll
    for (int ks = 0; ks < 4; ++ks) { q0 += u2f(qg[ks].x << 8); q1 += u2f(qg[ks].z << 8); }
    const unsigned hi = cvtpk(q0, q1);
    const unsigned lo = cvtpk(q0 - bflo(hi), q1 - bfhi(hi));
    *(LAS unsigned*)(L + L_Q + w * 272 + lane * 4) = hi;
    *(LAS unsigned*)(L + L_Q + (w + 8) * 272 + lane * 4) = lo;
  }
  WG_BARRIER();
  v4u qf[4];
#pragma unroll
  for (int kk = 0; kk < 4; ++kk) qf[kk] = *(LAS v4u*)(L + L_Q + i16 * 272 + kk * 64 + g4 * 16);
  STAMP(3);

  constexpr float C2 = 0.088388347648318440f * 1.4426950408889634f;
  float m = -INFINITY, l = 0.f;
  f32x4 o[8];
#pragma unroll
  for (int e = 0; e < 8; ++e) o[e] = f32x4{0.f, 0.f, 0.f, 0.f};
  float fsum[8];
#pragma unroll
  for (int u = 0; u < 8; ++u) {
    v4u(&kc)[4] = kb[u & 1];
    v4u(&vc)[4] = vb[u & 1];
    f32x4 s = {0.f, 0.f, 0.f, 0.f};
    asm volatile("" : "+v"(kc[0]), "+v"(kc[1]), "+v"(kc[2]), "+v"(kc[3]));
    if (u + 1 < 8) {
      LD4(kb[(u + 1) & 1], kp + (u + 1) * 2048, 4, kpl, rk, p.kc);
    }
    SB();
#pragma unroll
    for (int kk = 0; kk < 4; ++kk)
      s = __builtin_amdgcn_mfma_f32_16x16x32_bf16(__builtin_bit_cast(bf16x8, kc[kk]), __builtin_bit_cast(bf16x8, qf[kk]), s, 0, 0, 0);
#pragma unroll
    for (int r = 0; r < 4; ++r) s[r] = (s[r] + dpp<XOR8>(s[r])) * C2;
    float mx = fmaxf(fmaxf(s[0], s[1]), fmaxf(s[2], s[3]));
    mx = x32_max(x16_max(mx));
    const float mn = fmaxf(m, mx);
    const float alpha = __builtin_amdgcn_exp2f(m - mn);
    m = mn;
    const float p0 = __builtin_amdgcn_exp2f(s[0] - mn), p1 = __builtin_amdgcn_exp2f(s[1] - mn);
    const float p2 = __builtin_amdgcn_exp2f(s[2] - mn), p3 = __builtin_amdgcn_exp2f(s[3] - mn);
    l = fmaf(l, alpha, (p0 + p1) + (p2 + p3));
    const v2u pb = {cvtpk(p0, p1), cvtpk(p2, p3)};
#pragma unroll
    for (int e = 0; e < 8; ++e) o[e] *= alpha;
    SB();
    asm volatile("" : "+v"(vc[0]), "+v"(vc[1]), "+v"(vc[2]), "+v"(vc[3]));
    if (u + 1 < 8) {
      LD4(vb[(u + 1) & 1], vp + (u + 1) * 2048, 64, vpl, rv, p.vc);
    }
    SB();
#pragma unroll
    for (int e = 0; e < 8; ++e) {
      const unsigned sel = (e & 1) ? 0x07060302u : 0x05040100u;
      const v2u a = {__builtin_amdgcn_perm(vc[1][e >> 1], vc[0][e >> 1], sel), __builtin_amdgcn_perm(vc[3][e >> 1], vc[2][e >> 1], sel)};
      o[e] = __builtin_amdgcn_mfma_f32_16x16x16bf16_1k(__builtin_bit_cast(bf16x4, a), __builtin_bit_cast(bf16x4, pb), o[e], 0, 0, 0);
    }
    SB();

    if (u == 3) {
      if (w == 7) {
        for (unsigned spins = 0; spins < SPIN_CAP; ++spins) {
          bool ok = true;
#pragma unroll
          for (int j = 0; j < 4; ++j) {
            const int pb2 = lane * 4 + j;
            const v2u gq = __builtin_amdgcn_raw_buffer_load_b64(rs, WS_QG + ((((pb2 >> 6) * 2) * 4 + j) * 1024 + (lane & 15) * 64) * 8, 0, AUX_SC1);
            ok = ok && gq.y == tag_hi && (gq.x >> 24) == tag_lo;
          }
          if (__all(ok)) break;
          __builtin_amdgcn_s_sleep(4);
        }
      }
      if (has_ffn) {
        const int l5 = lane & 31;
        const int goff = WS_GU + (rg * 4 + (l5 >> 3)) * 1024 + ((l5 >> 1) & 3) * 256 + (w * 8 + (l5 & 1) * 4) * 4;
        v4u gu = __builtin_amdgcn_raw_buffer_load_b128(rs, goff, 0, AUX_SC1);
        for (unsigned spins = 0; spins < SPIN_CAP; ++spins) {
          if (__all(full4(gu))) break;
          __builtin_amdgcn_s_sleep(4);
          gu = __builtin_amdgcn_raw_buffer_load_b128(rs, goff, 0, AUX_SC1);
        }
        float h0[8], h1[8];
#pragma unroll
        for (int c = 0; c < 4; ++c) {
          float v = u2f(gu[c]);
          v += dpp<XOR8>(v);
          v = x16_sum(v);
          const float uu = dpp<0x104>(v);
          const float h = silu(v) * uu;
          h0[c] = rdl(h, 0); h0[4 + c] = rdl(h, 1); h1[c] = rdl(h, 2); h1[4 + c] = rdl(h, 3);
        }
        f32x4 s0, s1;
        gemv256(wdr, h0, h1, s0, s1);
        *(LAS f32x4*)(L + L_F + w * 2048 + lane * 16) = s0;
        *(LAS f32x4*)(L + L_F + w * 2048 + 1024 + lane * 16) = s1;
      }
      WG_BARRIER();
      if (tid < 128 && has_ffn) {
        f32x4 s4 = {0.f, 0.f, 0.f, 0.f};
#pragma unroll
        for (int ww = 0; ww < 8; ++ww) s4 += *(LAS f32x4*)(L + L_F + ww * 2048 + tid * 16);
        __builtin_amdgcn_raw_buffer_store_b128(clean4(s4), rs, WS_F2 + ((cg * 64 + (tid & 63)) * 64 + rg) * 32 + (tid >> 6) * 16, 0, AUX_SC1);
      }
      if (bid == 0 && tid == 0) {
        const v2u ne = {(unsigned)e1, (unsigned)(e1 >> 32)};
        __builtin_amdgcn_raw_buffer_store_b64(ne, rs, WS_E, 0, AUX_SC1);
      }
      STAMP(4);
      SB();
    }
  }
  STAMP(5);

  l = x32_sum(x16_sum(l));
  if (i16 < 8) {
#pragma unroll
    for (int e = 0; e < 8; ++e) *(LAS f32x4*)(L + L_O + ((w * 8 + e) * 32 + g4 * 8 + i16) * 16) = o[e];
    if (g4 == 0) *(LAS v2u*)(L + L_ML + (w * 8 + i16) * 8) = v2u{f2u(m), f2u(l)};
  }
  WG_BARRIER();
  const int prod = bb * 32 + sp;
  if (tid < 256) {
    const int j = tid & 7, e = tid >> 5, g = (tid >> 3) & 3;
    float mw[8], lw[8];
#pragma unroll
    for (int ww = 0; ww < 8; ++ww) { const v2u t = *(LAS v2u*)(L + L_ML + (ww * 8 + j) * 8); mw[ww] = u2f(t.x); lw[ww] = u2f(t.y); }
    float M = mw[0];
#pragma unroll
    for (int ww = 1; ww < 8; ++ww) M = fmaxf(M, mw[ww]);
    float Ls = 0.f;
    f32x4 acc = {0.f, 0.f, 0.f, 0.f};
#pragma unroll
    for (int ww = 0; ww < 8; ++ww) {
      const float wt = __builtin_amdgcn_exp2f(mw[ww] - M);
      Ls = fmaf(wt, lw[ww], Ls);
      acc += *(LAS f32x4*)(L + L_O + ww * 4096 + tid * 16) * wt;
    }
    const float inv = 1.0f / Ls;
    acc *= inv;
    const int head = hkv * 8 + j;
    __builtin_amdgcn_raw_buffer_store_b128(clean4(acc), rs, WS_P1 + ((head * 16 + g * 4 + (e >> 1)) * 64 + prod) * 32 + (e & 1) * 16, 0, AUX_SC1);
    if (tid < 8) __builtin_amdgcn_raw_buffer_store_b32(clean(M + __builtin_amdgcn_logf(Ls)), rs, WS_LSE + (head * 64 + prod) * 4, 0, AUX_SC1);
  }
  STAMP(6);
  if (w == 7) {
    const int foff = WS_F2 + ((cg * 64 + rg) * 64 + (lane < 56 ? lane : 0)) * 32;
    v4u fa = __builtin_amdgcn_raw_buffer_load_b128(rs, foff, 0, AUX_SC1), fb = __builtin_amdgcn_raw_buffer_load_b128(rs, foff + 16, 0, AUX_SC1);
    for (unsigned spins = 0; spins < SPIN_CAP; ++spins) {
      if (__all(full4(fa) && full4(fb))) break;
      __builtin_amdgcn_s_sleep(4);
      fa = __builtin_amdgcn_raw_buffer_load_b128(rs, foff, 0, AUX_SC1);
      fb = __builtin_amdgcn_raw_buffer_load_b128(rs, foff + 16, 0, AUX_SC1);
    }
#pragma unroll
    for (int c = 0; c < 4; ++c) {
      fsum[c] = wave_sum(lane < 56 ? u2f(fa[c]) : 0.f);
      fsum[4 + c] = wave_sum(lane < 56 ? u2f(fb[c]) : 0.f);
    }
  }

  {
    const int head = rg >> 1;
    const int loff = WS_LSE + (head * 64 + lane) * 4;
    const int poff = WS_P1 + ((head * 16 + dg) * 64 + lane) * 32;
    unsigned lse = SENT;
    v4u pa = {SENT, SENT, SENT, SENT}, pb = {SENT, SENT, SENT, SENT};
    bool hl = false, hd = false;
    for (unsigned spins = 0; spins < SPIN_CAP; ++spins) {
      const bool few = __builtin_popcountll(__ballot(!hd)) <= 8;
      const bool want = !hd && (hl || few);
      if (!hl) lse = __builtin_amdgcn_raw_buffer_load_b32(rs, loff, 0, AUX_SC1);
      if (want) {
        pa = __builtin_amdgcn_raw_buffer_load_b128(rs, poff, 0, AUX_SC1);
        pb = __builtin_amdgcn_raw_buffer_load_b128(rs, poff + 16, 0, AUX_SC1);
      }
      hl = lse != SENT;
      hd = full4(pa) && full4(pb);
      if (__all(hl && hd)) break;
      __builtin_amdgcn_s_sleep(2);
    }
    const float ls = u2f(lse);
    const float Mx = x16_max(row16_max(ls));
    const float wt = __builtin_amdgcn_exp2f(ls - Mx);
    const float W = x16_sum(row16_sum(wt));
    const float wn = wt / W;
    float c0[8], c1[8];
#pragma unroll
    for (int k = 0; k < 8; ++k) {
      float v = u2f(k < 4 ? pa[k & 3] : pb[k & 3]) * wn;
      v = x16_sum(row16_sum(v));
      c0[k] = rdl(v, 0);
      c1[k] = rdl(v, 32);
    }
    f32x4 s0, s1;
    gemv256(wor, c0, c1, s0, s1);
    *(LAS f32x4*)(L + L_S + w * 2048 + lane * 16) = s0;
    *(LAS f32x4*)(L + L_S + w * 2048 + 1024 + lane * 16) = s1;
  }
  STAMP(7);
  WG_BARRIER();
  if (tid < 128) {
    f32x4 s4 = {0.f, 0.f, 0.f, 0.f};
#pragma unroll
    for (int ww = 0; ww < 8; ++ww) s4 += *(LAS f32x4*)(L + L_S + ww * 2048 + tid * 16);
    __builtin_amdgcn_raw_buffer_store_b128(clean4(s4), rs, WS_S2 + ((cg * 64 + (tid & 63)) * 64 + rg) * 32 + (tid >> 6) * 16, 0, AUX_SC1);
  }
  STAMP(8);

  if (w == 7) {
    const int soff = WS_S2 + ((cg * 64 + rg) * 64 + lane) * 32;
    v4u sa = {SENT, SENT, SENT, SENT}, sb = {SENT, SENT, SENT, SENT};
    bool hd = false;
    const int col = cg * 256 + rg * 4;
    const v2u xo0 = *(const GAS v2u*)(p.x + col), xo1 = *(const GAS v2u*)(p.x + D + col);
    for (unsigned spins = 0; spins < SPIN_CAP; ++spins) {
      if (!hd) {
        sa = __builtin_amdgcn_raw_buffer_load_b128(rs, soff, 0, AUX_SC1);
        sb = __builtin_amdgcn_raw_buffer_load_b128(rs, soff + 16, 0, AUX_SC1);
      }
      hd = full4(sa) && full4(sb);
      if (__all(hd)) break;
      __builtin_amdgcn_s_sleep(2);
    }
    float r[8];
#pragma unroll
    for (int c = 0; c < 4; ++c) { r[c] = wave_sum(u2f(sa[c])); r[4 + c] = wave_sum(u2f(sb[c])); }
    const float xv[8] = {bflo(xo0.x), bfhi(xo0.x), bflo(xo0.y), bfhi(xo0.y), bflo(xo1.x), bfhi(xo1.x), bflo(xo1.y), bfhi(xo1.y)};
#pragma unroll
    for (int c = 0; c < 8; ++c) r[c] = fmaf(r[c], silu(fsum[c]), xv[c]);
    if (lane == 0) {
      *(GAS v2u*)(p.out + col) = v2u{cvtpk(r[0], r[1]), cvtpk(r[2], r[3])};
      *(GAS v2u*)(p.out + D + col) = v2u{cvtpk(r[4], r[5]), cvtpk(r[6], r[7])};
    }
  }
}

}

extern "C" void kernel_launch(void* const* d_in, const int* in_sizes, int n_in, void* d_out, int out_size, void* d_ws,
                              size_t ws_size, hipStream_t stream) {
  Params p;
  p.x = (const unsigned short*)d_in[0];
  p.kc = (const unsigned short*)d_in[1];
  p.vc = (const unsigned short*)d_in[2];
  p.wq = (const unsigned short*)d_in[3];
  p.wo = (const unsigned short*)d_in[4];
  p.wg = (const unsigned short*)d_in[6];
  p.wu = (const unsigned short*)d_in[7];
  p.wd = (const unsigned short*)d_in[8];
  p.out = (unsigned short*)d_out;
  p.ws = (unsigned char*)d_ws;
  hipLaunchKernelGGL(fused_decode, dim3(NWG), dim3(NTHR), 0, stream, p);
}
```

```cpp
#include <hip/hip_runtime.h>
#include <hip/hip_bf16.h>
#include <stdint.h>

#define GAS __attribute__((address_space(1)))
#define LAS __attribute__((address_space(3)))
typedef unsigned v4u __attribute__((ext_vector_type(4)));
typedef unsigned v2u __attribute__((ext_vector_type(2)));
typedef float f32x4 __attribute__((ext_vector_type(4)));
typedef short bf16x8 __attribute__((ext_vector_type(8)));
typedef short bf16x4 __attribute__((ext_vector_type(4)));
typedef unsigned long long u64;

namespace {

constexpr int NWG = 256, NTHR = 512;
constexpr int D = 1024, HD = 4096, FF = 3584, SK = 32768;
constexpr int AUX_SC1 = 16;
constexpr unsigned SENT = 0xFFFFFFFFu;
constexpr unsigned SPIN_CAP = 1u << 22;

constexpr int WS_E = 0;
constexpr int WS_QG = 4096;
constexpr int WS_GU = WS_QG + 262144;
constexpr int WS_P1 = WS_GU + 524288;
constexpr int WS_S2 = WS_P1 + 1048576;
constexpr int WS_F2 = WS_S2 + 524288;
constexpr int WS_END = WS_F2 + 524288;
constexpr int WS_ALL = WS_END;

constexpr int L_RED = 0;
constexpr int L_Q = 12288;
constexpr int L_ML = L_Q + 16 * 272;
constexpr int L_O = L_ML + 512;
constexpr int L_S = L_O + 32768;
constexpr int L_F = L_S + 16384;
constexpr int L_C = L_F + 16384;
constexpr int L_R = L_C + 2048 + 64;
constexpr int L_USED = L_R + 2048;
constexpr int L_BYTES = 88 * 1024;
static_assert(L_USED <= L_BYTES, "lds");

struct Params {
  const unsigned short* x;
  const unsigned short* kc;
  const unsigned short* vc;
  const unsigned short* wq;
  const unsigned short* wo;
  const unsigned short* wg;
  const unsigned short* wu;
  const unsigned short* wd;
  unsigned short* out;
  unsigned char* ws;
};

__device__ __forceinline__ float bflo(unsigned u) { return __builtin_bit_cast(float, u << 16); }
__device__ __forceinline__ float bfhi(unsigned u) { return __builtin_bit_cast(float, u & 0xFFFF0000u); }
__device__ __forceinline__ unsigned f2u(float f) { return __builtin_bit_cast(unsigned, f); }
__device__ __forceinline__ float u2f(unsigned u) { return __builtin_bit_cast(float, u); }
typedef __bf16 bf16p __attribute__((ext_vector_type(2)));
typedef float f32x2 __attribute__((ext_vector_type(2)));
__device__ __forceinline__ unsigned cvtpk(float lo, float hi) {
  return __builtin_bit_cast(unsigned, __builtin_convertvector(f32x2{lo, hi}, bf16p));
}
template <int CTRL> __device__ __forceinline__ float dpp(float x) {
  return __builtin_bit_cast(float, __builtin_amdgcn_mov_dpp(__builtin_bit_cast(int, x), CTRL, 0xf, 0xf, true));
}
constexpr int XOR1 = 0xB1, XOR2 = 0x4E, XOR7 = 0x141, XOR8 = 0x128, ROR4 = 0x124, XOR15 = 0x140;
__device__ __forceinline__ float x16_sum(float x) {
  auto s = __builtin_amdgcn_permlane16_swap(f2u(x), f2u(x), false, false);
  return u2f(s[0]) + u2f(s[1]);
}
__device__ __forceinline__ float x32_sum(float x) {
  auto s = __builtin_amdgcn_permlane32_swap(f2u(x), f2u(x), false, false);
  return u2f(s[0]) + u2f(s[1]);
}
__device__ __forceinline__ float x16_max(float x) {
  auto s = __builtin_amdgcn_permlane16_swap(f2u(x), f2u(x), false, false);
  return fmaxf(u2f(s[0]), u2f(s[1]));
}
__device__ __forceinline__ float x32_max(float x) {
  auto s = __builtin_amdgcn_permlane32_swap(f2u(x), f2u(x), false, false);
  return fmaxf(u2f(s[0]), u2f(s[1]));
}
__device__ __forceinline__ float row16_sum(float x) {
  x += dpp<XOR1>(x);
  x += dpp<XOR2>(x);
  x += dpp<XOR7>(x);
  x += dpp<XOR8>(x);
  return x;
}
__device__ __forceinline__ float row16_max(float x) {
  x = fmaxf(x, dpp<XOR1>(x));
  x = fmaxf(x, dpp<XOR2>(x));
  x = fmaxf(x, dpp<XOR7>(x));
  x = fmaxf(x, dpp<XOR8>(x));
  return x;
}
__device__ __forceinline__ float wave_sum(float x) { return x32_sum(x16_sum(row16_sum(x))); }
__device__ __forceinline__ float tr16(float a, float b) {
  auto s = __builtin_amdgcn_permlane16_swap(f2u(a), f2u(b), false, false);
  return u2f(s[0]) + u2f(s[1]);
}
__device__ __forceinline__ float tr32(float a, float b) {
  auto s = __builtin_amdgcn_permlane32_swap(f2u(a), f2u(b), false, false);
  return u2f(s[0]) + u2f(s[1]);
}
__device__ __forceinline__ v4u ldnt(const GAS v4u* p) { return __builtin_nontemporal_load(p); }
__device__ __forceinline__ v2u ldnt2(const GAS v2u* p) { return __builtin_nontemporal_load(p); }
__device__ __forceinline__ unsigned clean(float f) {
  const unsigned u = f2u(f);
  return u == SENT ? 0x7FC00000u : u;
}
__device__ __forceinline__ v4u clean4(f32x4 v) { return v4u{clean(v.x), clean(v.y), clean(v.z), clean(v.w)}; }
__device__ __forceinline__ bool full4(v4u v) { return v.x != SENT && v.y != SENT && v.z != SENT && v.w != SENT; }
__device__ __forceinline__ float rdl(float v, int lane) { return u2f(__builtin_amdgcn_readlane(f2u(v), lane)); }
__device__ __forceinline__ float silu(float v) { return v / (1.0f + __expf(-v)); }

#define SB() __builtin_amdgcn_sched_barrier(0)
#define WG_BARRIER()                                        \
  do {                                                      \
    asm volatile("s_waitcnt lgkmcnt(0)" ::: "memory");      \
    __builtin_amdgcn_s_barrier();                           \
    asm volatile("" ::: "memory");                          \
  } while (0)
#define STAMP(i)

__device__ __forceinline__ f32x4 gemv64(const v4u (&w)[4], const float (&x0)[4], const float (&x1)[4]) {
  float a0[8], a1[8];
#pragma unroll
  for (int c = 0; c < 8; ++c) { a0[c] = 0.f; a1[c] = 0.f; }
#pragma unroll
  for (int i = 0; i < 4; ++i) {
#pragma unroll
    for (int k = 0; k < 4; ++k) {
      const float lo = bflo(w[i][k]), hi = bfhi(w[i][k]);
      a0[2 * k] = fmaf(lo, x0[i], a0[2 * k]);
      a0[2 * k + 1] = fmaf(hi, x0[i], a0[2 * k + 1]);
      a1[2 * k] = fmaf(lo, x1[i], a1[2 * k]);
      a1[2 * k + 1] = fmaf(hi, x1[i], a1[2 * k + 1]);
    }
  }
  float t[8];
#pragma unroll
  for (int c = 0; c < 8; ++c) t[c] = tr16(a0[c], a1[c]);
  f32x4 r;
#pragma unroll
  for (int c = 0; c < 4; ++c) {
    float v = tr32(t[c], t[c + 4]);
    v += dpp<XOR8>(v);
    r[c] = v;
  }
  return r;
}

__device__ __forceinline__ void gemv256(const v2u (&w)[8], const float (&c0)[8], const float (&c1)[8], f32x4& s0, f32x4& s1) {
  s0 = f32x4{0.f, 0.f, 0.f, 0.f};
  s1 = f32x4{0.f, 0.f, 0.f, 0.f};
#pragma unroll
  for (int k = 0; k < 8; ++k) {
    const f32x4 wf = {bflo(w[k].x), bfhi(w[k].x), bflo(w[k].y), bfhi(w[k].y)};
#pragma unroll
    for (int c = 0; c < 4; ++c) {
      s0[c] = fmaf(wf[c], c0[k], s0[c]);
      s1[c] = fmaf(wf[c], c1[k], s1[c]);
    }
  }
}

__device__ __forceinline__ v2u granule(float v, unsigned tag_lo, unsigned tag_hi) {
  return v2u{(tag_lo << 24) | ((f2u(v) + 0x80u) >> 8), tag_hi};
}
__device__ __forceinline__ bool tag2(v4u g, unsigned tag_lo, unsigned tag_hi) {
  return (g.y == tag_hi) & ((g.x >> 24) == tag_lo) & (g.w == tag_hi) & ((g.z >> 24) == tag_lo);
}
#define TOUCH4(a) asm volatile("" : "+v"(a[0]), "+v"(a[1]), "+v"(a[2]), "+v"(a[3]))
#define TOUCH8(a) asm volatile("" : "+v"(a[0]), "+v"(a[1]), "+v"(a[2]), "+v"(a[3]), "+v"(a[4]), "+v"(a[5]), "+v"(a[6]), "+v"(a[7]))
#define LDK(u_) do { _Pragma("unroll") for (int j = 0; j < 4; ++j) kb[(u_) % NB][j] = ldnt(kp + (u_) * 2048 + j * 4); SB(); } while (0)
#define LDV(u_) do { _Pragma("unroll") for (int j = 0; j < 4; ++j) vb[(u_) % NB][j] = ldnt(vp + (u_) * 2048 + j * 64); SB(); } while (0)

__device__ __forceinline__ void reduce_lines(LAS unsigned char* dst, __amdgpu_buffer_rsrc_t rs, int off, bool live, int lane) {
  v4u a = __builtin_amdgcn_raw_buffer_load_b128(rs, off, 0, AUX_SC1);
  bool hd = !live | full4(a);
  for (unsigned spins = 0; spins < SPIN_CAP; ++spins) {
    if (__all(hd)) break;
    __builtin_amdgcn_s_sleep(1);
    if (!hd) a = __builtin_amdgcn_raw_buffer_load_b128(rs, off, 0, AUX_SC1);
    hd = !live | full4(a);
  }
  const float a0 = live ? u2f(a.x) : 0.f, a1 = live ? u2f(a.y) : 0.f, a2 = live ? u2f(a.z) : 0.f, a3 = live ? u2f(a.w) : 0.f;
  float v = tr32(tr16(a0, a1), tr16(a2, a3));
  v += dpp<XOR8>(v);
  if ((lane & 8) == 0) *(LAS float*)(dst + ((lane & 7) * 4 + (lane >> 4)) * 4) = v;
}

__global__ void __launch_bounds__(NTHR, 2) fused_decode(Params p) {
  __shared__ __attribute__((aligned(16))) unsigned char smem[L_BYTES];
  LAS unsigned char* L = (LAS unsigned char*)smem;
  const int tid = threadIdx.x, lane = tid & 63, w = __builtin_amdgcn_readfirstlane(tid >> 6);
  const int bid = blockIdx.x;
  const int cg = (bid >> 3) & 3, rg = (bid >> 5) * 8 + (bid & 7);
  const int hkv = bid >> 6, bb = (bid >> 5) & 1, sp = bid & 31;
  const bool has_ffn = rg < 56;
  const bool reducer = rg >= 48;
  const __amdgpu_buffer_rsrc_t rs = __builtin_amdgcn_make_buffer_rsrc((void*)p.ws, 0, WS_ALL, 0x00020000);
  STAMP(0);

  {
    const v4u s4 = {SENT, SENT, SENT, SENT};
    const int off = tid < 256 ? WS_P1 + bid * 4096 + tid * 16
                              : (tid < 384 ? WS_S2 - 4096 : WS_F2 - 6144) + (cg * 64 + rg) * 2048 + tid * 16;
    __builtin_amdgcn_raw_buffer_store_b128(s4, rs, off, 0, AUX_SC1);
  }
  SB();
  const v2u ev = __builtin_amdgcn_raw_buffer_load_b64(rs, WS_E, 0, AUX_SC1);
  const GAS v4u* xg = (const GAS v4u*)p.x;
  const v4u xa0 = xg[lane * 2], xa1 = xg[lane * 2 + 1], xb0 = xg[128 + lane * 2], xb1 = xg[128 + lane * 2 + 1];
  const int r8 = lane >> 3, c8 = lane & 7;
  const int krow = cg * 256 + w * 32 + r8 * 4;
  const v2u xr0 = *(const GAS v2u*)(p.x + krow), xr1 = *(const GAS v2u*)(p.x + D + krow);
  v4u wqr[4], wgr[4], wur[4];
#pragma unroll
  for (int i = 0; i < 4; ++i) wqr[i] = ldnt((const GAS v4u*)(p.wq + (size_t)(krow + i) * HD + rg * 64 + c8 * 8));
  const int rgf = has_ffn ? rg : 0;
#pragma unroll
  for (int i = 0; i < 4; ++i) wgr[i] = ldnt((const GAS v4u*)(p.wg + (size_t)(krow + i) * FF + rgf * 64 + c8 * 8));
  SB();

  float x0[4], x1[4];
  {
    float va[16], vb[16];
#pragma unroll
    for (int k = 0; k < 4; ++k) {
      va[2 * k] = bflo(xa0[k]); va[2 * k + 1] = bfhi(xa0[k]); va[8 + 2 * k] = bflo(xa1[k]); va[8 + 2 * k + 1] = bfhi(xa1[k]);
      vb[2 * k] = bflo(xb0[k]); vb[2 * k + 1] = bfhi(xb0[k]); vb[8 + 2 * k] = bflo(xb1[k]); vb[8 + 2 * k + 1] = bfhi(xb1[k]);
    }
    float sa = 0.f, sb = 0.f;
#pragma unroll
    for (int k = 0; k < 16; ++k) { sa += va[k]; sb += vb[k]; }
    const float ma = wave_sum(sa) * (1.0f / D), mb = wave_sum(sb) * (1.0f / D);
    float qa = 0.f, qb = 0.f;
#pragma unroll
    for (int k = 0; k < 16; ++k) { const float da = va[k] - ma, db = vb[k] - mb; qa = fmaf(da, da, qa); qb = fmaf(db, db, qb); }
    const float ra = 1.0f / sqrtf(wave_sum(qa) * (1.0f / D) + 1e-5f), rb = 1.0f / sqrtf(wave_sum(qb) * (1.0f / D) + 1e-5f);
    x0[0] = (bflo(xr0.x) - ma) * ra; x0[1] = (bfhi(xr0.x) - ma) * ra; x0[2] = (bflo(xr0.y) - ma) * ra; x0[3] = (bfhi(xr0.y) - ma) * ra;
    x1[0] = (bflo(xr1.x) - mb) * rb; x1[1] = (bfhi(xr1.x) - mb) * rb; x1[2] = (bflo(xr1.y) - mb) * rb; x1[3] = (bfhi(xr1.y) - mb) * rb;
  }
  const u64 e1 = ((((u64)__builtin_amdgcn_readfirstlane(ev.y)) << 32) | (u64)(unsigned)__builtin_amdgcn_readfirstlane(ev.x)) + 1ull;
  const unsigned tag_hi = (unsigned)(e1 >> 8), tag_lo = (unsigned)(e1 & 0xFFull);
  const int rho = lane >> 4;
  const int redoff = ((rho & 1) * 64 + c8 * 8 + (rho >> 1) * 4) * 4;

  {
    const f32x4 r = gemv64(wqr, x0, x1);
    if ((lane & 8) == 0) *(LAS f32x4*)(L + L_RED + w * 512 + redoff) = r;
  }
#pragma unroll
  for (int i = 0; i < 4; ++i) wur[i] = ldnt((const GAS v4u*)(p.wu + (size_t)(krow + i) * FF + rgf * 64 + c8 * 8));
  SB();
  WG_BARRIER();
  if (tid < 128) {
    float s = 0.f;
#pragma unroll
    for (int ww = 0; ww < 8; ++ww) s += *(LAS float*)(L + L_RED + ww * 512 + tid * 4);
    const int b = tid >> 6, c = tid & 63;
    __builtin_amdgcn_raw_buffer_store_b64(granule(s, tag_lo, tag_hi), rs, WS_QG + (((hkv * 2 + b) * 4 + cg) * 1024 + (rg & 15) * 64 + c) * 8, 0, AUX_SC1);
  }
  STAMP(1);
  SB();

  v2u wdr[8], wor[8];
  {
    const f32x4 r = gemv64(wgr, x0, x1);
    if ((lane & 8) == 0) *(LAS f32x4*)(L + L_RED + 4096 + w * 512 + redoff) = r;
  }
#pragma unroll
  for (int k = 0; k < 8; ++k) wdr[k] = ldnt2((const GAS v2u*)(p.wd + (size_t)(rgf * 64 + w * 8 + k) * D + cg * 256 + lane * 4));
  SB();
  {
    const f32x4 r = gemv64(wur, x0, x1);
    if ((lane & 8) == 0) *(LAS f32x4*)(L + L_RED + 8192 + w * 512 + redoff) = r;
  }
#pragma unroll
  for (int k = 0; k < 8; ++k) wor[k] = ldnt2((const GAS v2u*)(p.wo + (size_t)(rg * 64 + w * 8 + k) * D + cg * 256 + lane * 4));
  SB();
  WG_BARRIER();
  if (tid < 256 && has_ffn) {
    float s = 0.f;
#pragma unroll
    for (int ww = 0; ww < 8; ++ww) s += *(LAS float*)(L + L_RED + 4096 + (tid >> 7) * 4096 + ww * 512 + (tid & 127) * 4);
    __builtin_amdgcn_raw_buffer_store_b64(granule(s, tag_lo, tag_hi), rs, WS_GU + (rg * 4 + cg) * 2048 + tid * 8, 0, AUX_SC1);
  }
  STAMP(2);
  SB();

  constexpr int NB = 3;
  const int i16 = lane & 15, g4 = lane >> 4;
  const size_t kvbase = ((size_t)(bb * 4 + hkv) * SK + (size_t)sp * 1024 + w * 16) * 128;
  const GAS v4u* kp = (const GAS v4u*)(p.kc + kvbase) + (4 * (i16 & 3) + (i16 >> 2)) * 16 + g4;
  const GAS v4u* vp = (const GAS v4u*)(p.vc + kvbase) + lane;
  v4u kb[NB][4], vb[NB][4];
  TOUCH8(wdr);
  LDK(0);
  TOUCH8(wor);
  LDV(0);
  {
    const int qoff = WS_QG + ((hkv * 2 + bb) * 4096 + w * 128 + lane * 2) * 8;
    const int goff = WS_GU + (rgf * 4 + (lane >> 4)) * 2048 + ((lane >> 2) & 3) * 512 + (w * 8 + (lane & 3) * 2) * 8;
    v4u qg[4], gu;
    TOUCH4(kb[0]);
#pragma unroll
    for (int ks = 0; ks < 4; ++ks) qg[ks] = __builtin_amdgcn_raw_buffer_load_b128(rs, qoff + ks * 8192, 0, AUX_SC1);
    gu = __builtin_amdgcn_raw_buffer_load_b128(rs, goff, 0, AUX_SC1);
    SB();
    LDK(1);
    TOUCH4(vb[0]);
    LDV(1);
    bool ok = tag2(qg[0], tag_lo, tag_hi) & tag2(qg[1], tag_lo, tag_hi) & tag2(qg[2], tag_lo, tag_hi) & tag2(qg[3], tag_lo, tag_hi) &
              (tag2(gu, tag_lo, tag_hi) | !has_ffn);
    if (!__all(ok)) {
      for (unsigned spins = 0; spins < SPIN_CAP; ++spins) {
#pragma unroll
        for (int ks = 0; ks < 4; ++ks) qg[ks] = __builtin_amdgcn_raw_buffer_load_b128(rs, qoff + ks * 8192, 0, AUX_SC1);
        gu = __builtin_amdgcn_raw_buffer_load_b128(rs, goff, 0, AUX_SC1);
        ok = tag2(qg[0], tag_lo, tag_hi) & tag2(qg[1], tag_lo, tag_hi) & tag2(qg[2], tag_lo, tag_hi) & tag2(qg[3], tag_lo, tag_hi) &
             (tag2(gu, tag_lo, tag_hi) | !has_ffn);
        if (__all(ok)) break;
        __builtin_amdgcn_s_sleep(2);
      }
    }
    float q0 = 0.f, q1 = 0.f;
#pragma unroll
    for (int ks = 0; ks < 4; ++ks) { q0 += u2f(qg[ks].x << 8); q1 += u2f(qg[ks].z << 8); }
    const unsigned hi = cvtpk(q0, q1);
    const unsigned lo = cvtpk(q0 - bflo(hi), q1 - bfhi(hi));
    *(LAS unsigned*)(L + L_Q + w * 272 + lane * 4) = hi;
    *(LAS unsigned*)(L + L_Q + (w + 8) * 272 + lane * 4) = lo;
    const float ga = x32_sum(x16_sum(u2f(gu.x << 8))), gb = x32_sum(x16_sum(u2f(gu.z << 8)));
    const float ha = silu(ga) * dpp<XOR8>(ga), hb = silu(gb) * dpp<XOR8>(gb);
    float h0[8], h1[8];
#pragma unroll
    for (int pr = 0; pr < 4; ++pr) {
      h0[2 * pr] = rdl(ha, pr); h0[2 * pr + 1] = rdl(hb, pr);
      h1[2 * pr] = rdl(ha, 4 + pr); h1[2 * pr + 1] = rdl(hb, 4 + pr);
    }
    f32x4 s0, s1;
    gemv256(wdr, h0, h1, s0, s1);
    *(LAS f32x4*)(L + L_F + w * 2048 + lane * 16) = s0;
    *(LAS f32x4*)(L + L_F + w * 2048 + 1024 + lane * 16) = s1;
  }
  WG_BARRIER();
  const int sfoff = ((tid >> 2) & 1) * 1024 + (tid >> 3) * 64 + (tid & 3) * 16;
  if (tid < 128 && has_ffn) {
    f32x4 s4 = {0.f, 0.f, 0.f, 0.f};
#pragma unroll
    for (int ww = 0; ww < 8; ++ww) s4 += *(LAS f32x4*)(L + L_F + ww * 2048 + sfoff);
    __builtin_amdgcn_raw_buffer_store_b128(clean4(s4), rs, WS_F2 + (cg * 64 + rg) * 2048 + tid * 16, 0, AUX_SC1);
  }
  v4u qf[4];
#pragma unroll
  for (int kk = 0; kk < 4; ++kk) qf[kk] = *(LAS v4u*)(L + L_Q + i16 * 272 + kk * 64 + g4 * 16);
  STAMP(3);

  constexpr float C2 = 0.088388347648318440f * 1.4426950408889634f;
  float m = -INFINITY, l = 0.f;
  f32x4 o[8];
#pragma unroll
  for (int e = 0; e < 8; ++e) o[e] = f32x4{0.f, 0.f, 0.f, 0.f};
#pragma unroll
  for (int u = 0; u < 8; ++u) {
    v4u(&kc)[4] = kb[u % NB];
    v4u(&vc)[4] = vb[u % NB];
    f32x4 s = {0.f, 0.f, 0.f, 0.f};
    TOUCH4(kc);
    if (u >= 1 && u + 1 < 8) LDK(u + 1);
    SB();
#pragma unroll
    for (int kk = 0; kk < 4; ++kk)
      s = __builtin_amdgcn_mfma_f32_16x16x32_bf16(__builtin_bit_cast(bf16x8, kc[kk]), __builtin_bit_cast(bf16x8, qf[kk]), s, 0, 0, 0);
#pragma unroll
    for (int r = 0; r < 4; ++r) s[r] = (s[r] + dpp<XOR8>(s[r])) * C2;
    float mx = fmaxf(fmaxf(s[0], s[1]), fmaxf(s[2], s[3]));
    mx = x32_max(x16_max(mx));
    const float mn = fmaxf(m, mx);
    const float alpha = __builtin_amdgcn_exp2f(m - mn);
    m = mn;
    const float p0 = __builtin_amdgcn_exp2f(s[0] - mn), p1 = __builtin_amdgcn_exp2f(s[1] - mn);
    const float p2 = __builtin_amdgcn_exp2f(s[2] - mn), p3 = __builtin_amdgcn_exp2f(s[3] - mn);
    l = fmaf(l, alpha, (p0 + p1) + (p2 + p3));
    const v2u pb = {cvtpk(p0, p1), cvtpk(p2, p3)};
#pragma unroll
    for (int e = 0; e < 8; ++e) o[e] *= alpha;
    SB();
    TOUCH4(vc);
    if (u >= 1 && u + 1 < 8) LDV(u + 1);
    SB();
#pragma unroll
    for (int e = 0; e < 8; ++e) {
      const unsigned sel = (e & 1) ? 0x07060302u : 0x05040100u;
      const v2u a = {__builtin_amdgcn_perm(vc[1][e >> 1], vc[0][e >> 1], sel), __builtin_amdgcn_perm(vc[3][e >> 1], vc[2][e >> 1], sel)};
      o[e] = __builtin_amdgcn_mfma_f32_16x16x16bf16_1k(__builtin_bit_cast(bf16x4, a), __builtin_bit_cast(bf16x4, pb), o[e], 0, 0, 0);
    }
    SB();
    if (u == 3) STAMP(4);
  }
  STAMP(5);

  l = x32_sum(x16_sum(l));
  if (i16 < 8) {
#pragma unroll
    for (int e = 0; e < 8; ++e) *(LAS f32x4*)(L + L_O + (w * 256 + (i16 * 4 + g4) * 8 + (e ^ i16)) * 16) = o[e];
    if (g4 == 0) *(LAS v2u*)(L + L_ML + (w * 8 + i16) * 8) = v2u{f2u(m), f2u(l)};
  }
  WG_BARRIER();
  if (tid < 256) {
    const int j = tid >> 5;
    float mw[8], lw[8];
#pragma unroll
    for (int ww = 0; ww < 8; ++ww) { const v2u t = *(LAS v2u*)(L + L_ML + (ww * 8 + j) * 8); mw[ww] = u2f(t.x); lw[ww] = u2f(t.y); }
    float M = mw[0];
#pragma unroll
    for (int ww = 1; ww < 8; ++ww) M = fmaxf(M, mw[ww]);
    float Ls = 0.f;
    f32x4 acc = {0.f, 0.f, 0.f, 0.f};
#pragma unroll
    for (int ww = 0; ww < 8; ++ww) {
      const float wt = __builtin_amdgcn_exp2f(mw[ww] - M);
      Ls = fmaf(wt, lw[ww], Ls);
      acc += *(LAS f32x4*)(L + L_O + (ww * 256 + (tid ^ j)) * 16) * wt;
    }
    acc *= 1.0f / Ls;
    const unsigned lse = f2u(M + __builtin_amdgcn_logf(Ls));
    v4u pk;
#pragma unroll
    for (int r = 0; r < 4; ++r) {
      const unsigned t = ((f2u(acc[r]) + 0x80u) & 0xFFFFFF00u) | ((lse >> (8 * r)) & 0xFFu);
      pk[r] = t == SENT ? 0x7FC000FFu : t;
    }
    __builtin_amdgcn_raw_buffer_store_b128(pk, rs, WS_P1 + bid * 4096 + tid * 16, 0, AUX_SC1);
  }
  STAMP(6);
  if (reducer) {
    if (w >= 4) {
      const int pr = (w - 4) * 16 + (lane & 15), pc = lane >> 4;
      const int off = WS_QG + ((((pr >> 4) * 2) * 4 + pc) * 1024 + (pr & 15) * 64) * 8;
      for (unsigned spins = 0; spins < SPIN_CAP; ++spins) {
        const v2u gq = __builtin_amdgcn_raw_buffer_load_b64(rs, off, 0, AUX_SC1);
        if (__all((gq.y == tag_hi) & ((gq.x >> 24) == tag_lo))) break;
        __builtin_amdgcn_s_sleep(2);
      }
    }
    WG_BARRIER();
    if (bid == NWG - 1 && tid == 0) {
      const v2u ne = {(unsigned)e1, (unsigned)(e1 >> 32)};
      __builtin_amdgcn_raw_buffer_store_b64(ne, rs, WS_E, 0, AUX_SC1);
    }
    reduce_lines(L + L_R + w * 128, rs, WS_F2 + (cg * 64 + (tid >> 3)) * 2048 + (rg - 48) * 128 + (tid & 7) * 16, tid < 56 * 8, lane);
  }

  {
    const int poff = WS_P1 + (hkv * 64 + w * 8 + g4) * 4096 + (rg & 15) * 256 + i16 * 16;
    v4u pa = __builtin_amdgcn_raw_buffer_load_b128(rs, poff, 0, AUX_SC1);
    v4u pb = __builtin_amdgcn_raw_buffer_load_b128(rs, poff + 4 * 4096, 0, AUX_SC1);
    bool ha = full4(pa), hb = full4(pb);
    for (unsigned spins = 0; spins < SPIN_CAP; ++spins) {
      if (__all(ha & hb)) break;
      __builtin_amdgcn_s_sleep(1);
      if (!ha) pa = __builtin_amdgcn_raw_buffer_load_b128(rs, poff, 0, AUX_SC1);
      if (!hb) pb = __builtin_amdgcn_raw_buffer_load_b128(rs, poff + 4 * 4096, 0, AUX_SC1);
      ha = full4(pa);
      hb = full4(pb);
    }
    const float la = u2f(__builtin_amdgcn_perm(__builtin_amdgcn_perm(pa.w, pa.z, 0x0c0c0400u), __builtin_amdgcn_perm(pa.y, pa.x, 0x0c0c0400u), 0x05040100u));
    const float lb = u2f(__builtin_amdgcn_perm(__builtin_amdgcn_perm(pb.w, pb.z, 0x0c0c0400u), __builtin_amdgcn_perm(pb.y, pb.x, 0x0c0c0400u), 0x05040100u));
    const float Mw = x32_max(x16_max(fmaxf(la, lb)));
    const float wa = __builtin_amdgcn_exp2f(la - Mw), wb = __builtin_amdgcn_exp2f(lb - Mw);
    const float Ww = x32_sum(x16_sum(wa + wb));
    float a[4];
#pragma unroll
    for (int r = 0; r < 4; ++r) a[r] = fmaf(u2f(pa[r] & 0xFFFFFF00u), wa, u2f(pb[r] & 0xFFFFFF00u) * wb);
    const float v = tr32(tr16(a[0], a[1]), tr16(a[2], a[3]));
    *(LAS float*)(L + L_C + (w * 64 + (i16 >> 3) * 32 + g4 * 8 + (i16 & 7)) * 4) = v;
    if (lane == 0) *(LAS v2u*)(L + L_C + 2048 + w * 8) = v2u{f2u(Mw), f2u(Ww)};
  }
  STAMP(7);
  WG_BARRIER();
  {
    const int b = (lane >> 3) & 1, k = lane & 7;
    float mv[4], wv[4], av[4];
#pragma unroll
    for (int t = 0; t < 4; ++t) {
      const v2u mw = *(LAS v2u*)(L + L_C + 2048 + (b * 4 + t) * 8);
      mv[t] = u2f(mw.x); wv[t] = u2f(mw.y);
      av[t] = *(LAS float*)(L + L_C + ((b * 4 + t) * 64 + w * 8 + k) * 4);
    }
    const float M = fmaxf(fmaxf(mv[0], mv[1]), fmaxf(mv[2], mv[3]));
    float num = 0.f, den = 0.f;
#pragma unroll
    for (int t = 0; t < 4; ++t) { const float f = __builtin_amdgcn_exp2f(mv[t] - M); num = fmaf(f, av[t], num); den = fmaf(f, wv[t], den); }
    const float ov = num / den;
    float c0[8], c1[8];
#pragma unroll
    for (int kk = 0; kk < 8; ++kk) { c0[kk] = rdl(ov, kk); c1[kk] = rdl(ov, 8 + kk); }
    f32x4 s0, s1;
    gemv256(wor, c0, c1, s0, s1);
    *(LAS f32x4*)(L + L_S + w * 2048 + lane * 16) = s0;
    *(LAS f32x4*)(L + L_S + w * 2048 + 1024 + lane * 16) = s1;
  }
  WG_BARRIER();
  if (tid < 128) {
    f32x4 s4 = {0.f, 0.f, 0.f, 0.f};
#pragma unroll
    for (int ww = 0; ww < 8; ++ww) s4 += *(LAS f32x4*)(L + L_S + ww * 2048 + sfoff);
    __builtin_amdgcn_raw_buffer_store_b128(clean4(s4), rs, WS_S2 + (cg * 64 + rg) * 2048 + tid * 16, 0, AUX_SC1);
  }
  STAMP(8);

  if (reducer) {
    const int oidx = ((tid >> 4) & 1) * D + cg * 256 + (rg - 48) * 16 + (tid & 15);
    unsigned short xo = 0;
    if (tid < 32) xo = p.x[oidx];
    reduce_lines(L + L_R + 1024 + w * 128, rs, WS_S2 + (cg * 64 + (tid >> 3)) * 2048 + (rg - 48) * 128 + (tid & 7) * 16, true, lane);
    WG_BARRIER();
    if (tid < 32) {
      float f = 0.f, s = 0.f;
#pragma unroll
      for (int ww = 0; ww < 8; ++ww) { f += *(LAS float*)(L + L_R + ww * 128 + tid * 4); s += *(LAS float*)(L + L_R + 1024 + ww * 128 + tid * 4); }
      const float r = fmaf(s, silu(f), bflo(xo));
      p.out[oidx] = (unsigned short)(cvtpk(r, 0.f) & 0xFFFFu);
    }
  }
  STAMP(9);
}

}

extern "C" void kernel_launch(void* const* d_in, const int* in_sizes, int n_in, void* d_out, int out_size, void* d_ws,
                              size_t ws_size, hipStream_t stream) {
  Params p;
  p.x = (const unsigned short*)d_in[0];
  p.kc = (const unsigned short*)d_in[1];
  p.vc = (const unsigned short*)d_in[2];
  p.wq = (const unsigned short*)d_in[3];
  p.wo = (const unsigned short*)d_in[4];
  p.wg = (const unsigned short*)d_in[6];
  p.wu = (const unsigned short*)d_in[7];
  p.wd = (const unsigned short*)d_in[8];
  p.out = (unsigned short*)d_out;
  p.ws = (unsigned char*)d_ws;
  hipLaunchKernelGGL(fused_decode, dim3(NWG), dim3(NTHR), 0, stream, p);
}
```

```cpp
#define DIAG 0
#include <hip/hip_runtime.h>
#include <hip/hip_bf16.h>
#include <stdint.h>

#define GAS __attribute__((address_space(1)))
#define LAS __attribute__((address_space(3)))
typedef unsigned v4u __attribute__((ext_vector_type(4)));
typedef unsigned v2u __attribute__((ext_vector_type(2)));
typedef float f32x4 __attribute__((ext_vector_type(4)));
typedef short bf16x8 __attribute__((ext_vector_type(8)));
typedef short bf16x4 __attribute__((ext_vector_type(4)));
typedef unsigned long long u64;

namespace {

constexpr int NWG = 256, NTHR = 512;
constexpr int D = 1024, HD = 4096, FF = 3584, SK = 32768;
constexpr int AUX_SC1 = 16;
constexpr unsigned SENT = 0xFFFFFFFFu;
constexpr unsigned SPIN_CAP = 1u << 22;

constexpr int WS_E = 0;
constexpr int WS_QG = 4096;
constexpr int WS_GU = WS_QG + 262144;
constexpr int WS_P1 = WS_GU + 524288;
constexpr int WS_S2 = WS_P1 + 1048576;
constexpr int WS_F2 = WS_S2 + 524288;
constexpr int WS_END = WS_F2 + 524288;
constexpr int WS_ALL = WS_END;

constexpr int L_RED = 0;
constexpr int L_Q = 12288;
constexpr int L_ML = L_Q + 16 * 272;
constexpr int L_O = L_ML + 512;
constexpr int L_S = L_O + 32768;
constexpr int L_F = L_S + 16384;
constexpr int L_C = L_F + 16384;
constexpr int L_R = L_C + 2048 + 64;
constexpr int L_USED = L_R + 2048;
constexpr int L_BYTES = 88 * 1024;
static_assert(L_USED <= L_BYTES, "lds");

struct Params {
  const unsigned short* x;
  const unsigned short* kc;
  const unsigned short* vc;
  const unsigned short* wq;
  const unsigned short* wo;
  const unsigned short* wg;
  const unsigned short* wu;
  const unsigned short* wd;
  unsigned short* out;
  unsigned char* ws;
};

__device__ __forceinline__ float bflo(unsigned u) { return __builtin_bit_cast(float, u << 16); }
__device__ __forceinline__ float bfhi(unsigned u) { return __builtin_bit_cast(float, u & 0xFFFF0000u); }
__device__ __forceinline__ unsigned f2u(float f) { return __builtin_bit_cast(unsigned, f); }
__device__ __forceinline__ float u2f(unsigned u) { return __builtin_bit_cast(float, u); }
typedef __bf16 bf16p __attribute__((ext_vector_type(2)));
typedef float f32x2 __attribute__((ext_vector_type(2)));
__device__ __forceinline__ unsigned cvtpk(float lo, float hi) {
  return __builtin_bit_cast(unsigned, __builtin_convertvector(f32x2{lo, hi}, bf16p));
}
template <int CTRL> __device__ __forceinline__ float dpp(float x) {
  return __builtin_bit_cast(float, __builtin_amdgcn_mov_dpp(__builtin_bit_cast(int, x), CTRL, 0xf, 0xf, true));
}
constexpr int XOR1 = 0xB1, XOR2 = 0x4E, XOR7 = 0x141, XOR8 = 0x128, ROR4 = 0x124, XOR15 = 0x140;
__device__ __forceinline__ float x16_sum(float x) {
  auto s = __builtin_amdgcn_permlane16_swap(f2u(x), f2u(x), false, false);
  return u2f(s[0]) + u2f(s[1]);
}
__device__ __forceinline__ float x32_sum(float x) {
  auto s = __builtin_amdgcn_permlane32_swap(f2u(x), f2u(x), false, false);
  return u2f(s[0]) + u2f(s[1]);
}
__device__ __forceinline__ float x16_max(float x) {
  auto s = __builtin_amdgcn_permlane16_swap(f2u(x), f2u(x), false, false);
  return fmaxf(u2f(s[0]), u2f(s[1]));
}
__device__ __forceinline__ float x32_max(float x) {
  auto s = __builtin_amdgcn_permlane32_swap(f2u(x), f2u(x), false, false);
  return fmaxf(u2f(s[0]), u2f(s[1]));
}
__device__ __forceinline__ float row16_sum(float x) {
  x += dpp<XOR1>(x);
  x += dpp<XOR2>(x);
  x += dpp<XOR7>(x);
  x += dpp<XOR8>(x);
  return x;
}
__device__ __forceinline__ float row16_max(float x) {
  x = fmaxf(x, dpp<XOR1>(x));
  x = fmaxf(x, dpp<XOR2>(x));
  x = fmaxf(x, dpp<XOR7>(x));
  x = fmaxf(x, dpp<XOR8>(x));
  return x;
}
__device__ __forceinline__ float wave_sum(float x) { return x32_sum(x16_sum(row16_sum(x))); }
__device__ __forceinline__ float tr16(float a, float b) {
  auto s = __builtin_amdgcn_permlane16_swap(f2u(a), f2u(b), false, false);
  return u2f(s[0]) + u2f(s[1]);
}
__device__ __forceinline__ float tr32(float a, float b) {
  auto s = __builtin_amdgcn_permlane32_swap(f2u(a), f2u(b), false, false);
  return u2f(s[0]) + u2f(s[1]);
}
__device__ __forceinline__ v4u ldnt(const GAS v4u* p) { return __builtin_nontemporal_load(p); }
__device__ __forceinline__ v2u ldnt2(const GAS v2u* p) { return __builtin_nontemporal_load(p); }
__device__ __forceinline__ unsigned clean(float f) {
  const unsigned u = f2u(f);
  return u == SENT ? 0x7FC00000u : u;
}
__device__ __forceinline__ v4u clean4(f32x4 v) { return v4u{clean(v.x), clean(v.y), clean(v.z), clean(v.w)}; }
__device__ __forceinline__ bool full4(v4u v) { return v.x != SENT && v.y != SENT && v.z != SENT && v.w != SENT; }
__device__ __forceinline__ float rdl(float v, int lane) { return u2f(__builtin_amdgcn_readlane(f2u(v), lane)); }
__device__ __forceinline__ float silu(float v) { return v / (1.0f + __expf(-v)); }

#define SB() __builtin_amdgcn_sched_barrier(0)
#define WG_BARRIER()                                        \
  do {                                                      \
    asm volatile("s_waitcnt lgkmcnt(0)" ::: "memory");      \
    __builtin_amdgcn_s_barrier();                           \
    asm volatile("" ::: "memory");                          \
  } while (0)
#define STAMP(i) do { if ((i) == 0) tk0 = __builtin_amdgcn_s_memrealtime(); if ((i) == DIAG) tk1 = __builtin_amdgcn_s_memrealtime(); } while (0)

__device__ __forceinline__ f32x4 gemv64(const v4u (&w)[4], const float (&x0)[4], const float (&x1)[4]) {
  float a0[8], a1[8];
#pragma unroll
  for (int c = 0; c < 8; ++c) { a0[c] = 0.f; a1[c] = 0.f; }
#pragma unroll
  for (int i = 0; i < 4; ++i) {
#pragma unroll
    for (int k = 0; k < 4; ++k) {
      const float lo = bflo(w[i][k]), hi = bfhi(w[i][k]);
      a0[2 * k] = fmaf(lo, x0[i], a0[2 * k]);
      a0[2 * k + 1] = fmaf(hi, x0[i], a0[2 * k + 1]);
      a1[2 * k] = fmaf(lo, x1[i], a1[2 * k]);
      a1[2 * k + 1] = fmaf(hi, x1[i], a1[2 * k + 1]);
    }
  }
  float t[8];
#pragma unroll
  for (int c = 0; c < 8; ++c) t[c] = tr16(a0[c], a1[c]);
  f32x4 r;
#pragma unroll
  for (int c = 0; c < 4; ++c) {
    float v = tr32(t[c], t[c + 4]);
    v += dpp<XOR8>(v);
    r[c] = v;
  }
  return r;
}

__device__ __forceinline__ void gemv256(const v2u (&w)[8], const float (&c0)[8], const float (&c1)[8], f32x4& s0, f32x4& s1) {
  s0 = f32x4{0.f, 0.f, 0.f, 0.f};
  s1 = f32x4{0.f, 0.f, 0.f, 0.f};
#pragma unroll
  for (int k = 0; k < 8; ++k) {
    const f32x4 wf = {bflo(w[k].x), bfhi(w[k].x), bflo(w[k].y), bfhi(w[k].y)};
#pragma unroll
    for (int c = 0; c < 4; ++c) {
      s0[c] = fmaf(wf[c], c0[k], s0[c]);
      s1[c] = fmaf(wf[c], c1[k], s1[c]);
    }
  }
}

__device__ __forceinline__ v2u granule(float v, unsigned tag_lo, unsigned tag_hi) {
  return v2u{(tag_lo << 24) | ((f2u(v) + 0x80u) >> 8), tag_hi};
}
__device__ __forceinline__ bool tag2(v4u g, unsigned tag_lo, unsigned tag_hi) {
  return (g.y == tag_hi) & ((g.x >> 24) == tag_lo) & (g.w == tag_hi) & ((g.z >> 24) == tag_lo);
}
#define TOUCH4(a) asm volatile("" : "+v"(a[0]), "+v"(a[1]), "+v"(a[2]), "+v"(a[3]))
#define TOUCH8(a) asm volatile("" : "+v"(a[0]), "+v"(a[1]), "+v"(a[2]), "+v"(a[3]), "+v"(a[4]), "+v"(a[5]), "+v"(a[6]), "+v"(a[7]))
#define LDK(u_) do { _Pragma("unroll") for (int j = 0; j < 4; ++j) kb[(u_) % NB][j] = ldnt(kp + (u_) * 2048 + j * 4); SB(); } while (0)
#define LDV(u_) do { _Pragma("unroll") for (int j = 0; j < 4; ++j) vb[(u_) % NB][j] = ldnt(vp + (u_) * 2048 + j * 64); SB(); } while (0)

__device__ __forceinline__ void reduce_lines(LAS unsigned char* dst, __amdgpu_buffer_rsrc_t rs, int off, bool live, int lane) {
  v4u a = __builtin_amdgcn_raw_buffer_load_b128(rs, off, 0, AUX_SC1);
  bool hd = !live | full4(a);
  for (unsigned spins = 0; spins < SPIN_CAP; ++spins) {
    if (__all(hd)) break;
    __builtin_amdgcn_s_sleep(1);
    if (!hd) a = __builtin_amdgcn_raw_buffer_load_b128(rs, off, 0, AUX_SC1);
    hd = !live | full4(a);
  }
  const float a0 = live ? u2f(a.x) : 0.f, a1 = live ? u2f(a.y) : 0.f, a2 = live ? u2f(a.z) : 0.f, a3 = live ? u2f(a.w) : 0.f;
  float v = tr32(tr16(a0, a1), tr16(a2, a3));
  v += dpp<XOR8>(v);
  if ((lane & 8) == 0) *(LAS float*)(dst + ((lane & 7) * 4 + (lane >> 4)) * 4) = v;
}

__global__ void __launch_bounds__(NTHR, 2) fused_decode(Params p) {
  __shared__ __attribute__((aligned(16))) unsigned char smem[L_BYTES];
  LAS unsigned char* L = (LAS unsigned char*)smem;
  const int tid = threadIdx.x, lane = tid & 63, w = __builtin_amdgcn_readfirstlane(tid >> 6);
  const int bid = blockIdx.x;
  const int cg = (bid >> 3) & 3, rg = (bid >> 5) * 8 + (bid & 7);
  const int hkv = bid >> 6, bb = (bid >> 5) & 1, sp = bid & 31;
  const bool has_ffn = rg < 56;
  const bool reducer = rg >= 48;
  const __amdgpu_buffer_rsrc_t rs = __builtin_amdgcn_make_buffer_rsrc((void*)p.ws, 0, WS_ALL, 0x00020000);
  u64 tk0 = 0, tk1 = 0;
  STAMP(0);

  {
    const v4u s4 = {SENT, SENT, SENT, SENT};
    const int off = tid < 256 ? WS_P1 + bid * 4096 + tid * 16
                              : (tid < 384 ? WS_S2 - 4096 : WS_F2 - 6144) + (cg * 64 + rg) * 2048 + tid * 16;
    __builtin_amdgcn_raw_buffer_store_b128(s4, rs, off, 0, AUX_SC1);
  }
  SB();
  const v2u ev = __builtin_amdgcn_raw_buffer_load_b64(rs, WS_E, 0, AUX_SC1);
  const GAS v4u* xg = (const GAS v4u*)p.x;
  const v4u xa0 = xg[lane * 2], xa1 = xg[lane * 2 + 1], xb0 = xg[128 + lane * 2], xb1 = xg[128 + lane * 2 + 1];
  const int r8 = lane >> 3, c8 = lane & 7;
  const int krow = cg * 256 + w * 32 + r8 * 4;
  const v2u xr0 = *(const GAS v2u*)(p.x + krow), xr1 = *(const GAS v2u*)(p.x + D + krow);
  v4u wqr[4], wgr[4], wur[4];
#pragma unroll
  for (int i = 0; i < 4; ++i) wqr[i] = ldnt((const GAS v4u*)(p.wq + (size_t)(krow + i) * HD + rg * 64 + c8 * 8));
  const int rgf = has_ffn ? rg : 0;
#pragma unroll
  for (int i = 0; i < 4; ++i) wgr[i] = ldnt((const GAS v4u*)(p.wg + (size_t)(krow + i) * FF + rgf * 64 + c8 * 8));
  SB();

  float x0[4], x1[4];
  {
    float va[16], vb[16];
#pragma unroll
    for (int k = 0; k < 4; ++k) {
      va[2 * k] = bflo(xa0[k]); va[2 * k + 1] = bfhi(xa0[k]); va[8 + 2 * k] = bflo(xa1[k]); va[8 + 2 * k + 1] = bfhi(xa1[k]);
      vb[2 * k] = bflo(xb0[k]); vb[2 * k + 1] = bfhi(xb0[k]); vb[8 + 2 * k] = bflo(xb1[k]); vb[8 + 2 * k + 1] = bfhi(xb1[k]);
    }
    float sa = 0.f, sb = 0.f;
#pragma unroll
    for (int k = 0; k < 16; ++k) { sa += va[k]; sb += vb[k]; }
    const float ma = wave_sum(sa) * (1.0f / D), mb = wave_sum(sb) * (1.0f / D);
    float qa = 0.f, qb = 0.f;
#pragma unroll
    for (int k = 0; k < 16; ++k) { const float da = va[k] - ma, db = vb[k] - mb; qa = fmaf(da, da, qa); qb = fmaf(db, db, qb); }
    const float ra = 1.0f / sqrtf(wave_sum(qa) * (1.0f / D) + 1e-5f), rb = 1.0f / sqrtf(wave_sum(qb) * (1.0f / D) + 1e-5f);
    x0[0] = (bflo(xr0.x) - ma) * ra; x0[1] = (bfhi(xr0.x) - ma) * ra; x0[2] = (bflo(xr0.y) - ma) * ra; x0[3] = (bfhi(xr0.y) - ma) * ra;
    x1[0] = (bflo(xr1.x) - mb) * rb; x1[1] = (bfhi(xr1.x) - mb) * rb; x1[2] = (bflo(xr1.y) - mb) * rb; x1[3] = (bfhi(xr1.y) - mb) * rb;
  }
  const u64 e1 = ((((u64)__builtin_amdgcn_readfirstlane(ev.y)) << 32) | (u64)(unsigned)__builtin_amdgcn_readfirstlane(ev.x)) + 1ull;
  const unsigned tag_hi = (unsigned)(e1 >> 8), tag_lo = (unsigned)(e1 & 0xFFull);
  const int rho = lane >> 4;
  const int redoff = ((rho & 1) * 64 + c8 * 8 + (rho >> 1) * 4) * 4;

  {
    const f32x4 r = gemv64(wqr, x0, x1);
    if ((lane & 8) == 0) *(LAS f32x4*)(L + L_RED + w * 512 + redoff) = r;
  }
#pragma unroll
  for (int i = 0; i < 4; ++i) wur[i] = ldnt((const GAS v4u*)(p.wu + (size_t)(krow + i) * FF + rgf * 64 + c8 * 8));
  SB();
  WG_BARRIER();
  if (tid < 128) {
    float s = 0.f;
#pragma unroll
    for (int ww = 0; ww < 8; ++ww) s += *(LAS float*)(L + L_RED + ww * 512 + tid * 4);
    const int b = tid >> 6, c = tid & 63;
    __builtin_amdgcn_raw_buffer_store_b64(granule(s, tag_lo, tag_hi), rs, WS_QG + (((hkv * 2 + b) * 4 + cg) * 1024 + (rg & 15) * 64 + c) * 8, 0, AUX_SC1);
  }
  STAMP(1);
  SB();

  v2u wdr[8], wor[8];
  {
    const f32x4 r = gemv64(wgr, x0, x1);
    if ((lane & 8) == 0) *(LAS f32x4*)(L + L_RED + 4096 + w * 512 + redoff) = r;
  }
#pragma unroll
  for (int k = 0; k < 8; ++k) wdr[k] = ldnt2((const GAS v2u*)(p.wd + (size_t)(rgf * 64 + w * 8 + k) * D + cg * 256 + lane * 4));
  SB();
  {
    const f32x4 r = gemv64(wur, x0, x1);
    if ((lane & 8) == 0) *(LAS f32x4*)(L + L_RED + 8192 + w * 512 + redoff) = r;
  }
#pragma unroll
  for (int k = 0; k < 8; ++k) wor[k] = ldnt2((const GAS v2u*)(p.wo + (size_t)(rg * 64 + w * 8 + k) * D + cg * 256 + lane * 4));
  SB();
  WG_BARRIER();
  if (tid < 256 && has_ffn) {
    float s = 0.f;
#pragma unroll
    for (int ww = 0; ww < 8; ++ww) s += *(LAS float*)(L + L_RED + 4096 + (tid >> 7) * 4096 + ww * 512 + (tid & 127) * 4);
    __builtin_amdgcn_raw_buffer_store_b64(granule(s, tag_lo, tag_hi), rs, WS_GU + (rg * 4 + cg) * 2048 + tid * 8, 0, AUX_SC1);
  }
  STAMP(2);
  SB();

  constexpr int NB = 3;
  const int i16 = lane & 15, g4 = lane >> 4;
  const size_t kvbase = ((size_t)(bb * 4 + hkv) * SK + (size_t)sp * 1024 + w * 16) * 128;
  const GAS v4u* kp = (const GAS v4u*)(p.kc + kvbase) + (4 * (i16 & 3) + (i16 >> 2)) * 16 + g4;
  const GAS v4u* vp = (const GAS v4u*)(p.vc + kvbase) + lane;
  v4u kb[NB][4], vb[NB][4];
  TOUCH8(wdr);
  LDK(0);
  TOUCH8(wor);
  LDV(0);
  {
    const int qoff = WS_QG + ((hkv * 2 + bb) * 4096 + w * 128 + lane * 2) * 8;
    const int goff = WS_GU + (rgf * 4 + (lane >> 4)) * 2048 + ((lane >> 2) & 3) * 512 + (w * 8 + (lane & 3) * 2) * 8;
    v4u qg[4], gu;
    TOUCH4(kb[0]);
#pragma unroll
    for (int ks = 0; ks < 4; ++ks) qg[ks] = __builtin_amdgcn_raw_buffer_load_b128(rs, qoff + ks * 8192, 0, AUX_SC1);
    gu = __builtin_amdgcn_raw_buffer_load_b128(rs, goff, 0, AUX_SC1);
    SB();
    LDK(1);
    TOUCH4(vb[0]);
    LDV(1);
    bool ok = tag2(qg[0], tag_lo, tag_hi) & tag2(qg[1], tag_lo, tag_hi) & tag2(qg[2], tag_lo, tag_hi) & tag2(qg[3], tag_lo, tag_hi) &
              (tag2(gu, tag_lo, tag_hi) | !has_ffn);
    if (!__all(ok)) {
      for (unsigned spins = 0; spins < SPIN_CAP; ++spins) {
#pragma unroll
        for (int ks = 0; ks < 4; ++ks) qg[ks] = __builtin_amdgcn_raw_buffer_load_b128(rs, qoff + ks * 8192, 0, AUX_SC1);
        gu = __builtin_amdgcn_raw_buffer_load_b128(rs, goff, 0, AUX_SC1);
        ok = tag2(qg[0], tag_lo, tag_hi) & tag2(qg[1], tag_lo, tag_hi) & tag2(qg[2], tag_lo, tag_hi) & tag2(qg[3], tag_lo, tag_hi) &
             (tag2(gu, tag_lo, tag_hi) | !has_ffn);
        if (__all(ok)) break;
        __builtin_amdgcn_s_sleep(2);
      }
    }
    float q0 = 0.f, q1 = 0.f;
#pragma unroll
    for (int ks = 0; ks < 4; ++ks) { q0 += u2f(qg[ks].x << 8); q1 += u2f(qg[ks].z << 8); }
    const unsigned hi = cvtpk(q0, q1);
    const unsigned lo = cvtpk(q0 - bflo(hi), q1 - bfhi(hi));
    *(LAS unsigned*)(L + L_Q + w * 272 + lane * 4) = hi;
    *(LAS unsigned*)(L + L_Q + (w + 8) * 272 + lane * 4) = lo;
    const float ga = x32_sum(x16_sum(u2f(gu.x << 8))), gb = x32_sum(x16_sum(u2f(gu.z << 8)));
    const float ha = silu(ga) * dpp<XOR8>(ga), hb = silu(gb) * dpp<XOR8>(gb);
    float h0[8], h1[8];
#pragma unroll
    for (int pr = 0; pr < 4; ++pr) {
      h0[2 * pr] = rdl(ha, pr); h0[2 * pr + 1] = rdl(hb, pr);
      h1[2 * pr] = rdl(ha, 4 + pr); h1[2 * pr + 1] = rdl(hb, 4 + pr);
    }
    f32x4 s0, s1;
    gemv256(wdr, h0, h1, s0, s1);
    *(LAS f32x4*)(L + L_F + w * 2048 + lane * 16) = s0;
    *(LAS f32x4*)(L + L_F + w * 2048 + 1024 + lane * 16) = s1;
  }
  WG_BARRIER();
  const int sfoff = ((tid >> 2) & 1) * 1024 + (tid >> 3) * 64 + (tid & 3) * 16;
  if (tid < 128 && has_ffn) {
    f32x4 s4 = {0.f, 0.f, 0.f, 0.f};
#pragma unroll
    for (int ww = 0; ww < 8; ++ww) s4 += *(LAS f32x4*)(L + L_F + ww * 2048 + sfoff);
    __builtin_amdgcn_raw_buffer_store_b128(clean4(s4), rs, WS_F2 + (cg * 64 + rg) * 2048 + tid * 16, 0, AUX_SC1);
  }
  v4u qf[4];
#pragma unroll
  for (int kk = 0; kk < 4; ++kk) qf[kk] = *(LAS v4u*)(L + L_Q + i16 * 272 + kk * 64 + g4 * 16);
  STAMP(3);

  constexpr float C2 = 0.088388347648318440f * 1.4426950408889634f;
  float m = -INFINITY, l = 0.f;
  f32x4 o[8];
#pragma unroll
  for (int e = 0; e < 8; ++e) o[e] = f32x4{0.f, 0.f, 0.f, 0.f};
#pragma unroll
  for (int u = 0; u < 8; ++u) {
    v4u(&kc)[4] = kb[u % NB];
    v4u(&vc)[4] = vb[u % NB];
    f32x4 s = {0.f, 0.f, 0.f, 0.f};
    TOUCH4(kc);
    if (u >= 1 && u + 1 < 8) LDK(u + 1);
    SB();
#pragma unroll
    for (int kk = 0; kk < 4; ++kk)
      s = __builtin_amdgcn_mfma_f32_16x16x32_bf16(__builtin_bit_cast(bf16x8, kc[kk]), __builtin_bit_cast(bf16x8, qf[kk]), s, 0, 0, 0);
#pragma unroll
    for (int r = 0; r < 4; ++r) s[r] = (s[r] + dpp<XOR8>(s[r])) * C2;
    float mx = fmaxf(fmaxf(s[0], s[1]), fmaxf(s[2], s[3]));
    mx = x32_max(x16_max(mx));
    const float mn = fmaxf(m, mx);
    const float alpha = __builtin_amdgcn_exp2f(m - mn);
    m = mn;
    const float p0 = __builtin_amdgcn_exp2f(s[0] - mn), p1 = __builtin_amdgcn_exp2f(s[1] - mn);
    const float p2 = __builtin_amdgcn_exp2f(s[2] - mn), p3 = __builtin_amdgcn_exp2f(s[3] - mn);
    l = fmaf(l, alpha, (p0 + p1) + (p2 + p3));
    const v2u pb = {cvtpk(p0, p1), cvtpk(p2, p3)};
#pragma unroll
    for (int e = 0; e < 8; ++e) o[e] *= alpha;
    SB();
    TOUCH4(vc);
    if (u >= 1 && u + 1 < 8) LDV(u + 1);
    SB();
#pragma unroll
    for (int e = 0; e < 8; ++e) {
      const unsigned sel = (e & 1) ? 0x07060302u : 0x05040100u;
      const v2u a = {__builtin_amdgcn_perm(vc[1][e >> 1], vc[0][e >> 1], sel), __builtin_amdgcn_perm(vc[3][e >> 1], vc[2][e >> 1], sel)};
      o[e] = __builtin_amdgcn_mfma_f32_16x16x16bf16_1k(__builtin_bit_cast(bf16x4, a), __builtin_bit_cast(bf16x4, pb), o[e], 0, 0, 0);
    }
    SB();
    if (u == 3) STAMP(4);
  }
  STAMP(5);

  l = x32_sum(x16_sum(l));
  if (i16 < 8) {
#pragma unroll
    for (int e = 0; e < 8; ++e) *(LAS f32x4*)(L + L_O + (w * 256 + (i16 * 4 + g4) * 8 + (e ^ i16)) * 16) = o[e];
    if (g4 == 0) *(LAS v2u*)(L + L_ML + (w * 8 + i16) * 8) = v2u{f2u(m), f2u(l)};
  }
  WG_BARRIER();
  if (tid < 256) {
    const int j = tid >> 5;
    float mw[8], lw[8];
#pragma unroll
    for (int ww = 0; ww < 8; ++ww) { const v2u t = *(LAS v2u*)(L + L_ML + (ww * 8 + j) * 8); mw[ww] = u2f(t.x); lw[ww] = u2f(t.y); }
    float M = mw[0];
#pragma unroll
    for (int ww = 1; ww < 8; ++ww) M = fmaxf(M, mw[ww]);
    float Ls = 0.f;
    f32x4 acc = {0.f, 0.f, 0.f, 0.f};
#pragma unroll
    for (int ww = 0; ww < 8; ++ww) {
      const float wt = __builtin_amdgcn_exp2f(mw[ww] - M);
      Ls = fmaf(wt, lw[ww], Ls);
      acc += *(LAS f32x4*)(L + L_O + (ww * 256 + (tid ^ j)) * 16) * wt;
    }
    acc *= 1.0f / Ls;
    const unsigned lse = f2u(M + __builtin_amdgcn_logf(Ls));
    v4u pk;
#pragma unroll
    for (int r = 0; r < 4; ++r) {
      const unsigned t = ((f2u(acc[r]) + 0x80u) & 0xFFFFFF00u) | ((lse >> (8 * r)) & 0xFFu);
      pk[r] = t == SENT ? 0x7FC000FFu : t;
    }
    __builtin_amdgcn_raw_buffer_store_b128(pk, rs, WS_P1 + bid * 4096 + tid * 16, 0, AUX_SC1);
  }
  STAMP(6);
  if (reducer) {
    if (w >= 4) {
      const int pr = (w - 4) * 16 + (lane & 15), pc = lane >> 4;
      const int off = WS_QG + ((((pr >> 4) * 2) * 4 + pc) * 1024 + (pr & 15) * 64) * 8;
      for (unsigned spins = 0; spins < SPIN_CAP; ++spins) {
        const v2u gq = __builtin_amdgcn_raw_buffer_load_b64(rs, off, 0, AUX_SC1);
        if (__all((gq.y == tag_hi) & ((gq.x >> 24) == tag_lo))) break;
        __builtin_amdgcn_s_sleep(2);
      }
    }
    WG_BARRIER();
    if (bid == NWG - 1 && tid == 0) {
      const v2u ne = {(unsigned)e1, (unsigned)(e1 >> 32)};
      __builtin_amdgcn_raw_buffer_store_b64(ne, rs, WS_E, 0, AUX_SC1);
    }
    reduce_lines(L + L_R + w * 128, rs, WS_F2 + (cg * 64 + (tid >> 3)) * 2048 + (rg - 48) * 128 + (tid & 7) * 16, tid < 56 * 8, lane);
  }

  {
    const int poff = WS_P1 + (hkv * 64 + w * 8 + g4) * 4096 + (rg & 15) * 256 + i16 * 16;
    v4u pa = __builtin_amdgcn_raw_buffer_load_b128(rs, poff, 0, AUX_SC1);
    v4u pb = __builtin_amdgcn_raw_buffer_load_b128(rs, poff + 4 * 4096, 0, AUX_SC1);
    bool ha = full4(pa), hb = full4(pb);
    for (unsigned spins = 0; spins < SPIN_CAP; ++spins) {
      if (__all(ha & hb)) break;
      __builtin_amdgcn_s_sleep(1);
      if (!ha) pa = __builtin_amdgcn_raw_buffer_load_b128(rs, poff, 0, AUX_SC1);
      if (!hb) pb = __builtin_amdgcn_raw_buffer_load_b128(rs, poff + 4 * 4096, 0, AUX_SC1);
      ha = full4(pa);
      hb = full4(pb);
    }
    const float la = u2f(__builtin_amdgcn_perm(__builtin_amdgcn_perm(pa.w, pa.z, 0x0c0c0400u), __builtin_amdgcn_perm(pa.y, pa.x, 0x0c0c0400u), 0x05040100u));
    const float lb = u2f(__builtin_amdgcn_perm(__builtin_amdgcn_perm(pb.w, pb.z, 0x0c0c0400u), __builtin_amdgcn_perm(pb.y, pb.x, 0x0c0c0400u), 0x05040100u));
    const float Mw = x32_max(x16_max(fmaxf(la, lb)));
    const float wa = __builtin_amdgcn_exp2f(la - Mw), wb = __builtin_amdgcn_exp2f(lb - Mw);
    const float Ww = x32_sum(x16_sum(wa + wb));
    float a[4];
#pragma unroll
    for (int r = 0; r < 4; ++r) a[r] = fmaf(u2f(pa[r] & 0xFFFFFF00u), wa, u2f(pb[r] & 0xFFFFFF00u) * wb);
    const float v = tr32(tr16(a[0], a[1]), tr16(a[2], a[3]));
    *(LAS float*)(L + L_C + (w * 64 + (i16 >> 3) * 32 + g4 * 8 + (i16 & 7)) * 4) = v;
    if (lane == 0) *(LAS v2u*)(L + L_C + 2048 + w * 8) = v2u{f2u(Mw), f2u(Ww)};
  }
  STAMP(7);
  WG_BARRIER();
  {
    const int b = (lane >> 3) & 1, k = lane & 7;
    float mv[4], wv[4], av[4];
#pragma unroll
    for (int t = 0; t < 4; ++t) {
      const v2u mw = *(LAS v2u*)(L + L_C + 2048 + (b * 4 + t) * 8);
      mv[t] = u2f(mw.x); wv[t] = u2f(mw.y);
      av[t] = *(LAS float*)(L + L_C + ((b * 4 + t) * 64 + w * 8 + k) * 4);
    }
    const float M = fmaxf(fmaxf(mv[0], mv[1]), fmaxf(mv[2], mv[3]));
    float num = 0.f, den = 0.f;
#pragma unroll
    for (int t = 0; t < 4; ++t) { const float f = __builtin_amdgcn_exp2f(mv[t] - M); num = fmaf(f, av[t], num); den = fmaf(f, wv[t], den); }
    const float ov = num / den;
    float c0[8], c1[8];
#pragma unroll
    for (int kk = 0; kk < 8; ++kk) { c0[kk] = rdl(ov, kk); c1[kk] = rdl(ov, 8 + kk); }
    f32x4 s0, s1;
    gemv256(wor, c0, c1, s0, s1);
    *(LAS f32x4*)(L + L_S + w * 2048 + lane * 16) = s0;
    *(LAS f32x4*)(L + L_S + w * 2048 + 1024 + lane * 16) = s1;
  }
  WG_BARRIER();
  if (tid < 128) {
    f32x4 s4 = {0.f, 0.f, 0.f, 0.f};
#pragma unroll
    for (int ww = 0; ww < 8; ++ww) s4 += *(LAS f32x4*)(L + L_S + ww * 2048 + sfoff);
    __builtin_amdgcn_raw_buffer_store_b128(clean4(s4), rs, WS_S2 + (cg * 64 + rg) * 2048 + tid * 16, 0, AUX_SC1);
  }
  STAMP(8);

  if (reducer) {
    const int oidx = ((tid >> 4) & 1) * D + cg * 256 + (rg - 48) * 16 + (tid & 15);
    unsigned short xo = 0;
    if (tid < 32) xo = p.x[oidx];
    reduce_lines(L + L_R + 1024 + w * 128, rs, WS_S2 + (cg * 64 + (tid >> 3)) * 2048 + (rg - 48) * 128 + (tid & 7) * 16, true, lane);
    WG_BARRIER();
    if (tid < 32) {
      float f = 0.f, s = 0.f;
#pragma unroll
      for (int ww = 0; ww < 8; ++ww) { f += *(LAS float*)(L + L_R + ww * 128 + tid * 4); s += *(LAS float*)(L + L_R + 1024 + ww * 128 + tid * 4); }
      { const u64 tn = __builtin_amdgcn_s_memrealtime(); if (DIAG == 9) tk1 = tn; const u64 until = tn + 2 * (tk1 - tk0); while (__builtin_amdgcn_s_memrealtime() < until) {} }
      const float r = fmaf(s, silu(f), bflo(xo));
      p.out[oidx] = (unsigned short)(cvtpk(r, 0.f) & 0xFFFFu);
    }
  }
  STAMP(9);
}

}

extern "C" void kernel_launch(void* const* d_in, const int* in_sizes, int n_in, void* d_out, int out_size, void* d_ws,
                              size_t ws_size, hipStream_t stream) {
  Params p;
  p.x = (const unsigned short*)d_in[0];
  p.kc = (const unsigned short*)d_in[1];
  p.vc = (const unsigned short*)d_in[2];
  p.wq = (const unsigned short*)d_in[3];
  p.wo = (const unsigned short*)d_in[4];
  p.wg = (const unsigned short*)d_in[6];
  p.wu = (const unsigned short*)d_in[7];
  p.wd = (const unsigned short*)d_in[8];
  p.out = (unsigned short*)d_out;
  p.ws = (unsigned char*)d_ws;
  hipLaunchKernelGGL(fused_decode, dim3(NWG), dim3(NTHR), 0, stream, p);
}
```

```cpp
#include <hip/hip_runtime.h>
#include <hip/hip_bf16.h>
#include <stdint.h>

#define GAS __attribute__((address_space(1)))
#define LAS __attribute__((address_space(3)))
typedef unsigned v4u __attribute__((ext_vector_type(4)));
typedef unsigned v2u __attribute__((ext_vector_type(2)));
typedef float f32x4 __attribute__((ext_vector_type(4)));
typedef short bf16x8 __attribute__((ext_vector_type(8)));
typedef short bf16x4 __attribute__((ext_vector_type(4)));
typedef unsigned long long u64;

namespace {

constexpr int NWG = 256, NTHR = 512;
constexpr int D = 1024, HD = 4096, FF = 3584, SK = 32768;
constexpr int AUX_SC1 = 16;
constexpr unsigned SENT = 0xFFFFFFFFu;
constexpr unsigned SPIN_CAP = 1u << 22;

constexpr int WS_E = 0;
constexpr int WS_QF = 4096;
constexpr int WS_QP = WS_QF + 65536;
constexpr int WS_GU = WS_QP + 262144;
constexpr int WS_P1 = WS_GU + 524288;
constexpr int WS_S2 = WS_P1 + 1048576;
constexpr int WS_F2 = WS_S2 + 524288;
constexpr int WS_CT = WS_F2 + 524288;
constexpr int WS_END = WS_CT + 32 * 256;
constexpr int WS_ALL = WS_END;

constexpr int L_RED = 0;
constexpr int L_Q = 12288;
constexpr int L_ML = L_Q + 16 * 272;
constexpr int L_O = L_ML + 512;
constexpr int L_S = L_O + 32768;
constexpr int L_F = L_S + 16384;
constexpr int L_C = L_F + 16384;
constexpr int L_R = L_C + 2048 + 64;
constexpr int L_USED = L_R + 2048;
constexpr int L_BYTES = 88 * 1024;
static_assert(L_USED <= L_BYTES, "lds");

struct Params {
  const unsigned short* x;
  const unsigned short* wq;
  unsigned char* ws;
  const unsigned short* wg;
  const unsigned short* wu;
  const unsigned short* wd;
  const unsigned short* wo;
  const unsigned short* kc;
  const unsigned short* vc;
  unsigned short* out;
};

__device__ __forceinline__ float bflo(unsigned u) { return __builtin_bit_cast(float, u << 16); }
__device__ __forceinline__ float bfhi(unsigned u) { return __builtin_bit_cast(float, u & 0xFFFF0000u); }
__device__ __forceinline__ unsigned f2u(float f) { return __builtin_bit_cast(unsigned, f); }
__device__ __forceinline__ float u2f(unsigned u) { return __builtin_bit_cast(float, u); }
typedef __bf16 bf16p __attribute__((ext_vector_type(2)));
typedef float f32x2 __attribute__((ext_vector_type(2)));
__device__ __forceinline__ unsigned cvtpk(float lo, float hi) {
  return __builtin_bit_cast(unsigned, __builtin_convertvector(f32x2{lo, hi}, bf16p));
}
template <int CTRL> __device__ __forceinline__ float dpp(float x) {
  return __builtin_bit_cast(float, __builtin_amdgcn_mov_dpp(__builtin_bit_cast(int, x), CTRL, 0xf, 0xf, true));
}
constexpr int XOR1 = 0xB1, XOR2 = 0x4E, XOR7 = 0x141, XOR8 = 0x128, ROR4 = 0x124, XOR15 = 0x140;
__device__ __forceinline__ float x16_sum(float x) {
  auto s = __builtin_amdgcn_permlane16_swap(f2u(x), f2u(x), false, false);
  return u2f(s[0]) + u2f(s[1]);
}
__device__ __forceinline__ float x32_sum(float x) {
  auto s = __builtin_amdgcn_permlane32_swap(f2u(x), f2u(x), false, false);
  return u2f(s[0]) + u2f(s[1]);
}
__device__ __forceinline__ float x16_max(float x) {
  auto s = __builtin_amdgcn_permlane16_swap(f2u(x), f2u(x), false, false);
  return fmaxf(u2f(s[0]), u2f(s[1]));
}
__device__ __forceinline__ float x32_max(float x) {
  auto s = __builtin_amdgcn_permlane32_swap(f2u(x), f2u(x), false, false);
  return fmaxf(u2f(s[0]), u2f(s[1]));
}
__device__ __forceinline__ float row16_sum(float x) {
  x += dpp<XOR1>(x);
  x += dpp<XOR2>(x);
  x += dpp<XOR7>(x);
  x += dpp<XOR8>(x);
  return x;
}
__device__ __forceinline__ float row16_max(float x) {
  x = fmaxf(x, dpp<XOR1>(x));
  x = fmaxf(x, dpp<XOR2>(x));
  x = fmaxf(x, dpp<XOR7>(x));
  x = fmaxf(x, dpp<XOR8>(x));
  return x;
}
__device__ __forceinline__ float wave_sum(float x) { return x32_sum(x16_sum(row16_sum(x))); }
__device__ __forceinline__ float tr16(float a, float b) {
  auto s = __builtin_amdgcn_permlane16_swap(f2u(a), f2u(b), false, false);
  return u2f(s[0]) + u2f(s[1]);
}
__device__ __forceinline__ float tr32(float a, float b) {
  auto s = __builtin_amdgcn_permlane32_swap(f2u(a), f2u(b), false, false);
  return u2f(s[0]) + u2f(s[1]);
}
__device__ __forceinline__ v4u ldnt(const GAS v4u* p) { return __builtin_nontemporal_load(p); }
__device__ __forceinline__ v2u ldnt2(const GAS v2u* p) { return __builtin_nontemporal_load(p); }
__device__ __forceinline__ unsigned clean(float f) {
  const unsigned u = f2u(f);
  return u == SENT ? 0x7FC00000u : u;
}
__device__ __forceinline__ v4u clean4(f32x4 v) { return v4u{clean(v.x), clean(v.y), clean(v.z), clean(v.w)}; }
__device__ __forceinline__ bool full4(v4u v) { return v.x != SENT && v.y != SENT && v.z != SENT && v.w != SENT; }
__device__ __forceinline__ float rdl(float v, int lane) { return u2f(__builtin_amdgcn_readlane(f2u(v), lane)); }
__device__ __forceinline__ float silu(float v) { return v / (1.0f + __expf(-v)); }

#define SB() __builtin_amdgcn_sched_barrier(0)
#define WG_BARRIER()                                        \
  do {                                                      \
    asm volatile("s_waitcnt lgkmcnt(0)" ::: "memory");      \
    __builtin_amdgcn_s_barrier();                           \
    asm volatile("" ::: "memory");                          \
  } while (0)
#define STAMP(i)
#define STAMP_S(i)

__device__ __forceinline__ f32x4 gemv64(const v4u (&w)[4], const float (&x0)[4], const float (&x1)[4]) {
  float a0[8], a1[8];
#pragma unroll
  for (int c = 0; c < 8; ++c) { a0[c] = 0.f; a1[c] = 0.f; }
#pragma unroll
  for (int i = 0; i < 4; ++i) {
#pragma unroll
    for (int k = 0; k < 4; ++k) {
      const float lo = bflo(w[i][k]), hi = bfhi(w[i][k]);
      a0[2 * k] = fmaf(lo, x0[i], a0[2 * k]);
      a0[2 * k + 1] = fmaf(hi, x0[i], a0[2 * k + 1]);
      a1[2 * k] = fmaf(lo, x1[i], a1[2 * k]);
      a1[2 * k + 1] = fmaf(hi, x1[i], a1[2 * k + 1]);
    }
  }
  float t[8];
#pragma unroll
  for (int c = 0; c < 8; ++c) t[c] = tr16(a0[c], a1[c]);
  f32x4 r;
#pragma unroll
  for (int c = 0; c < 4; ++c) {
    float v = tr32(t[c], t[c + 4]);
    v += dpp<XOR8>(v);
    r[c] = v;
  }
  return r;
}

__device__ __forceinline__ void gemv256(const v2u (&w)[8], const float (&c0)[8], const float (&c1)[8], f32x4& s0, f32x4& s1) {
  s0 = f32x4{0.f, 0.f, 0.f, 0.f};
  s1 = f32x4{0.f, 0.f, 0.f, 0.f};
#pragma unroll
  for (int k = 0; k < 8; ++k) {
    const f32x4 wf = {bflo(w[k].x), bfhi(w[k].x), bflo(w[k].y), bfhi(w[k].y)};
#pragma unroll
    for (int c = 0; c < 4; ++c) {
      s0[c] = fmaf(wf[c], c0[k], s0[c]);
      s1[c] = fmaf(wf[c], c1[k], s1[c]);
    }
  }
}

__device__ __forceinline__ v2u granule(float v, unsigned tag_lo, unsigned tag_hi) {
  return v2u{(tag_lo << 24) | ((f2u(v) + 0x80u) >> 8), tag_hi};
}
__device__ __forceinline__ bool tag2(v4u g, unsigned tag_lo, unsigned tag_hi) {
  return (g.y == tag_hi) & ((g.x >> 24) == tag_lo) & (g.w == tag_hi) & ((g.z >> 24) == tag_lo);
}
#define TOUCH4(a) asm volatile("" : "+v"(a[0]), "+v"(a[1]), "+v"(a[2]), "+v"(a[3]))
#define TOUCH8(a) asm volatile("" : "+v"(a[0]), "+v"(a[1]), "+v"(a[2]), "+v"(a[3]), "+v"(a[4]), "+v"(a[5]), "+v"(a[6]), "+v"(a[7]))
constexpr int AUX_NT = 2;
#define LDK(kb_, off_) do { _Pragma("unroll") for (int j = 0; j < 4; ++j) kb_[j] = __builtin_amdgcn_raw_buffer_load_b128(rk, (klane + (off_)) + j * 64, 0, AUX_NT); SB(); } while (0)
#define LDV(vb_, off_) do { _Pragma("unroll") for (int j = 0; j < 4; ++j) vb_[j] = __builtin_amdgcn_raw_buffer_load_b128(rv, (vlane + (off_)) + j * 1024, 0, AUX_NT); SB(); } while (0)
#define LDK2(kb_, a_) do { _Pragma("unroll") for (int j = 0; j < 4; ++j) kb_[j] = __builtin_amdgcn_raw_buffer_load_b128(rk, (a_) + j * 64, 0, AUX_NT); SB(); } while (0)
#define LDV2(vb_, a_) do { _Pragma("unroll") for (int j = 0; j < 4; ++j) vb_[j] = __builtin_amdgcn_raw_buffer_load_b128(rv, (a_) + j * 1024, 0, AUX_NT); SB(); } while (0)

struct AttnState {
  float m, l;
  f32x4 o[8];
};
__device__ __forceinline__ void attn_unit(AttnState& st, v4u (&kc)[4], v4u (&vc)[4], const v4u (&qf)[4]) {
  constexpr float C2 = 0.088388347648318440f * 1.4426950408889634f;
  f32x4 s = {0.f, 0.f, 0.f, 0.f};
#pragma unroll
  for (int kk = 0; kk < 4; ++kk)
    s = __builtin_amdgcn_mfma_f32_16x16x32_bf16(__builtin_bit_cast(bf16x8, kc[kk]), __builtin_bit_cast(bf16x8, qf[kk]), s, 0, 0, 0);
#pragma unroll
  for (int r = 0; r < 4; ++r) s[r] = (s[r] + dpp<XOR8>(s[r])) * C2;
  float mx = fmaxf(fmaxf(s[0], s[1]), fmaxf(s[2], s[3]));
  mx = x32_max(x16_max(mx));
  const float mn = fmaxf(st.m, mx);
  const float alpha = __builtin_amdgcn_exp2f(st.m - mn);
  st.m = mn;
  const float p0 = __builtin_amdgcn_exp2f(s[0] - mn), p1 = __builtin_amdgcn_exp2f(s[1] - mn);
  const float p2 = __builtin_amdgcn_exp2f(s[2] - mn), p3 = __builtin_amdgcn_exp2f(s[3] - mn);
  st.l = fmaf(st.l, alpha, (p0 + p1) + (p2 + p3));
  const v2u pb = {cvtpk(p0, p1), cvtpk(p2, p3)};
#pragma unroll
  for (int e = 0; e < 8; ++e) st.o[e] *= alpha;
  SB();
#pragma unroll
  for (int e = 0; e < 8; ++e) {
    const unsigned sel = (e & 1) ? 0x07060302u : 0x05040100u;
    const v2u a = {__builtin_amdgcn_perm(vc[1][e >> 1], vc[0][e >> 1], sel), __builtin_amdgcn_perm(vc[3][e >> 1], vc[2][e >> 1], sel)};
    st.o[e] = __builtin_amdgcn_mfma_f32_16x16x16bf16_1k(__builtin_bit_cast(bf16x4, a), __builtin_bit_cast(bf16x4, pb), st.o[e], 0, 0, 0);
  }
  SB();
}

#define DEEP 0
#define HEAD3 1
#define NS 8
#define GPOS 1
__global__ void __launch_bounds__(NTHR, 2) fused_decode(Params p) {
  __shared__ __attribute__((aligned(16))) unsigned char smem[L_BYTES];
  LAS unsigned char* L = (LAS unsigned char*)smem;
  const int tid = threadIdx.x, lane = tid & 63, w = __builtin_amdgcn_readfirstlane(tid >> 6);
  const int bid = blockIdx.x;
  const int cg = (bid >> 3) & 3, rg = (bid >> 5) * 8 + (bid & 7);
  const int hkv = bid >> 6, bb = (bid >> 5) & 1, sp = bid & 31;
  const bool has_ffn = rg < 56;
  const bool reducer = rg >= 48;
  constexpr int OOB = 0x7FFF0000;
  const __amdgpu_buffer_rsrc_t rs = __builtin_amdgcn_make_buffer_rsrc((void*)p.ws, 0, WS_ALL, 0x00020000);
  STAMP(0);

  const GAS v4u* xg = (const GAS v4u*)p.x;
  const v4u xa0 = xg[lane * 2], xa1 = xg[lane * 2 + 1], xb0 = xg[128 + lane * 2], xb1 = xg[128 + lane * 2 + 1];
  const int r8 = lane >> 3, c8 = lane & 7;
  const int krow = cg * 256 + w * 32 + r8 * 4;
  const v2u xr0 = *(const GAS v2u*)(p.x + krow), xr1 = *(const GAS v2u*)(p.x + D + krow);
  v4u wqr[4], wgr[4], wur[4];
#pragma unroll
  for (int i = 0; i < 4; ++i) wqr[i] = ldnt((const GAS v4u*)(p.wq + (size_t)(krow + i) * HD + rg * 64 + c8 * 8));
  SB();
  const v2u ev = __builtin_amdgcn_raw_buffer_load_b64(rs, WS_E, 0, AUX_SC1);
  const int rgf = has_ffn ? rg : 0;
  const int fbytes = has_ffn ? D * FF * 2 : 0;
  const __amdgpu_buffer_rsrc_t rwg = __builtin_amdgcn_make_buffer_rsrc((void*)p.wg, 0, fbytes, 0x00020000);
  const __amdgpu_buffer_rsrc_t rwu = __builtin_amdgcn_make_buffer_rsrc((void*)p.wu, 0, fbytes, 0x00020000);
  const __amdgpu_buffer_rsrc_t rwd = __builtin_amdgcn_make_buffer_rsrc((void*)p.wd, 0, fbytes, 0x00020000);
  const int guoff = (krow * FF + rgf * 64 + c8 * 8) * 2;
#pragma unroll
  for (int i = 0; i < 4; ++i) wgr[i] = __builtin_amdgcn_raw_buffer_load_b128(rwg, guoff + i * FF * 2, 0, AUX_NT);
  SB();
  {
    const v4u s4 = {SENT, SENT, SENT, SENT};
    const int off = tid < 256 ? WS_P1 + bid * 4096 + tid * 16
                              : (tid < 384 ? WS_S2 - 4096 : WS_F2 - 6144) + (cg * 64 + rg) * 2048 + tid * 16;
    __builtin_amdgcn_raw_buffer_store_b128(s4, rs, off, 0, AUX_SC1);
    __builtin_amdgcn_raw_buffer_store_b32(0u, rs, (tid == 0 && (bid & 7) == 0) ? WS_CT + (bid >> 3) * 256 : OOB, 0, AUX_SC1);
  }
  SB();
  const int oidx = ((tid >> 4) & 1) * D + cg * 256 + (rg & 15) * 16 + (tid & 15);
  unsigned xo = *(const GAS unsigned short*)(p.x + oidx);
  SB();
  constexpr int NDYN = 64 * (8 - NS);
  const int i16 = lane & 15, g4 = lane >> 4;
  const __amdgpu_buffer_rsrc_t rk = __builtin_amdgcn_make_buffer_rsrc((void*)p.kc, 0, 8 * SK * 256, 0x00020000);
  const __amdgpu_buffer_rsrc_t rv = __builtin_amdgcn_make_buffer_rsrc((void*)p.vc, 0, 8 * SK * 256, 0x00020000);
  const int klane = (4 * (i16 & 3) + (i16 >> 2)) * 256 + g4 * 16, vlane = lane * 16;
  const int obase = ((bb * 4 + hkv) * SK + (sp >> 3) * 8192) * 256;
  const int sbase = obase + ((bid & 7) * NS * 8 + w) * 4096;
  const int dbase = obase + NS * 64 * 4096;
  v4u ka[4], va[4], kb[4], vb[4], kc[4], vc[4];
  v2u wdr[8], wor[8];
#define LDWU() do { _Pragma("unroll") for (int i = 0; i < 4; ++i) wur[i] = __builtin_amdgcn_raw_buffer_load_b128(rwu, guoff + i * FF * 2, 0, AUX_NT); SB(); } while (0)
#define LDWD() do { _Pragma("unroll") for (int k = 0; k < 8; ++k) wdr[k] = __builtin_amdgcn_raw_buffer_load_b64(rwd, lane * 8, ((rgf * 64 + w * 8 + k) * D + cg * 256) * 2, AUX_NT); SB(); } while (0)
#define LDWO() do { _Pragma("unroll") for (int k = 0; k < 8; ++k) wor[k] = ldnt2((const GAS v2u*)(p.wo + (size_t)(rg * 64 + w * 8 + k) * D + cg * 256 + lane * 4)); SB(); } while (0)

  float x0[4], x1[4];
  {
    float va_[16], vb_[16];
#pragma unroll
    for (int k = 0; k < 4; ++k) {
      va_[2 * k] = bflo(xa0[k]); va_[2 * k + 1] = bfhi(xa0[k]); va_[8 + 2 * k] = bflo(xa1[k]); va_[8 + 2 * k + 1] = bfhi(xa1[k]);
      vb_[2 * k] = bflo(xb0[k]); vb_[2 * k + 1] = bfhi(xb0[k]); vb_[8 + 2 * k] = bflo(xb1[k]); vb_[8 + 2 * k + 1] = bfhi(xb1[k]);
    }
    float sa = 0.f, sb = 0.f;
#pragma unroll
    for (int k = 0; k < 16; ++k) { sa += va_[k]; sb += vb_[k]; }
    const float ma = wave_sum(sa) * (1.0f / D), mb = wave_sum(sb) * (1.0f / D);
    float qa = 0.f, qb = 0.f;
#pragma unroll
    for (int k = 0; k < 16; ++k) { const float da = va_[k] - ma, db = vb_[k] - mb; qa = fmaf(da, da, qa); qb = fmaf(db, db, qb); }
    const float ra = 1.0f / sqrtf(wave_sum(qa) * (1.0f / D) + 1e-5f), rb = 1.0f / sqrtf(wave_sum(qb) * (1.0f / D) + 1e-5f);
    x0[0] = (bflo(xr0.x) - ma) * ra; x0[1] = (bfhi(xr0.x) - ma) * ra; x0[2] = (bflo(xr0.y) - ma) * ra; x0[3] = (bfhi(xr0.y) - ma) * ra;
    x1[0] = (bflo(xr1.x) - mb) * rb; x1[1] = (bfhi(xr1.x) - mb) * rb; x1[2] = (bflo(xr1.y) - mb) * rb; x1[3] = (bfhi(xr1.y) - mb) * rb;
  }
  const int rho = lane >> 4;
  const int redoff = ((rho & 1) * 64 + c8 * 8 + (rho >> 1) * 4) * 4;

  {
    const f32x4 r = gemv64(wqr, x0, x1);
    if ((lane & 8) == 0) *(LAS f32x4*)(L + L_RED + w * 512 + redoff) = r;
  }
  LDWU();
  const u64 e1 = ((((u64)__builtin_amdgcn_readfirstlane(ev.y)) << 32) | (u64)(unsigned)__builtin_amdgcn_readfirstlane(ev.x)) + 1ull;
  const unsigned tag_hi = (unsigned)(e1 >> 8), tag_lo = (unsigned)(e1 & 0xFFull);
  WG_BARRIER();
  if (tid < 128) {
    float s = 0.f;
#pragma unroll
    for (int ww = 0; ww < 8; ++ww) s += *(LAS float*)(L + L_RED + ww * 512 + tid * 4);
    __builtin_amdgcn_raw_buffer_store_b64(granule(s, tag_lo, tag_hi), rs, WS_QP + (rg * 4 + cg) * 1024 + tid * 8, 0, AUX_SC1);
  }
  STAMP(1);
  SB();

  {
    const f32x4 r = gemv64(wgr, x0, x1);
    if ((lane & 8) == 0) *(LAS f32x4*)(L + L_RED + 4096 + w * 512 + redoff) = r;
  }
  LDWD();
  {
    const f32x4 r = gemv64(wur, x0, x1);
    if ((lane & 8) == 0) *(LAS f32x4*)(L + L_RED + 8192 + w * 512 + redoff) = r;
  }
  asm volatile("" : "+v"(xo));
  const int qpoff = w == 7 ? WS_QP + (rg * 4 + (lane >> 4)) * 1024 + ((lane >> 3) & 1) * 512 + (cg * 16 + (lane & 7) * 2) * 8 : OOB;
  v4u qp = __builtin_amdgcn_raw_buffer_load_b128(rs, qpoff, 0, AUX_SC1);
  SB();
  LDWO();
  WG_BARRIER();
  if (tid < 256 && has_ffn) {
    float s = 0.f;
#pragma unroll
    for (int ww = 0; ww < 8; ++ww) s += *(LAS float*)(L + L_RED + 4096 + (tid >> 7) * 4096 + ww * 512 + (tid & 127) * 4);
    __builtin_amdgcn_raw_buffer_store_b64(granule(s, tag_lo, tag_hi), rs, WS_GU + (rg * 4 + cg) * 2048 + tid * 8, 0, AUX_SC1);
  }
  if (w == 7) {
    if (!__all(tag2(qp, tag_lo, tag_hi))) {
      for (unsigned spins = 0; spins < SPIN_CAP; ++spins) {
        qp = __builtin_amdgcn_raw_buffer_load_b128(rs, qpoff, 0, AUX_SC1);
        if (__all(tag2(qp, tag_lo, tag_hi))) break;
        __builtin_amdgcn_s_sleep(2);
      }
    }
    const float qa = x32_sum(x16_sum(u2f(qp.x << 8))), qb = x32_sum(x16_sum(u2f(qp.z << 8)));
    const v2u ga = granule(qa, tag_lo, tag_hi), gb = granule(qb, tag_lo, tag_hi);
    const int qo = WS_QF + ((hkv * 2 + ((lane >> 3) & 1)) * 1024 + (rg & 15) * 64 + cg * 16 + (lane & 7) * 2 + ((lane >> 4) & 1)) * 8;
    __builtin_amdgcn_raw_buffer_store_b64((lane & 16) ? gb : ga, rs, lane < 32 ? qo : OOB, 0, AUX_SC1);
  }
  STAMP(2);
  SB();

  const int sfoff = ((tid >> 2) & 1) * 1024 + ((tid & 127) >> 3) * 64 + (tid & 3) * 16;
  {
    const int qoff = WS_QF + ((hkv * 2 + bb) * 1024 + tid * 2) * 8;
    const int goff = WS_GU + (rgf * 4 + (lane >> 4)) * 2048 + ((lane >> 2) & 3) * 512 + (w * 8 + (lane & 3) * 2) * 8;
    const int coff = reducer ? WS_QF + (((tid >> 7) * 2) * 1024 + ((tid >> 3) & 15) * 64 + cg * 16) * 8 : OOB;
    v4u qg, gu, ck;
#define GATHER() do { qg = __builtin_amdgcn_raw_buffer_load_b128(rs, qoff, 0, AUX_SC1); gu = __builtin_amdgcn_raw_buffer_load_b128(rs, has_ffn ? goff : OOB, 0, AUX_SC1); \
      ck = __builtin_amdgcn_raw_buffer_load_b128(rs, coff, 0, AUX_SC1); SB(); } while (0)
#define GATHER_OK() (tag2(qg, tag_lo, tag_hi) & (tag2(gu, tag_lo, tag_hi) | !has_ffn) & (tag2(ck, tag_lo, tag_hi) | !reducer))
    TOUCH8(wdr);
    LDK(ka, sbase);
    TOUCH8(wor);
    LDV(va, sbase);
    TOUCH4(ka);
    GATHER();
    LDK(kb, sbase + 32768);
    TOUCH4(va);
    LDV(vb, sbase + 32768);
    TOUCH4(kb);
    LDK(kc, sbase + 2 * 32768);
    LDV(vc, sbase + 2 * 32768);
    if (!__all(GATHER_OK())) {
      for (unsigned spins = 0; spins < SPIN_CAP; ++spins) {
        GATHER();
        if (__all(GATHER_OK())) break;
        __builtin_amdgcn_s_sleep(2);
      }
    }
    const float q0 = u2f(qg.x << 8), q1 = u2f(qg.z << 8);
    const unsigned hi = cvtpk(q0, q1);
    const unsigned lo = cvtpk(q0 - bflo(hi), q1 - bfhi(hi));
    *(LAS unsigned*)(L + L_Q + w * 272 + lane * 4) = hi;
    *(LAS unsigned*)(L + L_Q + (w + 8) * 272 + lane * 4) = lo;
    const float ga = x32_sum(x16_sum(u2f(gu.x << 8))), gb = x32_sum(x16_sum(u2f(gu.z << 8)));
    const float ha = silu(ga) * dpp<XOR8>(ga), hb = silu(gb) * dpp<XOR8>(gb);
    float h0[8], h1[8];
#pragma unroll
    for (int pr = 0; pr < 4; ++pr) {
      h0[2 * pr] = rdl(ha, pr); h0[2 * pr + 1] = rdl(hb, pr);
      h1[2 * pr] = rdl(ha, 4 + pr); h1[2 * pr + 1] = rdl(hb, 4 + pr);
    }
    f32x4 s0, s1;
    gemv256(wdr, h0, h1, s0, s1);
    *(LAS f32x4*)(L + L_F + w * 2048 + lane * 16) = s0;
    *(LAS f32x4*)(L + L_F + w * 2048 + 1024 + lane * 16) = s1;
  }
  WG_BARRIER();
  if (tid < 128 && has_ffn) {
    f32x4 s4 = {0.f, 0.f, 0.f, 0.f};
#pragma unroll
    for (int ww = 0; ww < 8; ++ww) s4 += *(LAS f32x4*)(L + L_F + ww * 2048 + sfoff);
    __builtin_amdgcn_raw_buffer_store_b128(clean4(s4), rs, WS_F2 + (cg * 64 + rg) * 2048 + tid * 16, 0, AUX_SC1);
  }
  v4u qf[4];
#pragma unroll
  for (int kk = 0; kk < 4; ++kk) qf[kk] = *(LAS v4u*)(L + L_Q + i16 * 272 + kk * 64 + g4 * 16);
  STAMP(3);

  AttnState st;
  st.m = -INFINITY;
  st.l = 0.f;
#pragma unroll
  for (int e = 0; e < 8; ++e) st.o[e] = f32x4{0.f, 0.f, 0.f, 0.f};
  const int ctoff = lane == 0 ? WS_CT + (bid >> 3) * 256 : OOB;
  unsigned tk = 0;
#define TICKET2(a_) do { tk = (unsigned)__builtin_amdgcn_raw_ptr_buffer_atomic_add_i32(1, rs, (a_), 0, 0); SB(); } while (0)
  static_assert((NS & 1) == 0, "even");
  attn_unit(st, ka, va, qf);
  attn_unit(st, kb, vb, qf);
  if (NS == 4) TICKET2(ctoff);
  LDK(ka, sbase + 3 * 32768);
  LDV(va, sbase + 3 * 32768);
  attn_unit(st, kc, vc, qf);
#pragma unroll
  for (int u = 4; u < NS; u += 2) {
    LDK(kb, sbase + u * 32768);
    LDV(vb, sbase + u * 32768);
    attn_unit(st, ka, va, qf);
    if (u + 2 == NS && NDYN > 0) TICKET2(ctoff);
    LDK(ka, sbase + (u + 1) * 32768);
    LDV(va, sbase + (u + 1) * 32768);
    attn_unit(st, kb, vb, qf);
  }
  STAMP(4);
  if (NDYN == 0) {
    attn_unit(st, ka, va, qf);
  } else {
    for (;;) {
      const unsigned t0 = __builtin_amdgcn_readfirstlane(tk);
      const int o0 = t0 < NDYN ? dbase + (int)t0 * 4096 : OOB;
      const int ak0 = klane + o0, av0 = vlane + o0, at0 = t0 < NDYN ? ctoff : OOB;
      TICKET2(at0);
      LDK2(kb, ak0);
      LDV2(vb, av0);
      asm volatile("" ::"v"(ak0), "v"(av0), "v"(at0));
      attn_unit(st, ka, va, qf);
      if (t0 >= NDYN) break;
      const unsigned t1 = __builtin_amdgcn_readfirstlane(tk);
      const int o1 = t1 < NDYN ? dbase + (int)t1 * 4096 : OOB;
      const int ak1 = klane + o1, av1 = vlane + o1, at1 = t1 < NDYN ? ctoff : OOB;
      TICKET2(at1);
      LDK2(ka, ak1);
      LDV2(va, av1);
      asm volatile("" ::"v"(ak1), "v"(av1), "v"(at1));
      attn_unit(st, kb, vb, qf);
      if (t1 >= NDYN) break;
    }
  }
  STAMP(5);

  {
    const float l = x32_sum(x16_sum(st.l));
    if (i16 < 8) {
#pragma unroll
      for (int e = 0; e < 8; ++e) *(LAS f32x4*)(L + L_O + (w * 256 + (i16 * 4 + g4) * 8 + (e ^ i16)) * 16) = st.o[e];
      if (g4 == 0) *(LAS v2u*)(L + L_ML + (w * 8 + i16) * 8) = v2u{f2u(st.m), f2u(l)};
    }
  }
  WG_BARRIER();
  if (tid < 256) {
    const int j = tid >> 5;
    float mw[8], lw[8];
#pragma unroll
    for (int ww = 0; ww < 8; ++ww) { const v2u t = *(LAS v2u*)(L + L_ML + (ww * 8 + j) * 8); mw[ww] = u2f(t.x); lw[ww] = u2f(t.y); }
    float M = mw[0];
#pragma unroll
    for (int ww = 1; ww < 8; ++ww) M = fmaxf(M, mw[ww]);
    float Ls = 0.f;
    f32x4 acc = {0.f, 0.f, 0.f, 0.f};
#pragma unroll
    for (int ww = 0; ww < 8; ++ww) {
      const float wt = __builtin_amdgcn_exp2f(mw[ww] - M);
      Ls = fmaf(wt, lw[ww], Ls);
      acc += *(LAS f32x4*)(L + L_O + (ww * 256 + (tid ^ j)) * 16) * wt;
    }
    acc *= 1.0f / Ls;
    const unsigned lse = f2u(M + __builtin_amdgcn_logf(Ls));
    v4u pk;
#pragma unroll
    for (int r = 0; r < 4; ++r) {
      const unsigned t = ((f2u(acc[r]) + 0x80u) & 0xFFFFFF00u) | ((lse >> (8 * r)) & 0xFFu);
      pk[r] = t == SENT ? 0x7FC000FFu : t;
    }
    __builtin_amdgcn_raw_buffer_store_b128(pk, rs, WS_P1 + bid * 4096 + tid * 16, 0, AUX_SC1);
  }
  STAMP(6);
  SB();

  const int roff = (cg * 64 + (tid >> 3)) * 2048 + (rg & 15) * 128 + (tid & 7) * 16;
  v4u fl;
  {
    const int poff = WS_P1 + (hkv * 64 + w * 8 + g4) * 4096 + (rg & 15) * 256 + i16 * 16;
    fl = __builtin_amdgcn_raw_buffer_load_b128(rs, (reducer && tid < 56 * 8) ? WS_F2 + roff : OOB, 0, AUX_SC1);
    v4u pa = __builtin_amdgcn_raw_buffer_load_b128(rs, poff, 0, AUX_SC1);
    v4u pb = __builtin_amdgcn_raw_buffer_load_b128(rs, poff + 4 * 4096, 0, AUX_SC1);
    bool ha = full4(pa), hb = full4(pb);
    for (unsigned spins = 0; spins < SPIN_CAP; ++spins) {
      if (__all(ha & hb)) break;
      __builtin_amdgcn_s_sleep(1);
      if (!ha) pa = __builtin_amdgcn_raw_buffer_load_b128(rs, poff, 0, AUX_SC1);
      if (!hb) pb = __builtin_amdgcn_raw_buffer_load_b128(rs, poff + 4 * 4096, 0, AUX_SC1);
      ha = full4(pa);
      hb = full4(pb);
    }
    const float la = u2f(__builtin_amdgcn_perm(__builtin_amdgcn_perm(pa.w, pa.z, 0x0c0c0400u), __builtin_amdgcn_perm(pa.y, pa.x, 0x0c0c0400u), 0x05040100u));
    const float lb = u2f(__builtin_amdgcn_perm(__builtin_amdgcn_perm(pb.w, pb.z, 0x0c0c0400u), __builtin_amdgcn_perm(pb.y, pb.x, 0x0c0c0400u), 0x05040100u));
    const float Mw = x32_max(x16_max(fmaxf(la, lb)));
    const float wa = __builtin_amdgcn_exp2f(la - Mw), wb = __builtin_amdgcn_exp2f(lb - Mw);
    const float Ww = x32_sum(x16_sum(wa + wb));
    float a[4];
#pragma unroll
    for (int r = 0; r < 4; ++r) a[r] = fmaf(u2f(pa[r] & 0xFFFFFF00u), wa, u2f(pb[r] & 0xFFFFFF00u) * wb);
    const float v = tr32(tr16(a[0], a[1]), tr16(a[2], a[3]));
    *(LAS float*)(L + L_C + (w * 64 + (i16 >> 3) * 32 + g4 * 8 + (i16 & 7)) * 4) = v;
    if (lane == 0) *(LAS v2u*)(L + L_C + 2048 + w * 8) = v2u{f2u(Mw), f2u(Ww)};
  }
  STAMP(7);
  WG_BARRIER();
  {
    const int b = (lane >> 3) & 1, k = lane & 7;
    float mv[4], wv[4], av[4];
#pragma unroll
    for (int t = 0; t < 4; ++t) {
      const v2u mw = *(LAS v2u*)(L + L_C + 2048 + (b * 4 + t) * 8);
      mv[t] = u2f(mw.x); wv[t] = u2f(mw.y);
      av[t] = *(LAS float*)(L + L_C + ((b * 4 + t) * 64 + w * 8 + k) * 4);
    }
    const float M = fmaxf(fmaxf(mv[0], mv[1]), fmaxf(mv[2], mv[3]));
    float num = 0.f, den = 0.f;
#pragma unroll
    for (int t = 0; t < 4; ++t) { const float f = __builtin_amdgcn_exp2f(mv[t] - M); num = fmaf(f, av[t], num); den = fmaf(f, wv[t], den); }
    const float ov = num / den;
    float c0[8], c1[8];
#pragma unroll
    for (int kk = 0; kk < 8; ++kk) { c0[kk] = rdl(ov, kk); c1[kk] = rdl(ov, 8 + kk); }
    f32x4 s0, s1;
    gemv256(wor, c0, c1, s0, s1);
    *(LAS f32x4*)(L + L_S + w * 2048 + lane * 16) = s0;
    *(LAS f32x4*)(L + L_S + w * 2048 + 1024 + lane * 16) = s1;
  }
  WG_BARRIER();
  if (tid < 128) {
    f32x4 s4 = {0.f, 0.f, 0.f, 0.f};
#pragma unroll
    for (int ww = 0; ww < 8; ++ww) s4 += *(LAS f32x4*)(L + L_S + ww * 2048 + sfoff);
    __builtin_amdgcn_raw_buffer_store_b128(clean4(s4), rs, WS_S2 + (cg * 64 + rg) * 2048 + tid * 16, 0, AUX_SC1);
  }
  STAMP(8);

  if (reducer) {
    const int off = roff;
    v4u a = __builtin_amdgcn_raw_buffer_load_b128(rs, WS_S2 + off, 0, AUX_SC1);
    v4u c = fl;
    bool ha = full4(a), hc = full4(c);
    for (unsigned spins = 0; spins < SPIN_CAP; ++spins) {
      if (__all(ha & hc)) break;
      __builtin_amdgcn_s_sleep(1);
      if (!ha) a = __builtin_amdgcn_raw_buffer_load_b128(rs, WS_S2 + off, 0, AUX_SC1);
      if (!hc) c = __builtin_amdgcn_raw_buffer_load_b128(rs, WS_F2 + off, 0, AUX_SC1);
      ha = full4(a);
      hc = full4(c);
    }
    float vs = tr32(tr16(u2f(a.x), u2f(a.y)), tr16(u2f(a.z), u2f(a.w)));
    float vf = tr32(tr16(u2f(c.x), u2f(c.y)), tr16(u2f(c.z), u2f(c.w)));
    vs += dpp<XOR8>(vs);
    vf += dpp<XOR8>(vf);
    *(LAS float*)(L + L_R + ((lane >> 3) & 1) * 1024 + w * 128 + ((lane & 7) * 4 + (lane >> 4)) * 4) = (lane & 8) ? vs : vf;
    WG_BARRIER();
    if (tid < 32) {
      float f = 0.f, s = 0.f;
#pragma unroll
      for (int ww = 0; ww < 8; ++ww) { f += *(LAS float*)(L + L_R + ww * 128 + tid * 4); s += *(LAS float*)(L + L_R + 1024 + ww * 128 + tid * 4); }
      const float r = fmaf(s, silu(f), bflo(xo));
      p.out[oidx] = (unsigned short)(cvtpk(r, 0.f) & 0xFFFFu);
    }
    if (bid == NWG - 1 && tid == 0) {
      const v2u ne = {(unsigned)e1, (unsigned)(e1 >> 32)};
      __builtin_amdgcn_raw_buffer_store_b64(ne, rs, WS_E, 0, AUX_SC1);
    }
  }
  STAMP(9);
}

}

extern "C" void kernel_launch(void* const* d_in, const int* in_sizes, int n_in, void* d_out, int out_size, void* d_ws,
                              size_t ws_size, hipStream_t stream) {
  Params p;
  p.x = (const unsigned short*)d_in[0];
  p.kc = (const unsigned short*)d_in[1];
  p.vc = (const unsigned short*)d_in[2];
  p.wq = (const unsigned short*)d_in[3];
  p.wo = (const unsigned short*)d_in[4];
  p.wg = (const unsigned short*)d_in[6];
  p.wu = (const unsigned short*)d_in[7];
  p.wd = (const unsigned short*)d_in[8];
  p.out = (unsigned short*)d_out;
  p.ws = (unsigned char*)d_ws;
  hipLaunchKernelGGL(fused_decode, dim3(NWG), dim3(NTHR), 0, stream, p);
}
```

```cpp
#define KAUX 0
#define VAUX 2
#define WAUX 0
#include <hip/hip_runtime.h>
#include <hip/hip_bf16.h>
#include <stdint.h>

#define GAS __attribute__((address_space(1)))
#define LAS __attribute__((address_space(3)))
typedef unsigned v4u __attribute__((ext_vector_type(4)));
typedef unsigned v2u __attribute__((ext_vector_type(2)));
typedef float f32x4 __attribute__((ext_vector_type(4)));
typedef short bf16x8 __attribute__((ext_vector_type(8)));
typedef short bf16x4 __attribute__((ext_vector_type(4)));
typedef unsigned long long u64;

namespace {

constexpr int NWG = 256, NTHR = 576;
constexpr int D = 1024, HD = 4096, FF = 3584, SK = 32768;
constexpr int AUX_SC1 = 16;
constexpr unsigned SENT = 0xFFFFFFFFu;
constexpr unsigned SPIN_CAP = 1u << 22;

#define COMPACT 1
constexpr int RSQ = 32768, RSG = 65536, RSP = 131072, RSS = 65536;
constexpr int WS_E = 0;
constexpr int WS_QP = 4096;
constexpr int WS_GU = WS_QP + 8 * RSQ;
constexpr int WS_P1 = WS_GU + 8 * RSG;
constexpr int WS_S2 = WS_P1 + 8 * RSP;
constexpr int WS_F2 = WS_S2 + 8 * RSS;
constexpr int WS_QF = WS_F2 + 8 * RSS;
constexpr int WS_END = WS_QF + 65536;
constexpr int WS_ALL = WS_END;

constexpr int L_RED = 0;
constexpr int L_Q = 12288;
constexpr int L_ML = L_Q + 16 * 272;
constexpr int L_O = L_ML + 512;
constexpr int L_S = L_O + 32768;
constexpr int L_C = L_S + 16384;
constexpr int L_R = L_C + 2048 + 64;
constexpr int L_H = L_R + 2048;
constexpr int L_FL = L_H + 512;
constexpr int L_WO = L_FL + 64;
constexpr int L_WD = L_WO + 32768;
constexpr int L_F = L_WD + 32768;
constexpr int L_USED = L_F + 16384;
constexpr int L_BYTES = 152 * 1024;
static_assert(L_USED <= L_BYTES, "lds");

struct Params {
  const unsigned short* x;
  const unsigned short* wq;
  unsigned char* ws;
  const unsigned short* wg;
  const unsigned short* wu;
  const unsigned short* wd;
  const unsigned short* wo;
  const unsigned short* kc;
  const unsigned short* vc;
  unsigned short* out;
};

__device__ __forceinline__ float bflo(unsigned u) { return __builtin_bit_cast(float, u << 16); }
__device__ __forceinline__ float bfhi(unsigned u) { return __builtin_bit_cast(float, u & 0xFFFF0000u); }
__device__ __forceinline__ unsigned f2u(float f) { return __builtin_bit_cast(unsigned, f); }
__device__ __forceinline__ float u2f(unsigned u) { return __builtin_bit_cast(float, u); }
typedef __bf16 bf16p __attribute__((ext_vector_type(2)));
typedef float f32x2 __attribute__((ext_vector_type(2)));
__device__ __forceinline__ unsigned cvtpk(float lo, float hi) {
  return __builtin_bit_cast(unsigned, __builtin_convertvector(f32x2{lo, hi}, bf16p));
}
template <int CTRL> __device__ __forceinline__ float dpp(float x) {
  return __builtin_bit_cast(float, __builtin_amdgcn_mov_dpp(__builtin_bit_cast(int, x), CTRL, 0xf, 0xf, true));
}
constexpr int XOR1 = 0xB1, XOR2 = 0x4E, XOR7 = 0x141, XOR8 = 0x128, ROR4 = 0x124, XOR15 = 0x140;
__device__ __forceinline__ float x16_sum(float x) {
  auto s = __builtin_amdgcn_permlane16_swap(f2u(x), f2u(x), false, false);
  return u2f(s[0]) + u2f(s[1]);
}
__device__ __forceinline__ float x32_sum(float x) {
  auto s = __builtin_amdgcn_permlane32_swap(f2u(x), f2u(x), false, false);
  return u2f(s[0]) + u2f(s[1]);
}
__device__ __forceinline__ float x16_max(float x) {
  auto s = __builtin_amdgcn_permlane16_swap(f2u(x), f2u(x), false, false);
  return fmaxf(u2f(s[0]), u2f(s[1]));
}
__device__ __forceinline__ float x32_max(float x) {
  auto s = __builtin_amdgcn_permlane32_swap(f2u(x), f2u(x), false, false);
  return fmaxf(u2f(s[0]), u2f(s[1]));
}
__device__ __forceinline__ float row16_sum(float x) {
  x += dpp<XOR1>(x);
  x += dpp<XOR2>(x);
  x += dpp<XOR7>(x);
  x += dpp<XOR8>(x);
  return x;
}
__device__ __forceinline__ float row16_max(float x) {
  x = fmaxf(x, dpp<XOR1>(x));
  x = fmaxf(x, dpp<XOR2>(x));
  x = fmaxf(x, dpp<XOR7>(x));
  x = fmaxf(x, dpp<XOR8>(x));
  return x;
}
__device__ __forceinline__ float wave_sum(float x) { return x32_sum(x16_sum(row16_sum(x))); }
__device__ __forceinline__ float tr16(float a, float b) {
  auto s = __builtin_amdgcn_permlane16_swap(f2u(a), f2u(b), false, false);
  return u2f(s[0]) + u2f(s[1]);
}
__device__ __forceinline__ float tr32(float a, float b) {
  auto s = __builtin_amdgcn_permlane32_swap(f2u(a), f2u(b), false, false);
  return u2f(s[0]) + u2f(s[1]);
}
__device__ __forceinline__ v4u ldnt(const GAS v4u* p) { if (WAUX) return __builtin_nontemporal_load(p); else return *p; }
__device__ __forceinline__ v2u ldnt2(const GAS v2u* p) { return __builtin_nontemporal_load(p); }
__device__ __forceinline__ unsigned clean(float f) {
  const unsigned u = f2u(f);
  return u == SENT ? 0x7FC00000u : u;
}
__device__ __forceinline__ v4u clean4(f32x4 v) { return v4u{clean(v.x), clean(v.y), clean(v.z), clean(v.w)}; }
__device__ __forceinline__ bool full4(v4u v) { return v.x != SENT && v.y != SENT && v.z != SENT && v.w != SENT; }
__device__ __forceinline__ float rdl(float v, int lane) { return u2f(__builtin_amdgcn_readlane(f2u(v), lane)); }
__device__ __forceinline__ float rcp(float v) { return __builtin_amdgcn_rcpf(v); }
__device__ __forceinline__ float silu(float v) { return v * rcp(1.0f + __builtin_amdgcn_exp2f(v * -1.4426950408889634f)); }

#define SB() __builtin_amdgcn_sched_barrier(0)
#define WG_BARRIER()                                        \
  do {                                                      \
    asm volatile("s_waitcnt lgkmcnt(0)" ::: "memory");      \
    __builtin_amdgcn_s_barrier();                           \
    asm volatile("" ::: "memory");                          \
  } while (0)
#define STAMP(i)
#define STAMP_S(i)

__device__ __forceinline__ f32x4 gemv64(const v4u (&w)[4], const float (&x0)[4], const float (&x1)[4]) {
  float a0[8], a1[8];
#pragma unroll
  for (int c = 0; c < 8; ++c) { a0[c] = 0.f; a1[c] = 0.f; }
#pragma unroll
  for (int i = 0; i < 4; ++i) {
#pragma unroll
    for (int k = 0; k < 4; ++k) {
      const float lo = bflo(w[i][k]), hi = bfhi(w[i][k]);
      a0[2 * k] = fmaf(lo, x0[i], a0[2 * k]);
      a0[2 * k + 1] = fmaf(hi, x0[i], a0[2 * k + 1]);
      a1[2 * k] = fmaf(lo, x1[i], a1[2 * k]);
      a1[2 * k + 1] = fmaf(hi, x1[i], a1[2 * k + 1]);
    }
  }
  float t[8];
#pragma unroll
  for (int c = 0; c < 8; ++c) t[c] = tr16(a0[c], a1[c]);
  f32x4 r;
#pragma unroll
  for (int c = 0; c < 4; ++c) {
    float v = tr32(t[c], t[c + 4]);
    v += dpp<XOR8>(v);
    r[c] = v;
  }
  return r;
}

__device__ __forceinline__ void gemv256(const v2u (&w)[8], const float (&c0)[8], const float (&c1)[8], f32x4& s0, f32x4& s1) {
  s0 = f32x4{0.f, 0.f, 0.f, 0.f};
  s1 = f32x4{0.f, 0.f, 0.f, 0.f};
#pragma unroll
  for (int k = 0; k < 8; ++k) {
    const f32x4 wf = {bflo(w[k].x), bfhi(w[k].x), bflo(w[k].y), bfhi(w[k].y)};
#pragma unroll
    for (int c = 0; c < 4; ++c) {
      s0[c] = fmaf(wf[c], c0[k], s0[c]);
      s1[c] = fmaf(wf[c], c1[k], s1[c]);
    }
  }
}

__device__ __forceinline__ v2u granule(float v, unsigned tag_lo, unsigned tag_hi) {
  return v2u{(tag_lo << 24) | ((f2u(v) + 0x80u) >> 8), tag_hi};
}
__device__ __forceinline__ bool tag2(v4u g, unsigned tag_lo, unsigned tag_hi) {
  return (g.y == tag_hi) & ((g.x >> 24) == tag_lo) & (g.w == tag_hi) & ((g.z >> 24) == tag_lo);
}
#define TOUCH4(a) asm volatile("" : "+v"(a[0]), "+v"(a[1]), "+v"(a[2]), "+v"(a[3]))
#define TOUCH8(a) asm volatile("" : "+v"(a[0]), "+v"(a[1]), "+v"(a[2]), "+v"(a[3]), "+v"(a[4]), "+v"(a[5]), "+v"(a[6]), "+v"(a[7]))
constexpr int AUX_NT = WAUX;
#define LDK(kb_, off_) do { _Pragma("unroll") for (int j = 0; j < 4; ++j) kb_[j] = __builtin_amdgcn_raw_buffer_load_b128(rk, (klane + (off_)) + j * 64, 0, KAUX); SB(); } while (0)
#define LDV(vb_, off_) do { _Pragma("unroll") for (int j = 0; j < 4; ++j) vb_[j] = __builtin_amdgcn_raw_buffer_load_b128(rv, (vlane + (off_)) + j * 1024, 0, VAUX); SB(); } while (0)
#define LDK2(kb_, a_) do { _Pragma("unroll") for (int j = 0; j < 4; ++j) kb_[j] = __builtin_amdgcn_raw_buffer_load_b128(rk, (a_) + j * 64, 0, KAUX); SB(); } while (0)
#define LDV2(vb_, a_) do { _Pragma("unroll") for (int j = 0; j < 4; ++j) vb_[j] = __builtin_amdgcn_raw_buffer_load_b128(rv, (a_) + j * 1024, 0, VAUX); SB(); } while (0)

struct AttnState {
  float m, l;
  f32x4 o[8];
};
__device__ __forceinline__ void attn_unit(AttnState& st, v4u (&kc)[4], v4u (&vc)[4], const v4u (&qf)[4]) {
  constexpr float C2 = 0.088388347648318440f * 1.4426950408889634f;
  f32x4 s = {0.f, 0.f, 0.f, 0.f};
#pragma unroll
  for (int kk = 0; kk < 4; ++kk)
    s = __builtin_amdgcn_mfma_f32_16x16x32_bf16(__builtin_bit_cast(bf16x8, kc[kk]), __builtin_bit_cast(bf16x8, qf[kk]), s, 0, 0, 0);
#pragma unroll
  for (int r = 0; r < 4; ++r) s[r] = (s[r] + dpp<XOR8>(s[r])) * C2;
  float mx = fmaxf(fmaxf(s[0], s[1]), fmaxf(s[2], s[3]));
  mx = x32_max(x16_max(mx));
  const float mn = fmaxf(st.m, mx);
  const float alpha = __builtin_amdgcn_exp2f(st.m - mn);
  st.m = mn;
  const float p0 = __builtin_amdgcn_exp2f(s[0] - mn), p1 = __builtin_amdgcn_exp2f(s[1] - mn);
  const float p2 = __builtin_amdgcn_exp2f(s[2] - mn), p3 = __builtin_amdgcn_exp2f(s[3] - mn);
  st.l = fmaf(st.l, alpha, (p0 + p1) + (p2 + p3));
  const v2u pb = {cvtpk(p0, p1), cvtpk(p2, p3)};
#pragma unroll
  for (int e = 0; e < 8; ++e) st.o[e] *= alpha;
  SB();
#pragma unroll
  for (int e = 0; e < 8; ++e) {
    const unsigned sel = (e & 1) ? 0x07060302u : 0x05040100u;
    const v2u a = {__builtin_amdgcn_perm(vc[1][e >> 1], vc[0][e >> 1], sel), __builtin_amdgcn_perm(vc[3][e >> 1], vc[2][e >> 1], sel)};
    st.o[e] = __builtin_amdgcn_mfma_f32_16x16x16bf16_1k(__builtin_bit_cast(bf16x4, a), __builtin_bit_cast(bf16x4, pb), st.o[e], 0, 0, 0);
  }
  SB();
}

#define DRAIN() asm volatile("s_waitcnt vmcnt(0)" ::: "memory")
#define GUWAIT 4
#define HEAD3 1
#define QSTAGE 1
#define BAL 0
#define LNSVC 1
typedef float f32x2v __attribute__((ext_vector_type(2)));
__device__ __forceinline__ void lds_signal(LAS unsigned* c) {
  asm volatile("s_waitcnt lgkmcnt(0)" ::: "memory");
  if ((threadIdx.x & 63) == 0) __hip_atomic_fetch_add(c, 1u, __ATOMIC_RELAXED, __HIP_MEMORY_SCOPE_WORKGROUP);
}
__device__ __forceinline__ void lds_wait(LAS unsigned* c, unsigned want) {
#pragma nounroll
  for (unsigned spins = 0; spins < (SPIN_CAP << 3); ++spins) {
    asm volatile("" ::: "memory");
    if (__builtin_amdgcn_readfirstlane(*(volatile LAS unsigned*)c) >= want) break;
    __builtin_amdgcn_s_sleep(1);
  }
  asm volatile("" ::: "memory");
}

__global__ void __launch_bounds__(NTHR) fused_decode(Params p) {
  __shared__ __attribute__((aligned(16))) unsigned char smem[L_BYTES];
  LAS unsigned char* L = (LAS unsigned char*)smem;
  LAS unsigned* FL = (LAS unsigned*)(L + L_FL);
  const int tid = (int)threadIdx.x - 64, lane = threadIdx.x & 63;
  const int w = __builtin_amdgcn_readfirstlane(threadIdx.x >> 6) - 1;
  const int bid = blockIdx.x;
  const int grp = bid >> 5, xq = bid & 7;
  const int cg = (bid >> 3) & 3, rg = grp * 8 + xq;
  const int hkv = bid >> 6, bb = grp & 1, sp = bid & 31;
  const bool has_ffn = xq != grp;
  const int fi = has_ffn ? grp * 7 + (xq < grp ? xq : xq - 1) : 0;
  const bool reducer = rg >= 48;
  constexpr int OOB = 0x7FFF0000;
  const __amdgpu_buffer_rsrc_t rs = __builtin_amdgcn_make_buffer_rsrc((void*)p.ws, 0, WS_ALL, 0x00020000);
  const int p1own = (bid & 7) * RSP + WS_P1 + (bid >> 3) * 4096;
  const int s2own = (rg & 7) * RSS + (cg * 8 + (rg >> 3)) * 2048;
  const GAS v4u* xg = (const GAS v4u*)p.x;
  STAMP(0);

  if (w < 0) {
    __builtin_amdgcn_s_setprio(3);
    const v4u xa0 = xg[lane * 2], xa1 = xg[lane * 2 + 1], xb0 = xg[128 + lane * 2], xb1 = xg[128 + lane * 2 + 1];
    const v2u ev = __builtin_amdgcn_raw_buffer_load_b64(rs, WS_E, 0, AUX_SC1);
    if (lane < 6) FL[lane] = 0u;
    float rr;
    {
      float va_[16], vb_[16];
#pragma unroll
      for (int k = 0; k < 4; ++k) {
        va_[2 * k] = bflo(xa0[k]); va_[2 * k + 1] = bfhi(xa0[k]); va_[8 + 2 * k] = bflo(xa1[k]); va_[8 + 2 * k + 1] = bfhi(xa1[k]);
        vb_[2 * k] = bflo(xb0[k]); vb_[2 * k + 1] = bfhi(xb0[k]); vb_[8 + 2 * k] = bflo(xb1[k]); vb_[8 + 2 * k + 1] = bfhi(xb1[k]);
      }
      float sa = 0.f, sb = 0.f;
#pragma unroll
      for (int k = 0; k < 16; ++k) { sa += va_[k]; sb += vb_[k]; }
      const float ma = wave_sum(sa) * (1.0f / D), mb = wave_sum(sb) * (1.0f / D);
      if (lane == 0) *(LAS f32x2v*)(L + L_FL + 32) = f32x2v{ma, mb};
      WG_BARRIER();
      float qa = 0.f, qb = 0.f;
#pragma unroll
      for (int k = 0; k < 16; ++k) { const float da = va_[k] - ma, db = vb_[k] - mb; qa = fmaf(da, da, qa); qb = fmaf(db, db, qb); }
      const float ta = wave_sum(qa), tb = wave_sum(qb);
      rr = 1.0f / sqrtf((lane < 32 ? ta : tb) * (1.0f / D) + 1e-5f);
    }
    const u64 e1 = ((((u64)__builtin_amdgcn_readfirstlane(ev.y)) << 32) | (u64)(unsigned)__builtin_amdgcn_readfirstlane(ev.x)) + 1ull;
    const unsigned tag_hi = (unsigned)(e1 >> 8), tag_lo = (unsigned)(e1 & 0xFFull);
    if (lane == 0) { FL[6] = (unsigned)e1; FL[7] = (unsigned)(e1 >> 32); }
    lds_wait(FL + 0, 8);
    {
      f32x2v a = {0.f, 0.f};
#pragma unroll
      for (int ww = 0; ww < 8; ++ww) a += *(LAS f32x2v*)(L + L_RED + ww * 512 + lane * 8);
      const v2u g0 = granule(a.x * rr, tag_lo, tag_hi), g1 = granule(a.y * rr, tag_lo, tag_hi);
      __builtin_amdgcn_raw_buffer_store_b128(v4u{g0.x, g0.y, g1.x, g1.y}, rs,
          ((rg & 15) >> 1) * RSQ + WS_QP + ((hkv * 2 + (lane >> 5)) * 4 + cg) * 1024 + (rg & 1) * 512 + (lane & 31) * 16, 0, AUX_SC1);
    }
    STAMP_S(1);
    DRAIN();
    {
      const int qoff = WS_QP + (hkv * 2 + bb) * 4096 + lane * 16;
      v4u qg[32];
#pragma nounroll
      for (unsigned spins = 0; spins < SPIN_CAP; ++spins) {
        asm volatile("" ::: "memory");
        bool ok = true;
#pragma unroll
        for (int j = 0; j < 32; ++j) qg[j] = __builtin_amdgcn_raw_buffer_load_b128(rs, qoff + (j >> 3) * 1024, (j & 7) * RSQ, AUX_SC1);
#pragma unroll
        for (int j = 0; j < 32; ++j) ok &= tag2(qg[j], tag_lo, tag_hi);
        if (__all(ok)) break;
        __builtin_amdgcn_s_sleep(4);
      }
#pragma unroll
      for (int j = 0; j < 8; ++j) {
        const float q0 = (u2f(qg[j].x << 8) + u2f(qg[8 + j].x << 8)) + (u2f(qg[16 + j].x << 8) + u2f(qg[24 + j].x << 8));
        const float q1 = (u2f(qg[j].z << 8) + u2f(qg[8 + j].z << 8)) + (u2f(qg[16 + j].z << 8) + u2f(qg[24 + j].z << 8));
        const unsigned hi = cvtpk(q0, q1);
        const unsigned lo = cvtpk(q0 - bflo(hi), q1 - bfhi(hi));
        *(LAS unsigned*)(L + L_Q + j * 272 + lane * 4) = hi;
        *(LAS unsigned*)(L + L_Q + (j + 8) * 272 + lane * 4) = lo;
      }
      lds_signal(FL + 3);
    }
    const int coff = ((lane & 15) >> 1) * RSQ + WS_QP + (((lane >> 4) * 2) * 4 + cg) * 1024 + (lane & 1) * 512;
    STAMP_S(2);
    if (reducer) {
#pragma nounroll
      for (unsigned spins = 0; spins < SPIN_CAP; ++spins) {
        asm volatile("" ::: "memory");
        const v4u ck = __builtin_amdgcn_raw_buffer_load_b128(rs, coff, 0, AUX_SC1);
        if (__all(tag2(ck, tag_lo, tag_hi))) break;
        __builtin_amdgcn_s_sleep(4);
      }
    }
    if (has_ffn) {
      lds_wait(FL + 1, 8);
      v4u gr[2];
#pragma unroll
      for (int m = 0; m < 2; ++m) {
        f32x2v a = {0.f, 0.f};
#pragma unroll
        for (int ww = 0; ww < 8; ++ww) a += *(LAS f32x2v*)(L + L_RED + 4096 + m * 4096 + ww * 512 + lane * 8);
        const v2u g0 = granule(a.x * rr, tag_lo, tag_hi), g1 = granule(a.y * rr, tag_lo, tag_hi);
        gr[m] = v4u{g0.x, g0.y, g1.x, g1.y};
      }
      lds_wait(FL + 2, GUWAIT);
#pragma unroll
      for (int m = 0; m < 2; ++m) __builtin_amdgcn_raw_buffer_store_b128(gr[m], rs, (cg * 2 + m) * RSG + WS_GU + rg * 1024 + lane * 16, 0, AUX_SC1);
    }
    STAMP_S(4);
    WG_BARRIER();
    return;
  }

  {
    const v4u s4 = {SENT, SENT, SENT, SENT};
    const int off = w < 4 ? p1own + w * 1024 : (w < 6 ? WS_S2 - 4096 : WS_F2 - 6144) + s2own + w * 1024;
    __builtin_amdgcn_raw_buffer_store_b128(s4, rs, off + lane * 16, 0, AUX_SC1);
    SB();
  }
  const int r8 = lane >> 3, c8 = lane & 7;
  const int krow = cg * 256 + w * 32 + r8 * 4;
  const v2u xr0 = *(const GAS v2u*)(p.x + krow), xr1 = *(const GAS v2u*)(p.x + D + krow);
  v4u wqr[4], wgr[4], wur[4], wdr[4], wor[4];
#pragma unroll
  for (int i = 0; i < 4; ++i) wqr[i] = ldnt((const GAS v4u*)(p.wq + (size_t)(krow + i) * HD + rg * 64 + c8 * 8));
  SB();
  const int fbytes = has_ffn ? D * FF * 2 : 0;
  const __amdgpu_buffer_rsrc_t rwg = __builtin_amdgcn_make_buffer_rsrc((void*)p.wg, 0, fbytes, 0x00020000);
  const __amdgpu_buffer_rsrc_t rwu = __builtin_amdgcn_make_buffer_rsrc((void*)p.wu, 0, fbytes, 0x00020000);
  const __amdgpu_buffer_rsrc_t rwd = __builtin_amdgcn_make_buffer_rsrc((void*)p.wd, 0, fbytes, 0x00020000);
  const __amdgpu_buffer_rsrc_t rwo = __builtin_amdgcn_make_buffer_rsrc((void*)p.wo, 0, HD * D * 2, 0x00020000);
  const int guoff = (krow * FF + fi * 64 + c8 * 8) * 2;
#pragma unroll
  for (int i = 0; i < 4; ++i) wgr[i] = __builtin_amdgcn_raw_buffer_load_b128(rwg, guoff + i * FF * 2, 0, AUX_NT);
  SB();
  const int tile = ((w * 8 + (lane >> 5)) * D + cg * 256 + (lane & 31) * 8) * 2;
  const int i16 = lane & 15, g4 = lane >> 4;
  const __amdgpu_buffer_rsrc_t rk = __builtin_amdgcn_make_buffer_rsrc((void*)p.kc, 0, 8 * SK * 256, 0x00020000);
  const __amdgpu_buffer_rsrc_t rv = __builtin_amdgcn_make_buffer_rsrc((void*)p.vc, 0, 8 * SK * 256, 0x00020000);
  const int klane = (4 * (i16 & 3) + (i16 >> 2)) * 256 + g4 * 16, vlane = lane * 16;
  const int fj = xq < grp ? xq : xq - 1;
  const int nunit = has_ffn ? 64 - BAL / 7 - (fj < BAL % 7 ? 1 : 0) : 64 + BAL;
  const int obase = ((bb * 4 + hkv) * 2048 + cg * 512) * 4096;
  const int ubase = obase + xq * 262144;
#define UOFF(t_) (ubase + (t_) * 4096)
#define UOFFX(t_) ((t_) < 64 ? UOFF(t_) : obase + ((((t_) - 64) % 7) + ((((t_) - 64) % 7) >= grp ? 1 : 0)) * 262144 + (63 - ((t_) - 64) / 7) * 4096)
  v4u ka[4], va[4], kb[4], vb[4], kc[4], vc[4];
  WG_BARRIER();
  float x0[4], x1[4];
  {
    const f32x2v mm = *(LAS f32x2v*)(L + L_FL + 32);
    x0[0] = bflo(xr0.x) - mm.x; x0[1] = bfhi(xr0.x) - mm.x; x0[2] = bflo(xr0.y) - mm.x; x0[3] = bfhi(xr0.y) - mm.x;
    x1[0] = bflo(xr1.x) - mm.y; x1[1] = bfhi(xr1.x) - mm.y; x1[2] = bflo(xr1.y) - mm.y; x1[3] = bfhi(xr1.y) - mm.y;
  }
  const int redoff = ((g4 & 1) * 64 + c8 * 8 + (g4 >> 1) * 4) * 4;

  {
    const f32x4 r = gemv64(wqr, x0, x1);
    if ((lane & 8) == 0) *(LAS f32x4*)(L + L_RED + w * 512 + redoff) = r;
  }
  lds_signal(FL + 0);
#pragma unroll
  for (int i = 0; i < 4; ++i) wur[i] = __builtin_amdgcn_raw_buffer_load_b128(rwu, guoff + i * FF * 2, 0, AUX_NT);
  SB();
  {
    const f32x4 r = gemv64(wgr, x0, x1);
    if ((lane & 8) == 0) *(LAS f32x4*)(L + L_RED + 4096 + w * 512 + redoff) = r;
  }
#pragma unroll
  for (int i = 0; i < 4; ++i) wdr[i] = __builtin_amdgcn_raw_buffer_load_b128(rwd, tile + i * 4 * D, fi * 64 * D * 2, AUX_NT);
  SB();
  {
    const f32x4 r = gemv64(wur, x0, x1);
    if ((lane & 8) == 0) *(LAS f32x4*)(L + L_RED + 8192 + w * 512 + redoff) = r;
  }
  lds_signal(FL + 1);
#pragma unroll
  for (int i = 0; i < 4; ++i) wor[i] = __builtin_amdgcn_raw_buffer_load_b128(rwo, tile + i * 4 * D, rg * 64 * D * 2, AUX_NT);
  SB();
#pragma unroll
  for (int i = 0; i < 4; ++i) *(LAS v4u*)(L + L_WD + (w * 4 + i) * 1024 + lane * 16) = wdr[i];
  SB();
  LDK(ka, UOFF(w));
#pragma unroll
  for (int i = 0; i < 4; ++i) *(LAS v4u*)(L + L_WO + (w * 4 + i) * 1024 + lane * 16) = wor[i];
  SB();
  LDV(va, UOFF(w));
  TOUCH4(ka);
  LDK(kb, UOFF(w + 8));
  TOUCH4(va);
  LDV(vb, UOFF(w + 8));
  TOUCH4(kb);
  LDK(kc, UOFF(w + 16));
  TOUCH4(vb);
  LDV(vc, UOFF(w + 16));
  lds_wait(FL + 3, 1);
  v4u qf[4];
#pragma unroll
  for (int kk = 0; kk < 4; ++kk) qf[kk] = *(LAS v4u*)(L + L_Q + i16 * 272 + kk * 64 + g4 * 16);
  STAMP(3);

  AttnState st;
  st.m = -INFINITY;
  st.l = 0.f;
#pragma unroll
  for (int e = 0; e < 8; ++e) st.o[e] = f32x4{0.f, 0.f, 0.f, 0.f};
  attn_unit(st, ka, va, qf);
  attn_unit(st, kb, vb, qf);
#define PA_K kc
#define PA_V vc
#define PB_K kb
#define PB_V vb
  int t = w + 24;
  for (;;) {
    const int o0 = t < nunit ? UOFFX(t) : OOB;
    const int ak0 = klane + o0, av0 = vlane + o0;
    LDK2(PB_K, ak0);
    LDV2(PB_V, av0);
    asm volatile("" ::"v"(ak0), "v"(av0));
    attn_unit(st, PA_K, PA_V, qf);
    if (t >= nunit) break;
    const int o1 = t + 8 < nunit ? UOFFX(t + 8) : OOB;
    const int ak1 = klane + o1, av1 = vlane + o1;
    LDK2(PA_K, ak1);
    LDV2(PA_V, av1);
    asm volatile("" ::"v"(ak1), "v"(av1));
    attn_unit(st, PB_K, PB_V, qf);
    if (t + 8 >= nunit) break;
    t += 16;
  }
  lds_signal(FL + 2);
  STAMP(5);
  const int sfoff = ((tid >> 2) & 1) * 1024 + ((tid & 127) >> 3) * 64 + (tid & 3) * 16;
  const int oidx = ((tid >> 4) & 1) * D + cg * 256 + (rg & 15) * 16 + (tid & 15);
  unsigned xo = 0;
  if (w == 0) xo = *(const GAS unsigned short*)(p.x + oidx);

  {
    const float l = x32_sum(x16_sum(st.l));
    if (i16 < 8) {
#pragma unroll
      for (int e = 0; e < 8; ++e) *(LAS f32x4*)(L + L_O + (w * 256 + (i16 * 4 + g4) * 8 + (e ^ i16)) * 16) = st.o[e];
      if (g4 == 0) *(LAS v2u*)(L + L_ML + (i16 * 8 + w) * 8) = v2u{f2u(st.m), f2u(l)};
    }
  }
  int poff = g4 * RSP + WS_P1 + (hkv * 8 + w) * 4096 + (rg & 15) * 256 + i16 * 16;
  int goff = (lane >> 3) * RSG + WS_GU + rg * 1024 + ((lane >> 2) & 1) * 512 + (w * 8 + (lane & 3) * 2) * 8;
  int roff = ((tid >> 3) & 7) * RSS + (cg * 8 + (tid >> 6)) * 2048 + (rg & 15) * 128 + (tid & 7) * 16;
  int p1st = p1own + tid * 16, s2st = (w < 2 ? WS_S2 : WS_F2) + s2own + (tid & 127) * 16;
  __builtin_amdgcn_s_waitcnt(0x0F70);
  asm volatile("" : "+v"(poff), "+v"(goff), "+v"(roff), "+v"(p1st), "+v"(s2st));
  WG_BARRIER();
  if (w < 4) {
    const int j = tid >> 5;
    v4u ml[4];
    f32x4 ov[8];
#pragma unroll
    for (int k = 0; k < 4; ++k) ml[k] = *(LAS v4u*)(L + L_ML + j * 64 + k * 16);
#pragma unroll
    for (int ww = 0; ww < 8; ++ww) ov[ww] = *(LAS f32x4*)(L + L_O + (ww * 256 + (tid ^ j)) * 16);
    SB();
    float M = fmaxf(u2f(ml[0].x), u2f(ml[0].z));
#pragma unroll
    for (int k = 1; k < 4; ++k) M = fmaxf(M, fmaxf(u2f(ml[k].x), u2f(ml[k].z)));
    float Ls = 0.f;
    f32x4 acc = {0.f, 0.f, 0.f, 0.f};
#pragma unroll
    for (int ww = 0; ww < 8; ++ww) {
      const float wt = __builtin_amdgcn_exp2f(u2f(ml[ww >> 1][(ww & 1) * 2]) - M);
      Ls = fmaf(wt, u2f(ml[ww >> 1][(ww & 1) * 2 + 1]), Ls);
      acc += ov[ww] * wt;
    }
    acc *= rcp(Ls);
    const unsigned lse = f2u(M + __builtin_amdgcn_logf(Ls));
    v4u pk;
#pragma unroll
    for (int r = 0; r < 4; ++r) {
      const unsigned t = ((f2u(acc[r]) + 0x80u) & 0xFFFFFF00u) | ((lse >> (8 * r)) & 0xFFu);
      pk[r] = t == SENT ? 0x7FC000FFu : t;
    }
    __builtin_amdgcn_raw_buffer_store_b128(pk, rs, p1st, 0, AUX_SC1);
  }
  STAMP(6);
  SB();

  f32x4 wof[8];
  {
    v4u gu = __builtin_amdgcn_raw_buffer_load_b128(rs, goff, 0, AUX_SC1);
    v4u pa = __builtin_amdgcn_raw_buffer_load_b128(rs, poff, 0, AUX_SC1);
    v4u pb = __builtin_amdgcn_raw_buffer_load_b128(rs, poff, 4 * RSP, AUX_SC1);
    SB();
    if (has_ffn) {
      const unsigned e_lo = FL[6], e_hi = FL[7];
      const unsigned tag_hi = (e_lo >> 8) | (e_hi << 24), tag_lo = e_lo & 0xFFu;
      v2u wt[8];
#pragma unroll
      for (int k = 0; k < 8; ++k) wt[k] = *(LAS v2u*)(L + L_WD + (w * 8 + k) * 512 + lane * 8);
      if (!__all(tag2(gu, tag_lo, tag_hi))) {
#pragma nounroll
        for (unsigned spins = 0; spins < SPIN_CAP; ++spins) {
          asm volatile("" ::: "memory");
          __builtin_amdgcn_s_sleep(1);
          gu = __builtin_amdgcn_raw_buffer_load_b128(rs, goff, 0, AUX_SC1);
          if (__all(tag2(gu, tag_lo, tag_hi))) break;
        }
      }
      const float ga = x32_sum(x16_sum(u2f(gu.x << 8))), gb = x32_sum(x16_sum(u2f(gu.z << 8)));
      const float hx = silu(ga) * dpp<XOR8>(ga), hy = silu(gb) * dpp<XOR8>(gb);
      float h0[8], h1[8];
#pragma unroll
      for (int pr = 0; pr < 4; ++pr) {
        h0[2 * pr] = rdl(hx, pr); h0[2 * pr + 1] = rdl(hy, pr);
        h1[2 * pr] = rdl(hx, 4 + pr); h1[2 * pr + 1] = rdl(hy, 4 + pr);
      }
      f32x4 s0, s1;
      gemv256(wt, h0, h1, s0, s1);
      *(LAS f32x4*)(L + L_F + w * 2048 + lane * 16) = s0;
      *(LAS f32x4*)(L + L_F + w * 2048 + 1024 + lane * 16) = s1;
    }
#pragma unroll
    for (int k = 0; k < 8; ++k) {
      const v2u t = *(LAS v2u*)(L + L_WO + (w * 8 + k) * 512 + lane * 8);
      wof[k] = f32x4{bflo(t.x), bfhi(t.x), bflo(t.y), bfhi(t.y)};
      asm volatile("" : "+v"(wof[k]));
    }
    bool ha = full4(pa), hb = full4(pb);
#pragma nounroll
    for (unsigned spins = 0; spins < SPIN_CAP; ++spins) {
      asm volatile("" ::: "memory");
      if (__all(ha & hb)) break;
      if (!ha) pa = __builtin_amdgcn_raw_buffer_load_b128(rs, poff, 0, AUX_SC1);
      if (!hb) pb = __builtin_amdgcn_raw_buffer_load_b128(rs, poff, 4 * RSP, AUX_SC1);
      ha = full4(pa);
      hb = full4(pb);
    }
    const float la = u2f(__builtin_amdgcn_perm(__builtin_amdgcn_perm(pa.w, pa.z, 0x0c0c0400u), __builtin_amdgcn_perm(pa.y, pa.x, 0x0c0c0400u), 0x05040100u));
    const float lb = u2f(__builtin_amdgcn_perm(__builtin_amdgcn_perm(pb.w, pb.z, 0x0c0c0400u), __builtin_amdgcn_perm(pb.y, pb.x, 0x0c0c0400u), 0x05040100u));
    const float Mw = x32_max(x16_max(fmaxf(la, lb)));
    const float wa = __builtin_amdgcn_exp2f(la - Mw), wb = __builtin_amdgcn_exp2f(lb - Mw);
    const float Ww = x32_sum(x16_sum(wa + wb));
    float a[4];
#pragma unroll
    for (int r = 0; r < 4; ++r) a[r] = fmaf(u2f(pa[r] & 0xFFFFFF00u), wa, u2f(pb[r] & 0xFFFFFF00u) * wb);
    const float v = tr32(tr16(a[0], a[1]), tr16(a[2], a[3]));
    *(LAS float*)(L + L_C + (((w >> 2) * 64 + (i16 >> 3) * 32 + g4 * 8 + (i16 & 7)) * 4 + (w & 3)) * 4) = v;
    if (lane == 0) *(LAS v2u*)(L + L_C + 2048 + w * 8) = v2u{f2u(Mw), f2u(Ww)};
  }
  STAMP(7);
  WG_BARRIER();
  {
    const int b = (lane >> 3) & 1, k = lane & 7;
    const v4u m0 = *(LAS v4u*)(L + L_C + 2048 + b * 32), m1 = *(LAS v4u*)(L + L_C + 2048 + b * 32 + 16);
    const f32x4 av = *(LAS f32x4*)(L + L_C + (b * 64 + w * 8 + k) * 16);
    const float M = fmaxf(fmaxf(u2f(m0.x), u2f(m0.z)), fmaxf(u2f(m1.x), u2f(m1.z)));
    const float f0 = __builtin_amdgcn_exp2f(u2f(m0.x) - M), f1 = __builtin_amdgcn_exp2f(u2f(m0.z) - M);
    const float f2 = __builtin_amdgcn_exp2f(u2f(m1.x) - M), f3 = __builtin_amdgcn_exp2f(u2f(m1.z) - M);
    const float num = fmaf(f0, av.x, f1 * av.y) + fmaf(f2, av.z, f3 * av.w);
    const float den = fmaf(f0, u2f(m0.y), f1 * u2f(m0.w)) + fmaf(f2, u2f(m1.y), f3 * u2f(m1.w));
    const float ov = num * rcp(den);
    f32x4 s0 = {0.f, 0.f, 0.f, 0.f}, s1 = {0.f, 0.f, 0.f, 0.f};
#pragma unroll
    for (int kk = 0; kk < 8; ++kk) { s0 += wof[kk] * rdl(ov, kk); s1 += wof[kk] * rdl(ov, 8 + kk); }
    *(LAS f32x4*)(L + L_S + w * 2048 + lane * 16) = s0;
    *(LAS f32x4*)(L + L_S + w * 2048 + 1024 + lane * 16) = s1;
  }
  WG_BARRIER();
  if (w < 2 || (w < 4 && has_ffn)) {
    const int lb = (w < 2 ? L_S : L_F) + sfoff;
    f32x4 t[8];
#pragma unroll
    for (int ww = 0; ww < 8; ++ww) t[ww] = *(LAS f32x4*)(L + lb + ww * 2048);
    SB();
    const f32x4 s4 = ((t[0] + t[1]) + (t[2] + t[3])) + ((t[4] + t[5]) + (t[6] + t[7]));
    __builtin_amdgcn_raw_buffer_store_b128(clean4(s4), rs, s2st, 0, AUX_SC1);
  }
  STAMP(8);

  if (reducer) {
    v4u a = __builtin_amdgcn_raw_buffer_load_b128(rs, roff, WS_S2, AUX_SC1);
    v4u c = {0u, 0u, 0u, 0u};
    if (((tid >> 3) & 7) != w) c = __builtin_amdgcn_raw_buffer_load_b128(rs, roff, WS_F2, AUX_SC1);
    bool ha = full4(a), hc = full4(c);
#pragma nounroll
    for (unsigned spins = 0; spins < SPIN_CAP; ++spins) {
      asm volatile("" ::: "memory");
      if (__all(ha & hc)) break;
      if (!ha) a = __builtin_amdgcn_raw_buffer_load_b128(rs, roff, WS_S2, AUX_SC1);
      if (!hc) c = __builtin_amdgcn_raw_buffer_load_b128(rs, roff, WS_F2, AUX_SC1);
      ha = full4(a);
      hc = full4(c);
    }
    float vs = tr32(tr16(u2f(a.x), u2f(a.y)), tr16(u2f(a.z), u2f(a.w)));
    float vf = tr32(tr16(u2f(c.x), u2f(c.y)), tr16(u2f(c.z), u2f(c.w)));
    vs += dpp<XOR8>(vs);
    vf += dpp<XOR8>(vf);
    *(LAS float*)(L + L_R + ((lane >> 3) & 1) * 1024 + ((lane & 7) * 4 + (lane >> 4)) * 32 + w * 4) = (lane & 8) ? vs : vf;
    WG_BARRIER();
    if (tid < 32) {
      const f32x4 f0 = *(LAS f32x4*)(L + L_R + tid * 32), f1 = *(LAS f32x4*)(L + L_R + tid * 32 + 16);
      const f32x4 s0 = *(LAS f32x4*)(L + L_R + 1024 + tid * 32), s1 = *(LAS f32x4*)(L + L_R + 1024 + tid * 32 + 16);
      const f32x4 f4 = f0 + f1, s4 = s0 + s1;
      const float f = (f4.x + f4.y) + (f4.z + f4.w), sv = (s4.x + s4.y) + (s4.z + s4.w);
      const float r = fmaf(sv, silu(f), bflo(xo));
      p.out[oidx] = (unsigned short)(cvtpk(r, 0.f) & 0xFFFFu);
    }
    if (bid == NWG - 1 && tid == 0) {
      const v2u ne = {FL[6], FL[7]};
      __builtin_amdgcn_raw_buffer_store_b64(ne, rs, WS_E, 0, AUX_SC1);
    }
  }
  STAMP(9);
}

}

extern "C" void kernel_launch(void* const* d_in, const int* in_sizes, int n_in, void* d_out, int out_size, void* d_ws,
                              size_t ws_size, hipStream_t stream) {
  Params p;
  p.x = (const unsigned short*)d_in[0];
  p.kc = (const unsigned short*)d_in[1];
  p.vc = (const unsigned short*)d_in[2];
  p.wq = (const unsigned short*)d_in[3];
  p.wo = (const unsigned short*)d_in[4];
  p.wg = (const unsigned short*)d_in[6];
  p.wu = (const unsigned short*)d_in[7];
  p.wd = (const unsigned short*)d_in[8];
  p.out = (unsigned short*)d_out;
  p.ws = (unsigned char*)d_ws;
  hipLaunchKernelGGL(fused_decode, dim3(NWG), dim3(NTHR), 0, stream, p);
}
```

```cpp
#define DIAG 0
#include <hip/hip_runtime.h>
#include <hip/hip_bf16.h>
#include <stdint.h>

#define GAS __attribute__((address_space(1)))
#define LAS __attribute__((address_space(3)))
typedef unsigned v4u __attribute__((ext_vector_type(4)));
typedef unsigned v2u __attribute__((ext_vector_type(2)));
typedef float f32x4 __attribute__((ext_vector_type(4)));
typedef short bf16x8 __attribute__((ext_vector_type(8)));
typedef short bf16x4 __attribute__((ext_vector_type(4)));
typedef unsigned long long u64;

namespace {

constexpr int NWG = 256, NTHR = 576;
constexpr int D = 1024, HD = 4096, FF = 3584, SK = 32768;
constexpr int AUX_SC1 = 16;
constexpr unsigned SENT = 0xFFFFFFFFu;
constexpr unsigned SPIN_CAP = 1u << 22;

#define COMPACT 1
constexpr int RSQ = 32768, RSG = 65536, RSP = 131072, RSS = 65536;
constexpr int WS_E = 0;
constexpr int WS_QP = 4096;
constexpr int WS_GU = WS_QP + 8 * RSQ;
constexpr int WS_P1 = WS_GU + 8 * RSG;
constexpr int WS_S2 = WS_P1 + 8 * RSP;
constexpr int WS_F2 = WS_S2 + 8 * RSS;
constexpr int WS_QF = WS_F2 + 8 * RSS;
constexpr int WS_END = WS_QF + 65536;
constexpr int WS_ALL = WS_END;

constexpr int L_RED = 0;
constexpr int L_Q = 12288;
constexpr int L_ML = L_Q + 16 * 272;
constexpr int L_O = L_ML + 512;
constexpr int L_S = L_O + 32768;
constexpr int L_C = L_S + 16384;
constexpr int L_R = L_C + 2048 + 64;
constexpr int L_H = L_R + 2048;
constexpr int L_FL = L_H + 512;
constexpr int L_WO = L_FL + 64;
constexpr int L_WD = L_WO + 32768;
constexpr int L_F = L_WD + 32768;
constexpr int L_USED = L_F + 16384;
constexpr int L_BYTES = 152 * 1024;
static_assert(L_USED <= L_BYTES, "lds");

struct Params {
  const unsigned short* x;
  const unsigned short* wq;
  unsigned char* ws;
  const unsigned short* wg;
  const unsigned short* wu;
  const unsigned short* wd;
  const unsigned short* wo;
  const unsigned short* kc;
  const unsigned short* vc;
  unsigned short* out;
};

__device__ __forceinline__ float bflo(unsigned u) { return __builtin_bit_cast(float, u << 16); }
__device__ __forceinline__ float bfhi(unsigned u) { return __builtin_bit_cast(float, u & 0xFFFF0000u); }
__device__ __forceinline__ unsigned f2u(float f) { return __builtin_bit_cast(unsigned, f); }
__device__ __forceinline__ float u2f(unsigned u) { return __builtin_bit_cast(float, u); }
typedef __bf16 bf16p __attribute__((ext_vector_type(2)));
typedef float f32x2 __attribute__((ext_vector_type(2)));
__device__ __forceinline__ unsigned cvtpk(float lo, float hi) {
  return __builtin_bit_cast(unsigned, __builtin_convertvector(f32x2{lo, hi}, bf16p));
}
template <int CTRL> __device__ __forceinline__ float dpp(float x) {
  return __builtin_bit_cast(float, __builtin_amdgcn_mov_dpp(__builtin_bit_cast(int, x), CTRL, 0xf, 0xf, true));
}
constexpr int XOR1 = 0xB1, XOR2 = 0x4E, XOR7 = 0x141, XOR8 = 0x128, ROR4 = 0x124, XOR15 = 0x140;
__device__ __forceinline__ float x16_sum(float x) {
  auto s = __builtin_amdgcn_permlane16_swap(f2u(x), f2u(x), false, false);
  return u2f(s[0]) + u2f(s[1]);
}
__device__ __forceinline__ float x32_sum(float x) {
  auto s = __builtin_amdgcn_permlane32_swap(f2u(x), f2u(x), false, false);
  return u2f(s[0]) + u2f(s[1]);
}
__device__ __forceinline__ float x16_max(float x) {
  auto s = __builtin_amdgcn_permlane16_swap(f2u(x), f2u(x), false, false);
  return fmaxf(u2f(s[0]), u2f(s[1]));
}
__device__ __forceinline__ float x32_max(float x) {
  auto s = __builtin_amdgcn_permlane32_swap(f2u(x), f2u(x), false, false);
  return fmaxf(u2f(s[0]), u2f(s[1]));
}
__device__ __forceinline__ float row16_sum(float x) {
  x += dpp<XOR1>(x);
  x += dpp<XOR2>(x);
  x += dpp<XOR7>(x);
  x += dpp<XOR8>(x);
  return x;
}
__device__ __forceinline__ float row16_max(float x) {
  x = fmaxf(x, dpp<XOR1>(x));
  x = fmaxf(x, dpp<XOR2>(x));
  x = fmaxf(x, dpp<XOR7>(x));
  x = fmaxf(x, dpp<XOR8>(x));
  return x;
}
__device__ __forceinline__ float wave_sum(float x) { return x32_sum(x16_sum(row16_sum(x))); }
__device__ __forceinline__ float tr16(float a, float b) {
  auto s = __builtin_amdgcn_permlane16_swap(f2u(a), f2u(b), false, false);
  return u2f(s[0]) + u2f(s[1]);
}
__device__ __forceinline__ float tr32(float a, float b) {
  auto s = __builtin_amdgcn_permlane32_swap(f2u(a), f2u(b), false, false);
  return u2f(s[0]) + u2f(s[1]);
}
__device__ __forceinline__ v4u ldnt(const GAS v4u* p) { return __builtin_nontemporal_load(p); }
__device__ __forceinline__ v2u ldnt2(const GAS v2u* p) { return __builtin_nontemporal_load(p); }
__device__ __forceinline__ unsigned clean(float f) {
  const unsigned u = f2u(f);
  return u == SENT ? 0x7FC00000u : u;
}
__device__ __forceinline__ v4u clean4(f32x4 v) { return v4u{clean(v.x), clean(v.y), clean(v.z), clean(v.w)}; }
__device__ __forceinline__ bool full4(v4u v) { return v.x != SENT && v.y != SENT && v.z != SENT && v.w != SENT; }
__device__ __forceinline__ float rdl(float v, int lane) { return u2f(__builtin_amdgcn_readlane(f2u(v), lane)); }
__device__ __forceinline__ float rcp(float v) { return __builtin_amdgcn_rcpf(v); }
__device__ __forceinline__ float silu(float v) { return v * rcp(1.0f + __builtin_amdgcn_exp2f(v * -1.4426950408889634f)); }

#define SB() __builtin_amdgcn_sched_barrier(0)
#define WG_BARRIER()                                        \
  do {                                                      \
    asm volatile("s_waitcnt lgkmcnt(0)" ::: "memory");      \
    __builtin_amdgcn_s_barrier();                           \
    asm volatile("" ::: "memory");                          \
  } while (0)
#define STAMP(i) do { if ((i) == 0) tk0 = __builtin_amdgcn_s_memrealtime(); if ((i) == DIAG) tk1 = __builtin_amdgcn_s_memrealtime(); } while (0)
#define STAMP_S(i)

__device__ __forceinline__ f32x4 gemv64(const v4u (&w)[4], const float (&x0)[4], const float (&x1)[4]) {
  float a0[8], a1[8];
#pragma unroll
  for (int c = 0; c < 8; ++c) { a0[c] = 0.f; a1[c] = 0.f; }
#pragma unroll
  for (int i = 0; i < 4; ++i) {
#pragma unroll
    for (int k = 0; k < 4; ++k) {
      const float lo = bflo(w[i][k]), hi = bfhi(w[i][k]);
      a0[2 * k] = fmaf(lo, x0[i], a0[2 * k]);
      a0[2 * k + 1] = fmaf(hi, x0[i], a0[2 * k + 1]);
      a1[2 * k] = fmaf(lo, x1[i], a1[2 * k]);
      a1[2 * k + 1] = fmaf(hi, x1[i], a1[2 * k + 1]);
    }
  }
  float t[8];
#pragma unroll
  for (int c = 0; c < 8; ++c) t[c] = tr16(a0[c], a1[c]);
  f32x4 r;
#pragma unroll
  for (int c = 0; c < 4; ++c) {
    float v = tr32(t[c], t[c + 4]);
    v += dpp<XOR8>(v);
    r[c] = v;
  }
  return r;
}

__device__ __forceinline__ void gemv256(const v2u (&w)[8], const float (&c0)[8], const float (&c1)[8], f32x4& s0, f32x4& s1) {
  s0 = f32x4{0.f, 0.f, 0.f, 0.f};
  s1 = f32x4{0.f, 0.f, 0.f, 0.f};
#pragma unroll
  for (int k = 0; k < 8; ++k) {
    const f32x4 wf = {bflo(w[k].x), bfhi(w[k].x), bflo(w[k].y), bfhi(w[k].y)};
#pragma unroll
    for (int c = 0; c < 4; ++c) {
      s0[c] = fmaf(wf[c], c0[k], s0[c]);
      s1[c] = fmaf(wf[c], c1[k], s1[c]);
    }
  }
}

__device__ __forceinline__ v2u granule(float v, unsigned tag_lo, unsigned tag_hi) {
  return v2u{(tag_lo << 24) | ((f2u(v) + 0x80u) >> 8), tag_hi};
}
__device__ __forceinline__ bool tag2(v4u g, unsigned tag_lo, unsigned tag_hi) {
  return (g.y == tag_hi) & ((g.x >> 24) == tag_lo) & (g.w == tag_hi) & ((g.z >> 24) == tag_lo);
}
#define TOUCH4(a) asm volatile("" : "+v"(a[0]), "+v"(a[1]), "+v"(a[2]), "+v"(a[3]))
#define TOUCH8(a) asm volatile("" : "+v"(a[0]), "+v"(a[1]), "+v"(a[2]), "+v"(a[3]), "+v"(a[4]), "+v"(a[5]), "+v"(a[6]), "+v"(a[7]))
constexpr int AUX_NT = 2;
#define LDK(kb_, off_) do { _Pragma("unroll") for (int j = 0; j < 4; ++j) kb_[j] = __builtin_amdgcn_raw_buffer_load_b128(rk, (klane + (off_)) + j * 64, 0, AUX_NT); SB(); } while (0)
#define LDV(vb_, off_) do { _Pragma("unroll") for (int j = 0; j < 4; ++j) vb_[j] = __builtin_amdgcn_raw_buffer_load_b128(rv, (vlane + (off_)) + j * 1024, 0, AUX_NT); SB(); } while (0)
#define LDK2(kb_, a_) do { _Pragma("unroll") for (int j = 0; j < 4; ++j) kb_[j] = __builtin_amdgcn_raw_buffer_load_b128(rk, (a_) + j * 64, 0, AUX_NT); SB(); } while (0)
#define LDV2(vb_, a_) do { _Pragma("unroll") for (int j = 0; j < 4; ++j) vb_[j] = __builtin_amdgcn_raw_buffer_load_b128(rv, (a_) + j * 1024, 0, AUX_NT); SB(); } while (0)

struct AttnState {
  float m, l;
  f32x4 o[8];
};
__device__ __forceinline__ void attn_unit(AttnState& st, v4u (&kc)[4], v4u (&vc)[4], const v4u (&qf)[4]) {
  constexpr float C2 = 0.088388347648318440f * 1.4426950408889634f;
  f32x4 s = {0.f, 0.f, 0.f, 0.f};
#pragma unroll
  for (int kk = 0; kk < 4; ++kk)
    s = __builtin_amdgcn_mfma_f32_16x16x32_bf16(__builtin_bit_cast(bf16x8, kc[kk]), __builtin_bit_cast(bf16x8, qf[kk]), s, 0, 0, 0);
#pragma unroll
  for (int r = 0; r < 4; ++r) s[r] = (s[r] + dpp<XOR8>(s[r])) * C2;
  float mx = fmaxf(fmaxf(s[0], s[1]), fmaxf(s[2], s[3]));
  mx = x32_max(x16_max(mx));
  const float mn = fmaxf(st.m, mx);
  const float alpha = __builtin_amdgcn_exp2f(st.m - mn);
  st.m = mn;
  const float p0 = __builtin_amdgcn_exp2f(s[0] - mn), p1 = __builtin_amdgcn_exp2f(s[1] - mn);
  const float p2 = __builtin_amdgcn_exp2f(s[2] - mn), p3 = __builtin_amdgcn_exp2f(s[3] - mn);
  st.l = fmaf(st.l, alpha, (p0 + p1) + (p2 + p3));
  const v2u pb = {cvtpk(p0, p1), cvtpk(p2, p3)};
#pragma unroll
  for (int e = 0; e < 8; ++e) st.o[e] *= alpha;
  SB();
#pragma unroll
  for (int e = 0; e < 8; ++e) {
    const unsigned sel = (e & 1) ? 0x07060302u : 0x05040100u;
    const v2u a = {__builtin_amdgcn_perm(vc[1][e >> 1], vc[0][e >> 1], sel), __builtin_amdgcn_perm(vc[3][e >> 1], vc[2][e >> 1], sel)};
    st.o[e] = __builtin_amdgcn_mfma_f32_16x16x16bf16_1k(__builtin_bit_cast(bf16x4, a), __builtin_bit_cast(bf16x4, pb), st.o[e], 0, 0, 0);
  }
  SB();
}

#define DRAIN() asm volatile("s_waitcnt vmcnt(0)" ::: "memory")
#define GUWAIT 4
#define HEAD3 1
#define QSTAGE 1
#define BAL 0
#define LNSVC 1
typedef float f32x2v __attribute__((ext_vector_type(2)));
__device__ __forceinline__ void lds_signal(LAS unsigned* c) {
  asm volatile("s_waitcnt lgkmcnt(0)" ::: "memory");
  if ((threadIdx.x & 63) == 0) __hip_atomic_fetch_add(c, 1u, __ATOMIC_RELAXED, __HIP_MEMORY_SCOPE_WORKGROUP);
}
__device__ __forceinline__ void lds_wait(LAS unsigned* c, unsigned want) {
#pragma nounroll
  for (unsigned spins = 0; spins < (SPIN_CAP << 3); ++spins) {
    asm volatile("" ::: "memory");
    if (__builtin_amdgcn_readfirstlane(*(volatile LAS unsigned*)c) >= want) break;
    __builtin_amdgcn_s_sleep(1);
  }
  asm volatile("" ::: "memory");
}

__global__ void __launch_bounds__(NTHR) fused_decode(Params p) {
  __shared__ __attribute__((aligned(16))) unsigned char smem[L_BYTES];
  LAS unsigned char* L = (LAS unsigned char*)smem;
  LAS unsigned* FL = (LAS unsigned*)(L + L_FL);
  const int tid = (int)threadIdx.x - 64, lane = threadIdx.x & 63;
  const int w = __builtin_amdgcn_readfirstlane(threadIdx.x >> 6) - 1;
  const int bid = blockIdx.x;
  const int grp = bid >> 5, xq = bid & 7;
  const int cg = (bid >> 3) & 3, rg = grp * 8 + xq;
  const int hkv = bid >> 6, bb = grp & 1, sp = bid & 31;
  const bool has_ffn = xq != grp;
  const int fi = has_ffn ? grp * 7 + (xq < grp ? xq : xq - 1) : 0;
  const bool reducer = rg >= 48;
  constexpr int OOB = 0x7FFF0000;
  const __amdgpu_buffer_rsrc_t rs = __builtin_amdgcn_make_buffer_rsrc((void*)p.ws, 0, WS_ALL, 0x00020000);
  const int p1own = (bid & 7) * RSP + WS_P1 + (bid >> 3) * 4096;
  const int s2own = (rg & 7) * RSS + (cg * 8 + (rg >> 3)) * 2048;
  const GAS v4u* xg = (const GAS v4u*)p.x;
  u64 tk0 = 0, tk1 = 0;
  STAMP(0);

  if (w < 0) {
    __builtin_amdgcn_s_setprio(3);
    const v4u xa0 = xg[lane * 2], xa1 = xg[lane * 2 + 1], xb0 = xg[128 + lane * 2], xb1 = xg[128 + lane * 2 + 1];
    const v2u ev = __builtin_amdgcn_raw_buffer_load_b64(rs, WS_E, 0, AUX_SC1);
    if (lane < 6) FL[lane] = 0u;
    float rr;
    {
      float va_[16], vb_[16];
#pragma unroll
      for (int k = 0; k < 4; ++k) {
        va_[2 * k] = bflo(xa0[k]); va_[2 * k + 1] = bfhi(xa0[k]); va_[8 + 2 * k] = bflo(xa1[k]); va_[8 + 2 * k + 1] = bfhi(xa1[k]);
        vb_[2 * k] = bflo(xb0[k]); vb_[2 * k + 1] = bfhi(xb0[k]); vb_[8 + 2 * k] = bflo(xb1[k]); vb_[8 + 2 * k + 1] = bfhi(xb1[k]);
      }
      float sa = 0.f, sb = 0.f;
#pragma unroll
      for (int k = 0; k < 16; ++k) { sa += va_[k]; sb += vb_[k]; }
      const float ma = wave_sum(sa) * (1.0f / D), mb = wave_sum(sb) * (1.0f / D);
      if (lane == 0) *(LAS f32x2v*)(L + L_FL + 32) = f32x2v{ma, mb};
      WG_BARRIER();
      float qa = 0.f, qb = 0.f;
#pragma unroll
      for (int k = 0; k < 16; ++k) { const float da = va_[k] - ma, db = vb_[k] - mb; qa = fmaf(da, da, qa); qb = fmaf(db, db, qb); }
      const float ta = wave_sum(qa), tb = wave_sum(qb);
      rr = 1.0f / sqrtf((lane < 32 ? ta : tb) * (1.0f / D) + 1e-5f);
    }
    const u64 e1 = ((((u64)__builtin_amdgcn_readfirstlane(ev.y)) << 32) | (u64)(unsigned)__builtin_amdgcn_readfirstlane(ev.x)) + 1ull;
    const unsigned tag_hi = (unsigned)(e1 >> 8), tag_lo = (unsigned)(e1 & 0xFFull);
    if (lane == 0) { FL[6] = (unsigned)e1; FL[7] = (unsigned)(e1 >> 32); }
    lds_wait(FL + 0, 8);
    {
      f32x2v a = {0.f, 0.f};
#pragma unroll
      for (int ww = 0; ww < 8; ++ww) a += *(LAS f32x2v*)(L + L_RED + ww * 512 + lane * 8);
      const v2u g0 = granule(a.x * rr, tag_lo, tag_hi), g1 = granule(a.y * rr, tag_lo, tag_hi);
      __builtin_amdgcn_raw_buffer_store_b128(v4u{g0.x, g0.y, g1.x, g1.y}, rs,
          ((rg & 15) >> 1) * RSQ + WS_QP + ((hkv * 2 + (lane >> 5)) * 4 + cg) * 1024 + (rg & 1) * 512 + (lane & 31) * 16, 0, AUX_SC1);
    }
    STAMP_S(1);
    DRAIN();
    {
      const int qoff = WS_QP + (hkv * 2 + bb) * 4096 + lane * 16;
      v4u qg[32];
#pragma nounroll
      for (unsigned spins = 0; spins < SPIN_CAP; ++spins) {
        asm volatile("" ::: "memory");
        bool ok = true;
#pragma unroll
        for (int j = 0; j < 32; ++j) qg[j] = __builtin_amdgcn_raw_buffer_load_b128(rs, qoff + (j >> 3) * 1024, (j & 7) * RSQ, AUX_SC1);
#pragma unroll
        for (int j = 0; j < 32; ++j) ok &= tag2(qg[j], tag_lo, tag_hi);
        if (__all(ok)) break;
        __builtin_amdgcn_s_sleep(4);
      }
#pragma unroll
      for (int j = 0; j < 8; ++j) {
        const float q0 = (u2f(qg[j].x << 8) + u2f(qg[8 + j].x << 8)) + (u2f(qg[16 + j].x << 8) + u2f(qg[24 + j].x << 8));
        const float q1 = (u2f(qg[j].z << 8) + u2f(qg[8 + j].z << 8)) + (u2f(qg[16 + j].z << 8) + u2f(qg[24 + j].z << 8));
        const unsigned hi = cvtpk(q0, q1);
        const unsigned lo = cvtpk(q0 - bflo(hi), q1 - bfhi(hi));
        *(LAS unsigned*)(L + L_Q + j * 272 + lane * 4) = hi;
        *(LAS unsigned*)(L + L_Q + (j + 8) * 272 + lane * 4) = lo;
      }
      lds_signal(FL + 3);
    }
    const int coff = ((lane & 15) >> 1) * RSQ + WS_QP + (((lane >> 4) * 2) * 4 + cg) * 1024 + (lane & 1) * 512;
    STAMP_S(2);
    if (reducer) {
#pragma nounroll
      for (unsigned spins = 0; spins < SPIN_CAP; ++spins) {
        asm volatile("" ::: "memory");
        const v4u ck = __builtin_amdgcn_raw_buffer_load_b128(rs, coff, 0, AUX_SC1);
        if (__all(tag2(ck, tag_lo, tag_hi))) break;
        __builtin_amdgcn_s_sleep(4);
      }
    }
    if (has_ffn) {
      lds_wait(FL + 1, 8);
      v4u gr[2];
#pragma unroll
      for (int m = 0; m < 2; ++m) {
        f32x2v a = {0.f, 0.f};
#pragma unroll
        for (int ww = 0; ww < 8; ++ww) a += *(LAS f32x2v*)(L + L_RED + 4096 + m * 4096 + ww * 512 + lane * 8);
        const v2u g0 = granule(a.x * rr, tag_lo, tag_hi), g1 = granule(a.y * rr, tag_lo, tag_hi);
        gr[m] = v4u{g0.x, g0.y, g1.x, g1.y};
      }
      lds_wait(FL + 2, GUWAIT);
#pragma unroll
      for (int m = 0; m < 2; ++m) __builtin_amdgcn_raw_buffer_store_b128(gr[m], rs, (cg * 2 + m) * RSG + WS_GU + rg * 1024 + lane * 16, 0, AUX_SC1);
    }
    STAMP_S(4);
    WG_BARRIER();
    return;
  }

  {
    const v4u s4 = {SENT, SENT, SENT, SENT};
    const int off = w < 4 ? p1own + w * 1024 : (w < 6 ? WS_S2 - 4096 : WS_F2 - 6144) + s2own + w * 1024;
    __builtin_amdgcn_raw_buffer_store_b128(s4, rs, off + lane * 16, 0, AUX_SC1);
    SB();
  }
  const int r8 = lane >> 3, c8 = lane & 7;
  const int krow = cg * 256 + w * 32 + r8 * 4;
  const v2u xr0 = *(const GAS v2u*)(p.x + krow), xr1 = *(const GAS v2u*)(p.x + D + krow);
  v4u wqr[4], wgr[4], wur[4], wdr[4], wor[4];
#pragma unroll
  for (int i = 0; i < 4; ++i) wqr[i] = ldnt((const GAS v4u*)(p.wq + (size_t)(krow + i) * HD + rg * 64 + c8 * 8));
  SB();
  const int fbytes = has_ffn ? D * FF * 2 : 0;
  const __amdgpu_buffer_rsrc_t rwg = __builtin_amdgcn_make_buffer_rsrc((void*)p.wg, 0, fbytes, 0x00020000);
  const __amdgpu_buffer_rsrc_t rwu = __builtin_amdgcn_make_buffer_rsrc((void*)p.wu, 0, fbytes, 0x00020000);
  const __amdgpu_buffer_rsrc_t rwd = __builtin_amdgcn_make_buffer_rsrc((void*)p.wd, 0, fbytes, 0x00020000);
  const __amdgpu_buffer_rsrc_t rwo = __builtin_amdgcn_make_buffer_rsrc((void*)p.wo, 0, HD * D * 2, 0x00020000);
  const int guoff = (krow * FF + fi * 64 + c8 * 8) * 2;
#pragma unroll
  for (int i = 0; i < 4; ++i) wgr[i] = __builtin_amdgcn_raw_buffer_load_b128(rwg, guoff + i * FF * 2, 0, AUX_NT);
  SB();
  const int tile = ((w * 8 + (lane >> 5)) * D + cg * 256 + (lane & 31) * 8) * 2;
  const int i16 = lane & 15, g4 = lane >> 4;
  const __amdgpu_buffer_rsrc_t rk = __builtin_amdgcn_make_buffer_rsrc((void*)p.kc, 0, 8 * SK * 256, 0x00020000);
  const __amdgpu_buffer_rsrc_t rv = __builtin_amdgcn_make_buffer_rsrc((void*)p.vc, 0, 8 * SK * 256, 0x00020000);
  const int klane = (4 * (i16 & 3) + (i16 >> 2)) * 256 + g4 * 16, vlane = lane * 16;
  const int fj = xq < grp ? xq : xq - 1;
  const int nunit = has_ffn ? 64 - BAL / 7 - (fj < BAL % 7 ? 1 : 0) : 64 + BAL;
  const int obase = ((bb * 4 + hkv) * 2048 + cg * 512) * 4096;
  const int ubase = obase + xq * 262144;
#define UOFF(t_) (ubase + (t_) * 4096)
#define UOFFX(t_) ((t_) < 64 ? UOFF(t_) : obase + ((((t_) - 64) % 7) + ((((t_) - 64) % 7) >= grp ? 1 : 0)) * 262144 + (63 - ((t_) - 64) / 7) * 4096)
  v4u ka[4], va[4], kb[4], vb[4], kc[4], vc[4];
  WG_BARRIER();
  STAMP(1);
  float x0[4], x1[4];
  {
    const f32x2v mm = *(LAS f32x2v*)(L + L_FL + 32);
    x0[0] = bflo(xr0.x) - mm.x; x0[1] = bfhi(xr0.x) - mm.x; x0[2] = bflo(xr0.y) - mm.x; x0[3] = bfhi(xr0.y) - mm.x;
    x1[0] = bflo(xr1.x) - mm.y; x1[1] = bfhi(xr1.x) - mm.y; x1[2] = bflo(xr1.y) - mm.y; x1[3] = bfhi(xr1.y) - mm.y;
  }
  const int redoff = ((g4 & 1) * 64 + c8 * 8 + (g4 >> 1) * 4) * 4;

  {
    const f32x4 r = gemv64(wqr, x0, x1);
    if ((lane & 8) == 0) *(LAS f32x4*)(L + L_RED + w * 512 + redoff) = r;
  }
  lds_signal(FL + 0);
#pragma unroll
  for (int i = 0; i < 4; ++i) wur[i] = __builtin_amdgcn_raw_buffer_load_b128(rwu, guoff + i * FF * 2, 0, AUX_NT);
  SB();
  {
    const f32x4 r = gemv64(wgr, x0, x1);
    if ((lane & 8) == 0) *(LAS f32x4*)(L + L_RED + 4096 + w * 512 + redoff) = r;
  }
#pragma unroll
  for (int i = 0; i < 4; ++i) wdr[i] = __builtin_amdgcn_raw_buffer_load_b128(rwd, tile + i * 4 * D, fi * 64 * D * 2, AUX_NT);
  SB();
  {
    const f32x4 r = gemv64(wur, x0, x1);
    if ((lane & 8) == 0) *(LAS f32x4*)(L + L_RED + 8192 + w * 512 + redoff) = r;
  }
  lds_signal(FL + 1);
  STAMP(2);
#pragma unroll
  for (int i = 0; i < 4; ++i) wor[i] = __builtin_amdgcn_raw_buffer_load_b128(rwo, tile + i * 4 * D, rg * 64 * D * 2, AUX_NT);
  SB();
#pragma unroll
  for (int i = 0; i < 4; ++i) *(LAS v4u*)(L + L_WD + (w * 4 + i) * 1024 + lane * 16) = wdr[i];
  SB();
  LDK(ka, UOFF(w));
#pragma unroll
  for (int i = 0; i < 4; ++i) *(LAS v4u*)(L + L_WO + (w * 4 + i) * 1024 + lane * 16) = wor[i];
  SB();
  LDV(va, UOFF(w));
  TOUCH4(ka);
  LDK(kb, UOFF(w + 8));
  TOUCH4(va);
  LDV(vb, UOFF(w + 8));
  TOUCH4(kb);
  LDK(kc, UOFF(w + 16));
  TOUCH4(vb);
  LDV(vc, UOFF(w + 16));
  lds_wait(FL + 3, 1);
  v4u qf[4];
#pragma unroll
  for (int kk = 0; kk < 4; ++kk) qf[kk] = *(LAS v4u*)(L + L_Q + i16 * 272 + kk * 64 + g4 * 16);
  STAMP(3);

  AttnState st;
  st.m = -INFINITY;
  st.l = 0.f;
#pragma unroll
  for (int e = 0; e < 8; ++e) st.o[e] = f32x4{0.f, 0.f, 0.f, 0.f};
  attn_unit(st, ka, va, qf);
  attn_unit(st, kb, vb, qf);
#define PA_K kc
#define PA_V vc
#define PB_K kb
#define PB_V vb
  int t = w + 24;
  for (;;) {
    const int o0 = t < nunit ? UOFFX(t) : OOB;
    const int ak0 = klane + o0, av0 = vlane + o0;
    LDK2(PB_K, ak0);
    LDV2(PB_V, av0);
    asm volatile("" ::"v"(ak0), "v"(av0));
    attn_unit(st, PA_K, PA_V, qf);
    if (t >= nunit) break;
    const int o1 = t + 8 < nunit ? UOFFX(t + 8) : OOB;
    const int ak1 = klane + o1, av1 = vlane + o1;
    LDK2(PA_K, ak1);
    LDV2(PA_V, av1);
    asm volatile("" ::"v"(ak1), "v"(av1));
    attn_unit(st, PB_K, PB_V, qf);
    if (t + 8 >= nunit) break;
    t += 16;
  }
  lds_signal(FL + 2);
  STAMP(5);
  const int sfoff = ((tid >> 2) & 1) * 1024 + ((tid & 127) >> 3) * 64 + (tid & 3) * 16;
  const int oidx = ((tid >> 4) & 1) * D + cg * 256 + (rg & 15) * 16 + (tid & 15);
  unsigned xo = 0;
  if (w == 0) xo = *(const GAS unsigned short*)(p.x + oidx);

  {
    const float l = x32_sum(x16_sum(st.l));
    if (i16 < 8) {
#pragma unroll
      for (int e = 0; e < 8; ++e) *(LAS f32x4*)(L + L_O + (w * 256 + (i16 * 4 + g4) * 8 + (e ^ i16)) * 16) = st.o[e];
      if (g4 == 0) *(LAS v2u*)(L + L_ML + (i16 * 8 + w) * 8) = v2u{f2u(st.m), f2u(l)};
    }
  }
  int poff = g4 * RSP + WS_P1 + (hkv * 8 + w) * 4096 + (rg & 15) * 256 + i16 * 16;
  int goff = (lane >> 3) * RSG + WS_GU + rg * 1024 + ((lane >> 2) & 1) * 512 + (w * 8 + (lane & 3) * 2) * 8;
  int roff = ((tid >> 3) & 7) * RSS + (cg * 8 + (tid >> 6)) * 2048 + (rg & 15) * 128 + (tid & 7) * 16;
  int p1st = p1own + tid * 16, s2st = (w < 2 ? WS_S2 : WS_F2) + s2own + (tid & 127) * 16;
  __builtin_amdgcn_s_waitcnt(0x0F70);
  asm volatile("" : "+v"(poff), "+v"(goff), "+v"(roff), "+v"(p1st), "+v"(s2st));
  WG_BARRIER();
  if (w < 4) {
    const int j = tid >> 5;
    v4u ml[4];
    f32x4 ov[8];
#pragma unroll
    for (int k = 0; k < 4; ++k) ml[k] = *(LAS v4u*)(L + L_ML + j * 64 + k * 16);
#pragma unroll
    for (int ww = 0; ww < 8; ++ww) ov[ww] = *(LAS f32x4*)(L + L_O + (ww * 256 + (tid ^ j)) * 16);
    SB();
    float M = fmaxf(u2f(ml[0].x), u2f(ml[0].z));
#pragma unroll
    for (int k = 1; k < 4; ++k) M = fmaxf(M, fmaxf(u2f(ml[k].x), u2f(ml[k].z)));
    float Ls = 0.f;
    f32x4 acc = {0.f, 0.f, 0.f, 0.f};
#pragma unroll
    for (int ww = 0; ww < 8; ++ww) {
      const float wt = __builtin_amdgcn_exp2f(u2f(ml[ww >> 1][(ww & 1) * 2]) - M);
      Ls = fmaf(wt, u2f(ml[ww >> 1][(ww & 1) * 2 + 1]), Ls);
      acc += ov[ww] * wt;
    }
    acc *= rcp(Ls);
    const unsigned lse = f2u(M + __builtin_amdgcn_logf(Ls));
    v4u pk;
#pragma unroll
    for (int r = 0; r < 4; ++r) {
      const unsigned t = ((f2u(acc[r]) + 0x80u) & 0xFFFFFF00u) | ((lse >> (8 * r)) & 0xFFu);
      pk[r] = t == SENT ? 0x7FC000FFu : t;
    }
    __builtin_amdgcn_raw_buffer_store_b128(pk, rs, p1st, 0, AUX_SC1);
  }
  STAMP(6);
  SB();

  f32x4 wof[8];
  {
    v4u gu = __builtin_amdgcn_raw_buffer_load_b128(rs, goff, 0, AUX_SC1);
    v4u pa = __builtin_amdgcn_raw_buffer_load_b128(rs, poff, 0, AUX_SC1);
    v4u pb = __builtin_amdgcn_raw_buffer_load_b128(rs, poff, 4 * RSP, AUX_SC1);
    SB();
    if (has_ffn) {
      const unsigned e_lo = FL[6], e_hi = FL[7];
      const unsigned tag_hi = (e_lo >> 8) | (e_hi << 24), tag_lo = e_lo & 0xFFu;
      v2u wt[8];
#pragma unroll
      for (int k = 0; k < 8; ++k) wt[k] = *(LAS v2u*)(L + L_WD + (w * 8 + k) * 512 + lane * 8);
      if (!__all(tag2(gu, tag_lo, tag_hi))) {
#pragma nounroll
        for (unsigned spins = 0; spins < SPIN_CAP; ++spins) {
          asm volatile("" ::: "memory");
          __builtin_amdgcn_s_sleep(1);
          gu = __builtin_amdgcn_raw_buffer_load_b128(rs, goff, 0, AUX_SC1);
          if (__all(tag2(gu, tag_lo, tag_hi))) break;
        }
      }
      const float ga = x32_sum(x16_sum(u2f(gu.x << 8))), gb = x32_sum(x16_sum(u2f(gu.z << 8)));
      const float hx = silu(ga) * dpp<XOR8>(ga), hy = silu(gb) * dpp<XOR8>(gb);
      float h0[8], h1[8];
#pragma unroll
      for (int pr = 0; pr < 4; ++pr) {
        h0[2 * pr] = rdl(hx, pr); h0[2 * pr + 1] = rdl(hy, pr);
        h1[2 * pr] = rdl(hx, 4 + pr); h1[2 * pr + 1] = rdl(hy, 4 + pr);
      }
      f32x4 s0, s1;
      gemv256(wt, h0, h1, s0, s1);
      *(LAS f32x4*)(L + L_F + w * 2048 + lane * 16) = s0;
      *(LAS f32x4*)(L + L_F + w * 2048 + 1024 + lane * 16) = s1;
    }
#pragma unroll
    for (int k = 0; k < 8; ++k) {
      const v2u t = *(LAS v2u*)(L + L_WO + (w * 8 + k) * 512 + lane * 8);
      wof[k] = f32x4{bflo(t.x), bfhi(t.x), bflo(t.y), bfhi(t.y)};
      asm volatile("" : "+v"(wof[k]));
    }
    bool ha = full4(pa), hb = full4(pb);
#pragma nounroll
    for (unsigned spins = 0; spins < SPIN_CAP; ++spins) {
      asm volatile("" ::: "memory");
      if (__all(ha & hb)) break;
      if (!ha) pa = __builtin_amdgcn_raw_buffer_load_b128(rs, poff, 0, AUX_SC1);
      if (!hb) pb = __builtin_amdgcn_raw_buffer_load_b128(rs, poff, 4 * RSP, AUX_SC1);
      ha = full4(pa);
      hb = full4(pb);
    }
    const float la = u2f(__builtin_amdgcn_perm(__builtin_amdgcn_perm(pa.w, pa.z, 0x0c0c0400u), __builtin_amdgcn_perm(pa.y, pa.x, 0x0c0c0400u), 0x05040100u));
    const float lb = u2f(__builtin_amdgcn_perm(__builtin_amdgcn_perm(pb.w, pb.z, 0x0c0c0400u), __builtin_amdgcn_perm(pb.y, pb.x, 0x0c0c0400u), 0x05040100u));
    const float Mw = x32_max(x16_max(fmaxf(la, lb)));
    const float wa = __builtin_amdgcn_exp2f(la - Mw), wb = __builtin_amdgcn_exp2f(lb - Mw);
    const float Ww = x32_sum(x16_sum(wa + wb));
    float a[4];
#pragma unroll
    for (int r = 0; r < 4; ++r) a[r] = fmaf(u2f(pa[r] & 0xFFFFFF00u), wa, u2f(pb[r] & 0xFFFFFF00u) * wb);
    const float v = tr32(tr16(a[0], a[1]), tr16(a[2], a[3]));
    *(LAS float*)(L + L_C + (((w >> 2) * 64 + (i16 >> 3) * 32 + g4 * 8 + (i16 & 7)) * 4 + (w & 3)) * 4) = v;
    if (lane == 0) *(LAS v2u*)(L + L_C + 2048 + w * 8) = v2u{f2u(Mw), f2u(Ww)};
  }
  STAMP(7);
  WG_BARRIER();
  {
    const int b = (lane >> 3) & 1, k = lane & 7;
    const v4u m0 = *(LAS v4u*)(L + L_C + 2048 + b * 32), m1 = *(LAS v4u*)(L + L_C + 2048 + b * 32 + 16);
    const f32x4 av = *(LAS f32x4*)(L + L_C + (b * 64 + w * 8 + k) * 16);
    const float M = fmaxf(fmaxf(u2f(m0.x), u2f(m0.z)), fmaxf(u2f(m1.x), u2f(m1.z)));
    const float f0 = __builtin_amdgcn_exp2f(u2f(m0.x) - M), f1 = __builtin_amdgcn_exp2f(u2f(m0.z) - M);
    const float f2 = __builtin_amdgcn_exp2f(u2f(m1.x) - M), f3 = __builtin_amdgcn_exp2f(u2f(m1.z) - M);
    const float num = fmaf(f0, av.x, f1 * av.y) + fmaf(f2, av.z, f3 * av.w);
    const float den = fmaf(f0, u2f(m0.y), f1 * u2f(m0.w)) + fmaf(f2, u2f(m1.y), f3 * u2f(m1.w));
    const float ov = num * rcp(den);
    f32x4 s0 = {0.f, 0.f, 0.f, 0.f}, s1 = {0.f, 0.f, 0.f, 0.f};
#pragma unroll
    for (int kk = 0; kk < 8; ++kk) { s0 += wof[kk] * rdl(ov, kk); s1 += wof[kk] * rdl(ov, 8 + kk); }
    *(LAS f32x4*)(L + L_S + w * 2048 + lane * 16) = s0;
    *(LAS f32x4*)(L + L_S + w * 2048 + 1024 + lane * 16) = s1;
  }
  WG_BARRIER();
  if (w < 2 || (w < 4 && has_ffn)) {
    const int lb = (w < 2 ? L_S : L_F) + sfoff;
    f32x4 t[8];
#pragma unroll
    for (int ww = 0; ww < 8; ++ww) t[ww] = *(LAS f32x4*)(L + lb + ww * 2048);
    SB();
    const f32x4 s4 = ((t[0] + t[1]) + (t[2] + t[3])) + ((t[4] + t[5]) + (t[6] + t[7]));
    __builtin_amdgcn_raw_buffer_store_b128(clean4(s4), rs, s2st, 0, AUX_SC1);
  }
  STAMP(8);

  if (reducer) {
    v4u a = __builtin_amdgcn_raw_buffer_load_b128(rs, roff, WS_S2, AUX_SC1);
    v4u c = {0u, 0u, 0u, 0u};
    if (((tid >> 3) & 7) != w) c = __builtin_amdgcn_raw_buffer_load_b128(rs, roff, WS_F2, AUX_SC1);
    bool ha = full4(a), hc = full4(c);
#pragma nounroll
    for (unsigned spins = 0; spins < SPIN_CAP; ++spins) {
      asm volatile("" ::: "memory");
      if (__all(ha & hc)) break;
      if (!ha) a = __builtin_amdgcn_raw_buffer_load_b128(rs, roff, WS_S2, AUX_SC1);
      if (!hc) c = __builtin_amdgcn_raw_buffer_load_b128(rs, roff, WS_F2, AUX_SC1);
      ha = full4(a);
      hc = full4(c);
    }
    float vs = tr32(tr16(u2f(a.x), u2f(a.y)), tr16(u2f(a.z), u2f(a.w)));
    float vf = tr32(tr16(u2f(c.x), u2f(c.y)), tr16(u2f(c.z), u2f(c.w)));
    vs += dpp<XOR8>(vs);
    vf += dpp<XOR8>(vf);
    *(LAS float*)(L + L_R + ((lane >> 3) & 1) * 1024 + ((lane & 7) * 4 + (lane >> 4)) * 32 + w * 4) = (lane & 8) ? vs : vf;
    WG_BARRIER();
    if (tid < 32) {
      const f32x4 f0 = *(LAS f32x4*)(L + L_R + tid * 32), f1 = *(LAS f32x4*)(L + L_R + tid * 32 + 16);
      const f32x4 s0 = *(LAS f32x4*)(L + L_R + 1024 + tid * 32), s1 = *(LAS f32x4*)(L + L_R + 1024 + tid * 32 + 16);
      const f32x4 f4 = f0 + f1, s4 = s0 + s1;
      const float f = (f4.x + f4.y) + (f4.z + f4.w), sv = (s4.x + s4.y) + (s4.z + s4.w);
      { const u64 tn = __builtin_amdgcn_s_memrealtime(); if (DIAG == 9) tk1 = tn; const u64 until = tn + 2 * (tk1 - tk0); while (__builtin_amdgcn_s_memrealtime() < until) {} }
      const float r = fmaf(sv, silu(f), bflo(xo));
      p.out[oidx] = (unsigned short)(cvtpk(r, 0.f) & 0xFFFFu);
    }
    if (bid == NWG - 1 && tid == 0) {
      const v2u ne = {FL[6], FL[7]};
      __builtin_amdgcn_raw_buffer_store_b64(ne, rs, WS_E, 0, AUX_SC1);
    }
  }
  STAMP(9);
}

}

extern "C" void kernel_launch(void* const* d_in, const int* in_sizes, int n_in, void* d_out, int out_size, void* d_ws,
                              size_t ws_size, hipStream_t stream) {
  Params p;
  p.x = (const unsigned short*)d_in[0];
  p.kc = (const unsigned short*)d_in[1];
  p.vc = (const unsigned short*)d_in[2];
  p.wq = (const unsigned short*)d_in[3];
  p.wo = (const unsigned short*)d_in[4];
  p.wg = (const unsigned short*)d_in[6];
  p.wu = (const unsigned short*)d_in[7];
  p.wd = (const unsigned short*)d_in[8];
  p.out = (unsigned short*)d_out;
  p.ws = (unsigned char*)d_ws;
  hipLaunchKernelGGL(fused_decode, dim3(NWG), dim3(NTHR), 0, stream, p);
}
```

```cpp
#include <hip/hip_runtime.h>
#include <hip/hip_bf16.h>
#include <stdint.h>

#define GAS __attribute__((address_space(1)))
#define LAS __attribute__((address_space(3)))
typedef unsigned v4u __attribute__((ext_vector_type(4)));
typedef unsigned v2u __attribute__((ext_vector_type(2)));
typedef float f32x4 __attribute__((ext_vector_type(4)));
typedef short bf16x8 __attribute__((ext_vector_type(8)));
typedef short bf16x4 __attribute__((ext_vector_type(4)));
typedef unsigned long long u64;

namespace {

constexpr int NWG = 256, NTHR = 576;
constexpr int D = 1024, HD = 4096, FF = 3584, SK = 32768;
constexpr int AUX_SC1 = 16;
constexpr unsigned SENT = 0xFFFFFFFFu;
constexpr unsigned SPIN_CAP = 1u << 22;

constexpr int RSG = 65536, RSP = 131072, RSS = 65536;
constexpr int WS_E = 0;
constexpr int WS_QP = 4096;
constexpr int WS_GU = WS_QP + 262144;
constexpr int WS_P1 = WS_GU + 8 * RSG;
constexpr int WS_S2 = WS_P1 + 8 * RSP;
constexpr int WS_F2 = WS_S2 + 8 * RSS;
constexpr int WS_ALL = WS_F2 + 8 * RSS;

constexpr int L_RED = 0;
constexpr int L_Q = 12288;
constexpr int L_ML = L_Q + 16 * 272;
constexpr int L_O = L_ML + 512;
constexpr int L_S = L_O + 32768;
constexpr int L_C = L_S + 16384;
constexpr int L_R = L_C + 2048 + 64;
constexpr int L_FL = L_R + 2048 + 512;
constexpr int L_WO = L_FL + 64;
constexpr int L_WD = L_WO + 32768;
constexpr int L_F = L_WD + 32768;
constexpr int L_USED = L_F + 16384;
constexpr int L_BYTES = 152 * 1024;
static_assert(L_USED <= L_BYTES, "lds");

struct Params {
  const unsigned short* x;
  const unsigned short* wq;
  unsigned char* ws;
  const unsigned short* wg;
  const unsigned short* wu;
  const unsigned short* wd;
  const unsigned short* wo;
  const unsigned short* kc;
  const unsigned short* vc;
  unsigned short* out;
};

__device__ __forceinline__ float bflo(unsigned u) { return __builtin_bit_cast(float, u << 16); }
__device__ __forceinline__ float bfhi(unsigned u) { return __builtin_bit_cast(float, u & 0xFFFF0000u); }
__device__ __forceinline__ unsigned f2u(float f) { return __builtin_bit_cast(unsigned, f); }
__device__ __forceinline__ float u2f(unsigned u) { return __builtin_bit_cast(float, u); }
typedef __bf16 bf16p __attribute__((ext_vector_type(2)));
typedef float f32x2 __attribute__((ext_vector_type(2)));
__device__ __forceinline__ unsigned cvtpk(float lo, float hi) {
  return __builtin_bit_cast(unsigned, __builtin_convertvector(f32x2{lo, hi}, bf16p));
}
template <int CTRL> __device__ __forceinline__ float dpp(float x) {
  return __builtin_bit_cast(float, __builtin_amdgcn_mov_dpp(__builtin_bit_cast(int, x), CTRL, 0xf, 0xf, true));
}
constexpr int XOR1 = 0xB1, XOR2 = 0x4E, XOR7 = 0x141, XOR8 = 0x128;
__device__ __forceinline__ float x16_sum(float x) {
  auto s = __builtin_amdgcn_permlane16_swap(f2u(x), f2u(x), false, false);
  return u2f(s[0]) + u2f(s[1]);
}
__device__ __forceinline__ float x32_sum(float x) {
  auto s = __builtin_amdgcn_permlane32_swap(f2u(x), f2u(x), false, false);
  return u2f(s[0]) + u2f(s[1]);
}
__device__ __forceinline__ float x16_max(float x) {
  auto s = __builtin_amdgcn_permlane16_swap(f2u(x), f2u(x), false, false);
  return fmaxf(u2f(s[0]), u2f(s[1]));
}
__device__ __forceinline__ float x32_max(float x) {
  auto s = __builtin_amdgcn_permlane32_swap(f2u(x), f2u(x), false, false);
  return fmaxf(u2f(s[0]), u2f(s[1]));
}
__device__ __forceinline__ float row16_sum(float x) {
  x += dpp<XOR1>(x);
  x += dpp<XOR2>(x);
  x += dpp<XOR7>(x);
  x += dpp<XOR8>(x);
  return x;
}
__device__ __forceinline__ float wave_sum(float x) { return x32_sum(x16_sum(row16_sum(x))); }
__device__ __forceinline__ float tr16(float a, float b) {
  auto s = __builtin_amdgcn_permlane16_swap(f2u(a), f2u(b), false, false);
  return u2f(s[0]) + u2f(s[1]);
}
__device__ __forceinline__ float tr32(float a, float b) {
  auto s = __builtin_amdgcn_permlane32_swap(f2u(a), f2u(b), false, false);
  return u2f(s[0]) + u2f(s[1]);
}
__device__ __forceinline__ v4u ldnt(const GAS v4u* p) { return __builtin_nontemporal_load(p); }
__device__ __forceinline__ unsigned clean(float f) {
  const unsigned u = f2u(f);
  return u == SENT ? 0x7FC00000u : u;
}
__device__ __forceinline__ v4u clean4(f32x4 v) { return v4u{clean(v.x), clean(v.y), clean(v.z), clean(v.w)}; }
__device__ __forceinline__ bool full4(v4u v) { return v.x != SENT && v.y != SENT && v.z != SENT && v.w != SENT; }
__device__ __forceinline__ float rdl(float v, int lane) { return u2f(__builtin_amdgcn_readlane(f2u(v), lane)); }
__device__ __forceinline__ float rcp(float v) { return __builtin_amdgcn_rcpf(v); }
__device__ __forceinline__ float silu(float v) { return v * rcp(1.0f + __builtin_amdgcn_exp2f(v * -1.4426950408889634f)); }

#define SB() __builtin_amdgcn_sched_barrier(0)
#define WG_BARRIER()                                        \
  do {                                                      \
    asm volatile("s_waitcnt lgkmcnt(0)" ::: "memory");      \
    __builtin_amdgcn_s_barrier();                           \
    asm volatile("" ::: "memory");                          \
  } while (0)

__device__ __forceinline__ f32x4 gemv64(const v4u (&w)[4], const float (&x0)[4], const float (&x1)[4]) {
  float a0[8], a1[8];
#pragma unroll
  for (int c = 0; c < 8; ++c) { a0[c] = 0.f; a1[c] = 0.f; }
#pragma unroll
  for (int i = 0; i < 4; ++i) {
#pragma unroll
    for (int k = 0; k < 4; ++k) {
      const float lo = bflo(w[i][k]), hi = bfhi(w[i][k]);
      a0[2 * k] = fmaf(lo, x0[i], a0[2 * k]);
      a0[2 * k + 1] = fmaf(hi, x0[i], a0[2 * k + 1]);
      a1[2 * k] = fmaf(lo, x1[i], a1[2 * k]);
      a1[2 * k + 1] = fmaf(hi, x1[i], a1[2 * k + 1]);
    }
  }
  float t[8];
#pragma unroll
  for (int c = 0; c < 8; ++c) t[c] = tr16(a0[c], a1[c]);
  f32x4 r;
#pragma unroll
  for (int c = 0; c < 4; ++c) {
    float v = tr32(t[c], t[c + 4]);
    v += dpp<XOR8>(v);
    r[c] = v;
  }
  return r;
}

__device__ __forceinline__ void gemv256(const v2u (&w)[8], const float (&c0)[8], const float (&c1)[8], f32x4& s0, f32x4& s1) {
  s0 = f32x4{0.f, 0.f, 0.f, 0.f};
  s1 = f32x4{0.f, 0.f, 0.f, 0.f};
#pragma unroll
  for (int k = 0; k < 8; ++k) {
    const f32x4 wf = {bflo(w[k].x), bfhi(w[k].x), bflo(w[k].y), bfhi(w[k].y)};
#pragma unroll
    for (int c = 0; c < 4; ++c) {
      s0[c] = fmaf(wf[c], c0[k], s0[c]);
      s1[c] = fmaf(wf[c], c1[k], s1[c]);
    }
  }
}

__device__ __forceinline__ v2u granule(float v, unsigned tag_lo, unsigned tag_hi) {
  return v2u{(tag_lo << 24) | ((f2u(v) + 0x80u) >> 8), tag_hi};
}
__device__ __forceinline__ bool tag2(v4u g, unsigned tag_lo, unsigned tag_hi) {
  return (g.y == tag_hi) & ((g.x >> 24) == tag_lo) & (g.w == tag_hi) & ((g.z >> 24) == tag_lo);
}
#define TOUCH4(a) asm volatile("" : "+v"(a[0]), "+v"(a[1]), "+v"(a[2]), "+v"(a[3]))
constexpr int AUX_NT = 2;
#define LDK(kb_, off_) do { _Pragma("unroll") for (int j = 0; j < 4; ++j) kb_[j] = __builtin_amdgcn_raw_buffer_load_b128(rk, (klane + (off_)) + j * 64, 0, AUX_NT); SB(); } while (0)
#define LDV(vb_, off_) do { _Pragma("unroll") for (int j = 0; j < 4; ++j) vb_[j] = __builtin_amdgcn_raw_buffer_load_b128(rv, (vlane + (off_)) + j * 1024, 0, AUX_NT); SB(); } while (0)
#define LDK2(kb_, a_) do { _Pragma("unroll") for (int j = 0; j < 4; ++j) kb_[j] = __builtin_amdgcn_raw_buffer_load_b128(rk, (a_) + j * 64, 0, AUX_NT); SB(); } while (0)
#define LDV2(vb_, a_) do { _Pragma("unroll") for (int j = 0; j < 4; ++j) vb_[j] = __builtin_amdgcn_raw_buffer_load_b128(rv, (a_) + j * 1024, 0, AUX_NT); SB(); } while (0)

struct AttnState {
  float m, l;
  f32x4 o[8];
};
__device__ __forceinline__ v2u attn_qk(AttnState& st, const v4u (&kc)[4], const v4u (&qf)[4]) {
  constexpr float C2 = 0.088388347648318440f * 1.4426950408889634f;
  f32x4 s = {0.f, 0.f, 0.f, 0.f};
#pragma unroll
  for (int kk = 0; kk < 4; ++kk)
    s = __builtin_amdgcn_mfma_f32_16x16x32_bf16(__builtin_bit_cast(bf16x8, kc[kk]), __builtin_bit_cast(bf16x8, qf[kk]), s, 0, 0, 0);
#pragma unroll
  for (int r = 0; r < 4; ++r) s[r] = (s[r] + dpp<XOR8>(s[r])) * C2;
  float mx = fmaxf(fmaxf(s[0], s[1]), fmaxf(s[2], s[3]));
  mx = x32_max(x16_max(mx));
  const float mn = fmaxf(st.m, mx);
  const float alpha = __builtin_amdgcn_exp2f(st.m - mn);
  st.m = mn;
  const float p0 = __builtin_amdgcn_exp2f(s[0] - mn), p1 = __builtin_amdgcn_exp2f(s[1] - mn);
  const float p2 = __builtin_amdgcn_exp2f(s[2] - mn), p3 = __builtin_amdgcn_exp2f(s[3] - mn);
  st.l = fmaf(st.l, alpha, (p0 + p1) + (p2 + p3));
  const v2u pb = {cvtpk(p0, p1), cvtpk(p2, p3)};
#pragma unroll
  for (int e = 0; e < 8; ++e) st.o[e] *= alpha;
  SB();
  return pb;
}
__device__ __forceinline__ void attn_pv(AttnState& st, const v4u (&vc)[4], v2u pb) {
#pragma unroll
  for (int e = 0; e < 8; ++e) {
    const unsigned sel = (e & 1) ? 0x07060302u : 0x05040100u;
    const v2u a = {__builtin_amdgcn_perm(vc[1][e >> 1], vc[0][e >> 1], sel), __builtin_amdgcn_perm(vc[3][e >> 1], vc[2][e >> 1], sel)};
    st.o[e] = __builtin_amdgcn_mfma_f32_16x16x16bf16_1k(__builtin_bit_cast(bf16x4, a), __builtin_bit_cast(bf16x4, pb), st.o[e], 0, 0, 0);
  }
  SB();
}
__device__ __forceinline__ void attn_unit(AttnState& st, const v4u (&kc)[4], const v4u (&vc)[4], const v4u (&qf)[4]) {
  attn_pv(st, vc, attn_qk(st, kc, qf));
}

#define DRAIN() asm volatile("s_waitcnt vmcnt(0)" ::: "memory")
constexpr int MV = 3;
typedef float f32x2v __attribute__((ext_vector_type(2)));
__device__ __forceinline__ void lds_signal(LAS unsigned* c) {
  asm volatile("s_waitcnt lgkmcnt(0)" ::: "memory");
  if ((threadIdx.x & 63) == 0) __hip_atomic_fetch_add(c, 1u, __ATOMIC_RELAXED, __HIP_MEMORY_SCOPE_WORKGROUP);
}
__device__ __forceinline__ void lds_wait(LAS unsigned* c, unsigned want) {
#pragma nounroll
  for (unsigned spins = 0; spins < (SPIN_CAP << 3); ++spins) {
    asm volatile("" ::: "memory");
    if (__builtin_amdgcn_readfirstlane(*(volatile LAS unsigned*)c) >= want) break;
    __builtin_amdgcn_s_sleep(1);
  }
  asm volatile("" ::: "memory");
}

__global__ void __launch_bounds__(NTHR) fused_decode(Params p) {
  __shared__ __attribute__((aligned(16))) unsigned char smem[L_BYTES];
  LAS unsigned char* L = (LAS unsigned char*)smem;
  LAS unsigned* FL = (LAS unsigned*)(L + L_FL);
  const int tid = (int)threadIdx.x - 64, lane = threadIdx.x & 63;
  const int w = __builtin_amdgcn_readfirstlane(threadIdx.x >> 6) - 1;
  const int bid = blockIdx.x;
  const int grp = bid >> 5, xq = bid & 7;
  const int cg = (bid >> 3) & 3, rg = grp * 8 + xq;
  const int hkv = bid >> 6, bb = grp & 1;
  const int xl = (3 - (int)((size_t)p.wq >> 7)) & 7;
  const bool has_ffn = xq != xl;
  const int fi = has_ffn ? grp * 7 + (xq < xl ? xq : xq - 1) : 0;
  const bool reducer = rg >= 48;
  constexpr int OOB = 0x7FFF0000;
  const __amdgpu_buffer_rsrc_t rs = __builtin_amdgcn_make_buffer_rsrc((void*)p.ws, 0, WS_ALL, 0x00020000);
  const int p1own = (bid & 7) * RSP + WS_P1 + (bid >> 3) * 4096;
  const int s2own = (rg & 7) * RSS + (cg * 8 + (rg >> 3)) * 2048;
  const GAS v4u* xg = (const GAS v4u*)p.x;

  if (w < 0) {
    __builtin_amdgcn_s_setprio(3);
    const v4u xa0 = xg[lane * 2], xa1 = xg[lane * 2 + 1], xb0 = xg[128 + lane * 2], xb1 = xg[128 + lane * 2 + 1];
    const v2u ev = __builtin_amdgcn_raw_buffer_load_b64(rs, WS_E, 0, AUX_SC1);
    if (lane < 6) FL[lane] = 0u;
    float rr;
    {
      float va_[16], vb_[16];
#pragma unroll
      for (int k = 0; k < 4; ++k) {
        va_[2 * k] = bflo(xa0[k]); va_[2 * k + 1] = bfhi(xa0[k]); va_[8 + 2 * k] = bflo(xa1[k]); va_[8 + 2 * k + 1] = bfhi(xa1[k]);
        vb_[2 * k] = bflo(xb0[k]); vb_[2 * k + 1] = bfhi(xb0[k]); vb_[8 + 2 * k] = bflo(xb1[k]); vb_[8 + 2 * k + 1] = bfhi(xb1[k]);
      }
      float sa = 0.f, sb = 0.f;
#pragma unroll
      for (int k = 0; k < 16; ++k) { sa += va_[k]; sb += vb_[k]; }
      const float ma = wave_sum(sa) * (1.0f / D), mb = wave_sum(sb) * (1.0f / D);
      if (lane == 0) *(LAS f32x2v*)(L + L_FL + 32) = f32x2v{ma, mb};
      WG_BARRIER();
      float qa = 0.f, qb = 0.f;
#pragma unroll
      for (int k = 0; k < 16; ++k) { const float da = va_[k] - ma, db = vb_[k] - mb; qa = fmaf(da, da, qa); qb = fmaf(db, db, qb); }
      const float ta = wave_sum(qa), tb = wave_sum(qb);
      rr = 1.0f / sqrtf((lane < 32 ? ta : tb) * (1.0f / D) + 1e-5f);
    }
    const u64 e1 = ((((u64)__builtin_amdgcn_readfirstlane(ev.y)) << 32) | (u64)(unsigned)__builtin_amdgcn_readfirstlane(ev.x)) + 1ull;
    const unsigned tag_hi = (unsigned)(e1 >> 8), tag_lo = (unsigned)(e1 & 0xFFull);
    const unsigned tq = (unsigned)e1 & 0xFFFFFFu;
    if (lane == 0) { FL[6] = (unsigned)e1; FL[7] = (unsigned)(e1 >> 32); }
    lds_wait(FL + 0, 8);
    {
      f32x2v a = {0.f, 0.f};
#pragma unroll
      for (int ww = 0; ww < 8; ++ww) a += *(LAS f32x2v*)(L + L_RED + ww * 512 + lane * 8);
      const unsigned v0 = (f2u(a.x * rr) + 0x800u) >> 12, v1 = (f2u(a.y * rr) + 0x800u) >> 12;
      __builtin_amdgcn_raw_buffer_store_b64(v2u{v0 | (v1 << 20), (v1 >> 12) | (tq << 8)}, rs,
          WS_QP + ((hkv * 2 + (lane >> 5)) * 4 + cg) * 4096 + (rg & 15) * 256 + (lane & 31) * 8, 0, AUX_SC1);
    }
    DRAIN();
    {
      const int qoff = WS_QP + (hkv * 2 + bb) * 16384 + lane * 16;
      v4u qg[16];
#pragma nounroll
      for (unsigned spins = 0; spins < SPIN_CAP; ++spins) {
        asm volatile("" ::: "memory");
        bool ok = true;
#pragma unroll
        for (int j = 0; j < 16; ++j) qg[j] = __builtin_amdgcn_raw_buffer_load_b128(rs, qoff + (j & 3) * 1024, (j >> 2) * 4096, AUX_SC1);
#pragma unroll
        for (int j = 0; j < 16; ++j) ok &= ((qg[j].y >> 8) == tq) & ((qg[j].w >> 8) == tq);
        if (__all(ok)) break;
        __builtin_amdgcn_s_sleep(4);
      }
#pragma unroll
      for (int j = 0; j < 4; ++j) {
        f32x4 q = {0.f, 0.f, 0.f, 0.f};
#pragma unroll
        for (int ks = 0; ks < 4; ++ks) {
          const v4u g = qg[ks * 4 + j];
          q += f32x4{u2f(g.x << 12), u2f((g.y << 24) | ((g.x >> 20) << 12)), u2f(g.z << 12), u2f((g.w << 24) | ((g.z >> 20) << 12))};
        }
        const v2u hi = {cvtpk(q.x, q.y), cvtpk(q.z, q.w)};
        const v2u lo = {cvtpk(q.x - bflo(hi.x), q.y - bfhi(hi.x)), cvtpk(q.z - bflo(hi.y), q.w - bfhi(hi.y))};
        *(LAS v2u*)(L + L_Q + (2 * j + (lane >> 5)) * 272 + (lane & 31) * 8) = hi;
        *(LAS v2u*)(L + L_Q + (2 * j + 8 + (lane >> 5)) * 272 + (lane & 31) * 8) = lo;
      }
      lds_signal(FL + 3);
    }
    const int coff = WS_QP + (((lane >> 4) * 2) * 4 + cg) * 4096 + (lane & 15) * 256;
    if (reducer) {
#pragma nounroll
      for (unsigned spins = 0; spins < SPIN_CAP; ++spins) {
        asm volatile("" ::: "memory");
        const v4u ck = __builtin_amdgcn_raw_buffer_load_b128(rs, coff, 0, AUX_SC1);
        if (__all(((ck.y >> 8) == tq) & ((ck.w >> 8) == tq))) break;
        __builtin_amdgcn_s_sleep(4);
      }
    }
    if (has_ffn) {
      lds_wait(FL + 1, 8);
      v4u gr[2];
#pragma unroll
      for (int m = 0; m < 2; ++m) {
        f32x2v a = {0.f, 0.f};
#pragma unroll
        for (int ww = 0; ww < 8; ++ww) a += *(LAS f32x2v*)(L + L_RED + 4096 + m * 4096 + ww * 512 + lane * 8);
        const v2u g0 = granule(a.x * rr, tag_lo, tag_hi), g1 = granule(a.y * rr, tag_lo, tag_hi);
        gr[m] = v4u{g0.x, g0.y, g1.x, g1.y};
      }
      lds_wait(FL + 2, 4);
#pragma unroll
      for (int m = 0; m < 2; ++m) __builtin_amdgcn_raw_buffer_store_b128(gr[m], rs, (cg * 2 + m) * RSG + WS_GU + rg * 1024 + lane * 16, 0, AUX_SC1);
    }
    WG_BARRIER();
    return;
  }

  {
    const v4u s4 = {SENT, SENT, SENT, SENT};
    const int off = w < 4 ? p1own + w * 1024 : (w < 6 ? WS_S2 - 4096 : WS_F2 - 6144) + s2own + w * 1024;
    __builtin_amdgcn_raw_buffer_store_b128(s4, rs, off + lane * 16, 0, AUX_SC1);
    SB();
  }
  const int r8 = lane >> 3, c8 = lane & 7;
  const int krow = cg * 256 + w * 32 + r8 * 4;
  const v2u xr0 = *(const GAS v2u*)(p.x + krow), xr1 = *(const GAS v2u*)(p.x + D + krow);
  v4u wqr[4], wgr[4], wur[4], wdr[4], wor[4];
#pragma unroll
  for (int i = 0; i < 4; ++i) wqr[i] = ldnt((const GAS v4u*)(p.wq + (size_t)(krow + i) * HD + rg * 64 + c8 * 8));
  SB();
  const int fbytes = has_ffn ? D * FF * 2 : 0;
  const __amdgpu_buffer_rsrc_t rwg = __builtin_amdgcn_make_buffer_rsrc((void*)p.wg, 0, fbytes, 0x00020000);
  const __amdgpu_buffer_rsrc_t rwu = __builtin_amdgcn_make_buffer_rsrc((void*)p.wu, 0, fbytes, 0x00020000);
  const __amdgpu_buffer_rsrc_t rwd = __builtin_amdgcn_make_buffer_rsrc((void*)p.wd, 0, fbytes, 0x00020000);
  const __amdgpu_buffer_rsrc_t rwo = __builtin_amdgcn_make_buffer_rsrc((void*)p.wo, 0, HD * D * 2, 0x00020000);
  const int guoff = (krow * FF + fi * 64 + c8 * 8) * 2;
  const int tile = ((w * 8 + (lane >> 5)) * D + cg * 256 + (lane & 31) * 8) * 2;
#define LDWG() do { _Pragma("unroll") for (int i = 0; i < 4; ++i) wgr[i] = __builtin_amdgcn_raw_buffer_load_b128(rwg, guoff + i * FF * 2, 0, AUX_NT); SB(); } while (0)
#define LDWU() do { _Pragma("unroll") for (int i = 0; i < 4; ++i) wur[i] = __builtin_amdgcn_raw_buffer_load_b128(rwu, guoff + i * FF * 2, 0, AUX_NT); SB(); } while (0)
#define LDWD() do { _Pragma("unroll") for (int i = 0; i < 4; ++i) wdr[i] = __builtin_amdgcn_raw_buffer_load_b128(rwd, tile + i * 4 * D, fi * 64 * D * 2, AUX_NT); SB(); } while (0)
#define LDWO() do { _Pragma("unroll") for (int i = 0; i < 4; ++i) wor[i] = __builtin_amdgcn_raw_buffer_load_b128(rwo, tile + i * 4 * D, rg * 64 * D * 2, AUX_NT); SB(); } while (0)
#define USEQ() do { const f32x4 r = gemv64(wqr, x0, x1); if ((lane & 8) == 0) *(LAS f32x4*)(L + L_RED + w * 512 + redoff) = r; lds_signal(FL + 0); } while (0)
#define USEG() do { const f32x4 r = gemv64(wgr, x0, x1); if ((lane & 8) == 0) *(LAS f32x4*)(L + L_RED + 4096 + w * 512 + redoff) = r; SB(); } while (0)
#define USEU() do { const f32x4 r = gemv64(wur, x0, x1); if ((lane & 8) == 0) *(LAS f32x4*)(L + L_RED + 8192 + w * 512 + redoff) = r; lds_signal(FL + 1); } while (0)
#define USED() do { _Pragma("unroll") for (int i = 0; i < 4; ++i) *(LAS v4u*)(L + L_WD + (w * 4 + i) * 1024 + lane * 16) = wdr[i]; SB(); } while (0)
#define USEO() do { _Pragma("unroll") for (int i = 0; i < 4; ++i) *(LAS v4u*)(L + L_WO + (w * 4 + i) * 1024 + lane * 16) = wor[i]; SB(); } while (0)
  LDWO();
  const int i16 = lane & 15, g4 = lane >> 4;
  const __amdgpu_buffer_rsrc_t rk = __builtin_amdgcn_make_buffer_rsrc((void*)p.kc, 0, 8 * SK * 256, 0x00020000);
  const __amdgpu_buffer_rsrc_t rv = __builtin_amdgcn_make_buffer_rsrc((void*)p.vc, 0, 8 * SK * 256, 0x00020000);
  const int klane = (4 * (i16 & 3) + (i16 >> 2)) * 256 + g4 * 16, vlane = lane * 16;
  const int rsl = (((3 - (int)((size_t)p.wg >> 7)) & 7) + grp) & 7;
  const int xs = rsl < xl ? rsl : rsl + 1;
  const int nunit = rsl == 7 ? 64 : (!has_ffn ? 64 + MV : (xq == xs ? 64 - MV : 64));
  const int obase = ((bb * 4 + hkv) * 2048 + cg * 512) * 4096;
  const int ubase = obase + xq * 262144, xbase = obase + xs * 262144 - MV * 4096;
#define UOFF(t_) (ubase + (t_) * 4096)
#define UOFFX(t_) (((t_) < 64 ? ubase : xbase) + (t_) * 4096)
  v4u ka[4], va[4], kb[4], vb[4], kc[4], vc[4];
  WG_BARRIER();
  float x0[4], x1[4];
  {
    const f32x2v mm = *(LAS f32x2v*)(L + L_FL + 32);
    x0[0] = bflo(xr0.x) - mm.x; x0[1] = bfhi(xr0.x) - mm.x; x0[2] = bflo(xr0.y) - mm.x; x0[3] = bfhi(xr0.y) - mm.x;
    x1[0] = bflo(xr1.x) - mm.y; x1[1] = bfhi(xr1.x) - mm.y; x1[2] = bflo(xr1.y) - mm.y; x1[3] = bfhi(xr1.y) - mm.y;
  }
  const int redoff = ((g4 & 1) * 64 + c8 * 8 + (g4 >> 1) * 4) * 4;

  USEQ(); LDWG();
  USEO(); LDWD();
  USEG(); LDWU();
  USED(); LDK(ka, UOFF(w));
  USEU(); LDV(va, UOFF(w));
  TOUCH4(ka);
  LDK(kb, UOFF(w + 8));
  TOUCH4(va);
  LDV(vb, UOFF(w + 8));
  TOUCH4(kb);
  LDK(kc, UOFF(w + 16));
  TOUCH4(vb);
  LDV(vc, UOFF(w + 16));
  lds_wait(FL + 3, 1);
  v4u qf[4];
#pragma unroll
  for (int kk = 0; kk < 4; ++kk) qf[kk] = *(LAS v4u*)(L + L_Q + i16 * 272 + kk * 64 + g4 * 16);

  AttnState st;
  st.m = -INFINITY;
  st.l = 0.f;
#pragma unroll
  for (int e = 0; e < 8; ++e) st.o[e] = f32x4{0.f, 0.f, 0.f, 0.f};
  attn_unit(st, ka, va, qf);
  attn_unit(st, kb, vb, qf);
  int t = w + 24;
  for (;;) {
    const int o0 = t < nunit ? UOFFX(t) : OOB;
    const int ak0 = klane + o0, av0 = vlane + o0;
    LDK2(kb, ak0);
    LDV2(vb, av0);
    asm volatile("" ::"v"(ak0), "v"(av0));
    attn_unit(st, kc, vc, qf);
    if (t >= nunit) break;
    const int o1 = t + 8 < nunit ? UOFFX(t + 8) : OOB;
    const int ak1 = klane + o1, av1 = vlane + o1;
    LDK2(kc, ak1);
    LDV2(vc, av1);
    asm volatile("" ::"v"(ak1), "v"(av1));
    attn_unit(st, kb, vb, qf);
    if (t + 8 >= nunit) break;
    t += 16;
  }
  lds_signal(FL + 2);
  const int sfoff = ((tid >> 2) & 1) * 1024 + ((tid & 127) >> 3) * 64 + (tid & 3) * 16;
  const int oidx = ((tid >> 4) & 1) * D + cg * 256 + (rg & 15) * 16 + (tid & 15);
  unsigned xo = 0;
  if (w == 0) xo = *(const GAS unsigned short*)(p.x + oidx);

  {
    const float l = x32_sum(x16_sum(st.l));
    if (i16 < 8) {
#pragma unroll
      for (int e = 0; e < 8; ++e) *(LAS f32x4*)(L + L_O + (w * 256 + (i16 * 4 + g4) * 8 + (e ^ i16)) * 16) = st.o[e];
      if (g4 == 0) *(LAS v2u*)(L + L_ML + (i16 * 8 + w) * 8) = v2u{f2u(st.m), f2u(l)};
    }
  }
  int poff = g4 * RSP + WS_P1 + (hkv * 8 + w) * 4096 + (rg & 15) * 256 + i16 * 16;
  int goff = (lane >> 3) * RSG + WS_GU + rg * 1024 + ((lane >> 2) & 1) * 512 + (w * 8 + (lane & 3) * 2) * 8;
  int roff = ((tid >> 3) & 7) * RSS + (cg * 8 + (tid >> 6)) * 2048 + (rg & 15) * 128 + (tid & 7) * 16;
  int p1st = p1own + tid * 16;
  int s2st = (w < 2 ? WS_S2 : WS_F2) + s2own + (tid & 127) * 16;
  __builtin_amdgcn_s_waitcnt(0x0F70);
  asm volatile("" : "+v"(poff), "+v"(goff), "+v"(roff), "+v"(p1st), "+v"(s2st));
  WG_BARRIER();
  if (w < 4) {
    const int j = tid >> 5;
    v4u ml[4];
    f32x4 ov[8];
#pragma unroll
    for (int k = 0; k < 4; ++k) ml[k] = *(LAS v4u*)(L + L_ML + j * 64 + k * 16);
#pragma unroll
    for (int ww = 0; ww < 8; ++ww) ov[ww] = *(LAS f32x4*)(L + L_O + (ww * 256 + (tid ^ j)) * 16);
    SB();
    float M = fmaxf(u2f(ml[0].x), u2f(ml[0].z));
#pragma unroll
    for (int k = 1; k < 4; ++k) M = fmaxf(M, fmaxf(u2f(ml[k].x), u2f(ml[k].z)));
    float Ls = 0.f;
    f32x4 acc = {0.f, 0.f, 0.f, 0.f};
#pragma unroll
    for (int ww = 0; ww < 8; ++ww) {
      const float wt = __builtin_amdgcn_exp2f(u2f(ml[ww >> 1][(ww & 1) * 2]) - M);
      Ls = fmaf(wt, u2f(ml[ww >> 1][(ww & 1) * 2 + 1]), Ls);
      acc += ov[ww] * wt;
    }
    acc *= rcp(Ls);
    const unsigned lse = f2u(M + __builtin_amdgcn_logf(Ls));
    v4u pk;
#pragma unroll
    for (int r = 0; r < 4; ++r) {
      const unsigned t = ((f2u(acc[r]) + 0x80u) & 0xFFFFFF00u) | ((lse >> (8 * r)) & 0xFFu);
      pk[r] = t == SENT ? 0x7FC000FFu : t;
    }
    __builtin_amdgcn_raw_buffer_store_b128(pk, rs, p1st, 0, AUX_SC1);
  }
  SB();

  f32x4 wof[8];
  {
    v4u gu = __builtin_amdgcn_raw_buffer_load_b128(rs, goff, 0, AUX_SC1);
    v4u pa = __builtin_amdgcn_raw_buffer_load_b128(rs, poff, 0, AUX_SC1);
    v4u pb = __builtin_amdgcn_raw_buffer_load_b128(rs, poff, 4 * RSP, AUX_SC1);
    SB();
    if (has_ffn) {
      const unsigned e_lo = FL[6], e_hi = FL[7];
      const unsigned tag_hi = (e_lo >> 8) | (e_hi << 24), tag_lo = e_lo & 0xFFu;
      v2u wt[8];
#pragma unroll
      for (int k = 0; k < 8; ++k) wt[k] = *(LAS v2u*)(L + L_WD + (w * 8 + k) * 512 + lane * 8);
      if (!__all(tag2(gu, tag_lo, tag_hi))) {
#pragma nounroll
        for (unsigned spins = 0; spins < SPIN_CAP; ++spins) {
          asm volatile("" ::: "memory");
          __builtin_amdgcn_s_sleep(1);
          gu = __builtin_amdgcn_raw_buffer_load_b128(rs, goff, 0, AUX_SC1);
          if (__all(tag2(gu, tag_lo, tag_hi))) break;
        }
      }
      const float ga = x32_sum(x16_sum(u2f(gu.x << 8))), gb = x32_sum(x16_sum(u2f(gu.z << 8)));
      const float hx = silu(ga) * dpp<XOR8>(ga), hy = silu(gb) * dpp<XOR8>(gb);
      float h0[8], h1[8];
#pragma unroll
      for (int pr = 0; pr < 4; ++pr) {
        h0[2 * pr] = rdl(hx, pr); h0[2 * pr + 1] = rdl(hy, pr);
        h1[2 * pr] = rdl(hx, 4 + pr); h1[2 * pr + 1] = rdl(hy, 4 + pr);
      }
      f32x4 s0, s1;
      gemv256(wt, h0, h1, s0, s1);
      *(LAS f32x4*)(L + L_F + w * 2048 + lane * 16) = s0;
      *(LAS f32x4*)(L + L_F + w * 2048 + 1024 + lane * 16) = s1;
    }
#pragma unroll
    for (int k = 0; k < 8; ++k) {
      const v2u t = *(LAS v2u*)(L + L_WO + (w * 8 + k) * 512 + lane * 8);
      wof[k] = f32x4{bflo(t.x), bfhi(t.x), bflo(t.y), bfhi(t.y)};
      asm volatile("" : "+v"(wof[k]));
    }
    bool ha = full4(pa), hb = full4(pb);
#pragma nounroll
    for (unsigned spins = 0; spins < SPIN_CAP; ++spins) {
      asm volatile("" ::: "memory");
      if (__all(ha & hb)) break;
      if (!ha) pa = __builtin_amdgcn_raw_buffer_load_b128(rs, poff, 0, AUX_SC1);
      if (!hb) pb = __builtin_amdgcn_raw_buffer_load_b128(rs, poff, 4 * RSP, AUX_SC1);
      ha = full4(pa);
      hb = full4(pb);
    }
    const float la = u2f(__builtin_amdgcn_perm(__builtin_amdgcn_perm(pa.w, pa.z, 0x0c0c0400u), __builtin_amdgcn_perm(pa.y, pa.x, 0x0c0c0400u), 0x05040100u));
    const float lb = u2f(__builtin_amdgcn_perm(__builtin_amdgcn_perm(pb.w, pb.z, 0x0c0c0400u), __builtin_amdgcn_perm(pb.y, pb.x, 0x0c0c0400u), 0x05040100u));
    const float Mw = x32_max(x16_max(fmaxf(la, lb)));
    const float wa = __builtin_amdgcn_exp2f(la - Mw), wb = __builtin_amdgcn_exp2f(lb - Mw);
    const float Ww = x32_sum(x16_sum(wa + wb));
    float a[4];
#pragma unroll
    for (int r = 0; r < 4; ++r) a[r] = fmaf(u2f(pa[r] & 0xFFFFFF00u), wa, u2f(pb[r] & 0xFFFFFF00u) * wb);
    const float v = tr32(tr16(a[0], a[1]), tr16(a[2], a[3]));
    *(LAS float*)(L + L_C + (((w >> 2) * 64 + (i16 >> 3) * 32 + g4 * 8 + (i16 & 7)) * 4 + (w & 3)) * 4) = v;
    if (lane == 0) *(LAS v2u*)(L + L_C + 2048 + w * 8) = v2u{f2u(Mw), f2u(Ww)};
  }
  WG_BARRIER();
  {
    const int b = (lane >> 3) & 1, k = lane & 7;
    const v4u m0 = *(LAS v4u*)(L + L_C + 2048 + b * 32), m1 = *(LAS v4u*)(L + L_C + 2048 + b * 32 + 16);
    const f32x4 av = *(LAS f32x4*)(L + L_C + (b * 64 + w * 8 + k) * 16);
    const float M = fmaxf(fmaxf(u2f(m0.x), u2f(m0.z)), fmaxf(u2f(m1.x), u2f(m1.z)));
    const float f0 = __builtin_amdgcn_exp2f(u2f(m0.x) - M), f1 = __builtin_amdgcn_exp2f(u2f(m0.z) - M);
    const float f2 = __builtin_amdgcn_exp2f(u2f(m1.x) - M), f3 = __builtin_amdgcn_exp2f(u2f(m1.z) - M);
    const float num = fmaf(f0, av.x, f1 * av.y) + fmaf(f2, av.z, f3 * av.w);
    const float den = fmaf(f0, u2f(m0.y), f1 * u2f(m0.w)) + fmaf(f2, u2f(m1.y), f3 * u2f(m1.w));
    const float ov = num * rcp(den);
    f32x4 s0 = {0.f, 0.f, 0.f, 0.f}, s1 = {0.f, 0.f, 0.f, 0.f};
#pragma unroll
    for (int kk = 0; kk < 8; ++kk) { s0 += wof[kk] * rdl(ov, kk); s1 += wof[kk] * rdl(ov, 8 + kk); }
    *(LAS f32x4*)(L + L_S + w * 2048 + lane * 16) = s0;
    *(LAS f32x4*)(L + L_S + w * 2048 + 1024 + lane * 16) = s1;
  }
  WG_BARRIER();
  if (w < 2 || (w < 4 && has_ffn)) {
    const int lb = (w < 2 ? L_S : L_F) + sfoff;
    f32x4 t[8];
#pragma unroll
    for (int ww = 0; ww < 8; ++ww) t[ww] = *(LAS f32x4*)(L + lb + ww * 2048);
    SB();
    const f32x4 s4 = ((t[0] + t[1]) + (t[2] + t[3])) + ((t[4] + t[5]) + (t[6] + t[7]));
    __builtin_amdgcn_raw_buffer_store_b128(clean4(s4), rs, s2st, 0, AUX_SC1);
  }

  if (reducer) {
    v4u a = __builtin_amdgcn_raw_buffer_load_b128(rs, roff, WS_S2, AUX_SC1);
    v4u c = {0u, 0u, 0u, 0u};
    if (((tid >> 3) & 7) != xl) c = __builtin_amdgcn_raw_buffer_load_b128(rs, roff, WS_F2, AUX_SC1);
    bool ha = full4(a), hc = full4(c);
#pragma nounroll
    for (unsigned spins = 0; spins < SPIN_CAP; ++spins) {
      asm volatile("" ::: "memory");
      if (__all(ha & hc)) break;
      if (!ha) a = __builtin_amdgcn_raw_buffer_load_b128(rs, roff, WS_S2, AUX_SC1);
      if (!hc) c = __builtin_amdgcn_raw_buffer_load_b128(rs, roff, WS_F2, AUX_SC1);
      ha = full4(a);
      hc = full4(c);
    }
    float vs = tr32(tr16(u2f(a.x), u2f(a.y)), tr16(u2f(a.z), u2f(a.w)));
    float vf = tr32(tr16(u2f(c.x), u2f(c.y)), tr16(u2f(c.z), u2f(c.w)));
    vs += dpp<XOR8>(vs);
    vf += dpp<XOR8>(vf);
    *(LAS float*)(L + L_R + ((lane >> 3) & 1) * 1024 + ((lane & 7) * 4 + (lane >> 4)) * 32 + w * 4) = (lane & 8) ? vs : vf;
    WG_BARRIER();
    if (tid < 32) {
      const f32x4 f0 = *(LAS f32x4*)(L + L_R + tid * 32), f1 = *(LAS f32x4*)(L + L_R + tid * 32 + 16);
      const f32x4 s0 = *(LAS f32x4*)(L + L_R + 1024 + tid * 32), s1 = *(LAS f32x4*)(L + L_R + 1024 + tid * 32 + 16);
      const f32x4 f4 = f0 + f1, s4 = s0 + s1;
      const float f = (f4.x + f4.y) + (f4.z + f4.w), sv = (s4.x + s4.y) + (s4.z + s4.w);
      const float r = fmaf(sv, silu(f), bflo(xo));
      p.out[oidx] = (unsigned short)(cvtpk(r, 0.f) & 0xFFFFu);
    }
    if (bid == NWG - 1 && tid == 0) {
      const v2u ne = {FL[6], FL[7]};
      __builtin_amdgcn_raw_buffer_store_b64(ne, rs, WS_E, 0, AUX_SC1);
    }
  }
}

}

extern "C" void kernel_launch(void* const* d_in, const int* in_sizes, int n_in, void* d_out, int out_size, void* d_ws,
                              size_t ws_size, hipStream_t stream) {
  Params p;
  p.x = (const unsigned short*)d_in[0];
  p.kc = (const unsigned short*)d_in[1];
  p.vc = (const unsigned short*)d_in[2];
  p.wq = (const unsigned short*)d_in[3];
  p.wo = (const unsigned short*)d_in[4];
  p.wg = (const unsigned short*)d_in[6];
  p.wu = (const unsigned short*)d_in[7];
  p.wd = (const unsigned short*)d_in[8];
  p.out = (unsigned short*)d_out;
  p.ws = (unsigned char*)d_ws;
  hipLaunchKernelGGL(fused_decode, dim3(NWG), dim3(NTHR), 0, stream, p);
}
```

```cpp
#include <hip/hip_runtime.h>
#include <hip/hip_bf16.h>
#include <stdint.h>

#define GAS __attribute__((address_space(1)))
#define LAS __attribute__((address_space(3)))
typedef unsigned v4u __attribute__((ext_vector_type(4)));
typedef unsigned v2u __attribute__((ext_vector_type(2)));
typedef float f32x4 __attribute__((ext_vector_type(4)));
typedef short bf16x8 __attribute__((ext_vector_type(8)));
typedef short bf16x4 __attribute__((ext_vector_type(4)));
typedef unsigned long long u64;

namespace {

constexpr int NWG = 256, NTHR = 576;
constexpr int D = 1024, HD = 4096, FF = 3584, SK = 32768;
constexpr int AUX_SC1 = 16;
constexpr unsigned SENT = 0xFFFFFFFFu;
constexpr unsigned SPIN_CAP = 1u << 22;

#define COMPACT 1
constexpr int RSQ = 32768, RSG = 65536, RSP = 131072, RSS = 65536;
constexpr int WS_E = 0;
constexpr int WS_QP = 4096;
constexpr int WS_GU = WS_QP + 8 * RSQ;
constexpr int WS_P1 = WS_GU + 8 * RSG;
constexpr int WS_S2 = WS_P1 + 8 * RSP;
constexpr int WS_F2 = WS_S2 + 8 * RSS;
constexpr int WS_QF = WS_F2 + 8 * RSS;
constexpr int WS_END = WS_QF + 65536;
constexpr int WS_ALL = WS_END;

constexpr int L_RED = 0;
constexpr int L_Q = 12288;
constexpr int L_ML = L_Q + 16 * 272;
constexpr int L_O = L_ML + 512;
constexpr int L_S = L_O + 32768;
constexpr int L_C = L_S + 16384;
constexpr int L_R = L_C + 2048 + 64;
constexpr int L_H = L_R + 2048;
constexpr int L_FL = L_H + 512;
constexpr int L_WO = L_FL + 64;
constexpr int L_WD = L_WO + 32768;
constexpr int L_F = L_WD + 32768;
constexpr int L_USED = L_F + 16384;
constexpr int L_BYTES = 152 * 1024;
static_assert(L_USED <= L_BYTES, "lds");

struct Params {
  const unsigned short* x;
  const unsigned short* wq;
  unsigned char* ws;
  const unsigned short* wg;
  const unsigned short* wu;
  const unsigned short* wd;
  const unsigned short* wo;
  const unsigned short* kc;
  const unsigned short* vc;
  unsigned short* out;
};

__device__ __forceinline__ float bflo(unsigned u) { return __builtin_bit_cast(float, u << 16); }
__device__ __forceinline__ float bfhi(unsigned u) { return __builtin_bit_cast(float, u & 0xFFFF0000u); }
__device__ __forceinline__ unsigned f2u(float f) { return __builtin_bit_cast(unsigned, f); }
__device__ __forceinline__ float u2f(unsigned u) { return __builtin_bit_cast(float, u); }
typedef __bf16 bf16p __attribute__((ext_vector_type(2)));
typedef float f32x2 __attribute__((ext_vector_type(2)));
__device__ __forceinline__ unsigned cvtpk(float lo, float hi) {
  return __builtin_bit_cast(unsigned, __builtin_convertvector(f32x2{lo, hi}, bf16p));
}
template <int CTRL> __device__ __forceinline__ float dpp(float x) {
  return __builtin_bit_cast(float, __builtin_amdgcn_mov_dpp(__builtin_bit_cast(int, x), CTRL, 0xf, 0xf, true));
}
constexpr int XOR1 = 0xB1, XOR2 = 0x4E, XOR7 = 0x141, XOR8 = 0x128, ROR4 = 0x124, XOR15 = 0x140;
__device__ __forceinline__ float x16_sum(float x) {
  auto s = __builtin_amdgcn_permlane16_swap(f2u(x), f2u(x), false, false);
  return u2f(s[0]) + u2f(s[1]);
}
__device__ __forceinline__ float x32_sum(float x) {
  auto s = __builtin_amdgcn_permlane32_swap(f2u(x), f2u(x), false, false);
  return u2f(s[0]) + u2f(s[1]);
}
__device__ __forceinline__ float x16_max(float x) {
  auto s = __builtin_amdgcn_permlane16_swap(f2u(x), f2u(x), false, false);
  return fmaxf(u2f(s[0]), u2f(s[1]));
}
__device__ __forceinline__ float x32_max(float x) {
  auto s = __builtin_amdgcn_permlane32_swap(f2u(x), f2u(x), false, false);
  return fmaxf(u2f(s[0]), u2f(s[1]));
}
__device__ __forceinline__ float row16_sum(float x) {
  x += dpp<XOR1>(x);
  x += dpp<XOR2>(x);
  x += dpp<XOR7>(x);
  x += dpp<XOR8>(x);
  return x;
}
__device__ __forceinline__ float row16_max(float x) {
  x = fmaxf(x, dpp<XOR1>(x));
  x = fmaxf(x, dpp<XOR2>(x));
  x = fmaxf(x, dpp<XOR7>(x));
  x = fmaxf(x, dpp<XOR8>(x));
  return x;
}
__device__ __forceinline__ float wave_sum(float x) { return x32_sum(x16_sum(row16_sum(x))); }
__device__ __forceinline__ float tr16(float a, float b) {
  auto s = __builtin_amdgcn_permlane16_swap(f2u(a), f2u(b), false, false);
  return u2f(s[0]) + u2f(s[1]);
}
__device__ __forceinline__ float tr32(float a, float b) {
  auto s = __builtin_amdgcn_permlane32_swap(f2u(a), f2u(b), false, false);
  return u2f(s[0]) + u2f(s[1]);
}
__device__ __forceinline__ v4u ldnt(const GAS v4u* p) { return __builtin_nontemporal_load(p); }
__device__ __forceinline__ v2u ldnt2(const GAS v2u* p) { return __builtin_nontemporal_load(p); }
__device__ __forceinline__ unsigned clean(float f) {
  const unsigned u = f2u(f);
  return u == SENT ? 0x7FC00000u : u;
}
__device__ __forceinline__ v4u clean4(f32x4 v) { return v4u{clean(v.x), clean(v.y), clean(v.z), clean(v.w)}; }
__device__ __forceinline__ bool full4(v4u v) { return v.x != SENT && v.y != SENT && v.z != SENT && v.w != SENT; }
__device__ __forceinline__ float rdl(float v, int lane) { return u2f(__builtin_amdgcn_readlane(f2u(v), lane)); }
__device__ __forceinline__ float rcp(float v) { return __builtin_amdgcn_rcpf(v); }
__device__ __forceinline__ float silu(float v) { return v * rcp(1.0f + __builtin_amdgcn_exp2f(v * -1.4426950408889634f)); }

#define SB() __builtin_amdgcn_sched_barrier(0)
#define WG_BARRIER()                                        \
  do {                                                      \
    asm volatile("s_waitcnt lgkmcnt(0)" ::: "memory");      \
    __builtin_amdgcn_s_barrier();                           \
    asm volatile("" ::: "memory");                          \
  } while (0)
#define STAMP(i)
#define STAMP_S(i)

__device__ __forceinline__ f32x4 gemv64(const v4u (&w)[4], const float (&x0)[4], const float (&x1)[4]) {
  float a0[8], a1[8];
#pragma unroll
  for (int c = 0; c < 8; ++c) { a0[c] = 0.f; a1[c] = 0.f; }
#pragma unroll
  for (int i = 0; i < 4; ++i) {
#pragma unroll
    for (int k = 0; k < 4; ++k) {
      const float lo = bflo(w[i][k]), hi = bfhi(w[i][k]);
      a0[2 * k] = fmaf(lo, x0[i], a0[2 * k]);
      a0[2 * k + 1] = fmaf(hi, x0[i], a0[2 * k + 1]);
      a1[2 * k] = fmaf(lo, x1[i], a1[2 * k]);
      a1[2 * k + 1] = fmaf(hi, x1[i], a1[2 * k + 1]);
    }
  }
  float t[8];
#pragma unroll
  for (int c = 0; c < 8; ++c) t[c] = tr16(a0[c], a1[c]);
  f32x4 r;
#pragma unroll
  for (int c = 0; c < 4; ++c) {
    float v = tr32(t[c], t[c + 4]);
    v += dpp<XOR8>(v);
    r[c] = v;
  }
  return r;
}

__device__ __forceinline__ void gemv256(const v2u (&w)[8], const float (&c0)[8], const float (&c1)[8], f32x4& s0, f32x4& s1) {
  s0 = f32x4{0.f, 0.f, 0.f, 0.f};
  s1 = f32x4{0.f, 0.f, 0.f, 0.f};
#pragma unroll
  for (int k = 0; k < 8; ++k) {
    const f32x4 wf = {bflo(w[k].x), bfhi(w[k].x), bflo(w[k].y), bfhi(w[k].y)};
#pragma unroll
    for (int c = 0; c < 4; ++c) {
      s0[c] = fmaf(wf[c], c0[k], s0[c]);
      s1[c] = fmaf(wf[c], c1[k], s1[c]);
    }
  }
}

__device__ __forceinline__ v2u granule(float v, unsigned tag_lo, unsigned tag_hi) {
  return v2u{(tag_lo << 24) | ((f2u(v) + 0x80u) >> 8), tag_hi};
}
__device__ __forceinline__ bool tag2(v4u g, unsigned tag_lo, unsigned tag_hi) {
  return (g.y == tag_hi) & ((g.x >> 24) == tag_lo) & (g.w == tag_hi) & ((g.z >> 24) == tag_lo);
}
#define TOUCH4(a) asm volatile("" : "+v"(a[0]), "+v"(a[1]), "+v"(a[2]), "+v"(a[3]))
#define TOUCH8(a) asm volatile("" : "+v"(a[0]), "+v"(a[1]), "+v"(a[2]), "+v"(a[3]), "+v"(a[4]), "+v"(a[5]), "+v"(a[6]), "+v"(a[7]))
constexpr int AUX_NT = 2;
#define LDK(kb_, off_) do { _Pragma("unroll") for (int j = 0; j < 4; ++j) kb_[j] = __builtin_amdgcn_raw_buffer_load_b128(rk, (klane + (off_)) + j * 64, 0, AUX_NT); SB(); } while (0)
#define LDV(vb_, off_) do { _Pragma("unroll") for (int j = 0; j < 4; ++j) vb_[j] = __builtin_amdgcn_raw_buffer_load_b128(rv, (vlane + (off_)) + j * 1024, 0, AUX_NT); SB(); } while (0)
#define LDK2(kb_, a_) do { _Pragma("unroll") for (int j = 0; j < 4; ++j) kb_[j] = __builtin_amdgcn_raw_buffer_load_b128(rk, (a_) + j * 64, 0, AUX_NT); SB(); } while (0)
#define LDV2(vb_, a_) do { _Pragma("unroll") for (int j = 0; j < 4; ++j) vb_[j] = __builtin_amdgcn_raw_buffer_load_b128(rv, (a_) + j * 1024, 0, AUX_NT); SB(); } while (0)

struct AttnState {
  float m, l;
  f32x4 o[8];
};
__device__ __forceinline__ void attn_unit(AttnState& st, v4u (&kc)[4], v4u (&vc)[4], const v4u (&qf)[4]) {
  constexpr float C2 = 0.088388347648318440f * 1.4426950408889634f;
  f32x4 s = {0.f, 0.f, 0.f, 0.f};
#pragma unroll
  for (int kk = 0; kk < 4; ++kk)
    s = __builtin_amdgcn_mfma_f32_16x16x32_bf16(__builtin_bit_cast(bf16x8, kc[kk]), __builtin_bit_cast(bf16x8, qf[kk]), s, 0, 0, 0);
#pragma unroll
  for (int r = 0; r < 4; ++r) s[r] = (s[r] + dpp<XOR8>(s[r])) * C2;
  float mx = fmaxf(fmaxf(s[0], s[1]), fmaxf(s[2], s[3]));
  mx = x32_max(x16_max(mx));
  const float mn = fmaxf(st.m, mx);
  const float alpha = __builtin_amdgcn_exp2f(st.m - mn);
  st.m = mn;
  const float p0 = __builtin_amdgcn_exp2f(s[0] - mn), p1 = __builtin_amdgcn_exp2f(s[1] - mn);
  const float p2 = __builtin_amdgcn_exp2f(s[2] - mn), p3 = __builtin_amdgcn_exp2f(s[3] - mn);
  st.l = fmaf(st.l, alpha, (p0 + p1) + (p2 + p3));
  const v2u pb = {cvtpk(p0, p1), cvtpk(p2, p3)};
#pragma unroll
  for (int e = 0; e < 8; ++e) st.o[e] *= alpha;
  SB();
#pragma unroll
  for (int e = 0; e < 8; ++e) {
    const unsigned sel = (e & 1) ? 0x07060302u : 0x05040100u;
    const v2u a = {__builtin_amdgcn_perm(vc[1][e >> 1], vc[0][e >> 1], sel), __builtin_amdgcn_perm(vc[3][e >> 1], vc[2][e >> 1], sel)};
    st.o[e] = __builtin_amdgcn_mfma_f32_16x16x16bf16_1k(__builtin_bit_cast(bf16x4, a), __builtin_bit_cast(bf16x4, pb), st.o[e], 0, 0, 0);
  }
  SB();
}

#define DRAIN() asm volatile("s_waitcnt vmcnt(0)" ::: "memory")
#define GUWAIT 4
#define HEAD3 1
#define QSTAGE 1
#define BAL 0
#define LNSVC 1
typedef float f32x2v __attribute__((ext_vector_type(2)));
__device__ __forceinline__ void lds_signal(LAS unsigned* c) {
  asm volatile("s_waitcnt lgkmcnt(0)" ::: "memory");
  if ((threadIdx.x & 63) == 0) __hip_atomic_fetch_add(c, 1u, __ATOMIC_RELAXED, __HIP_MEMORY_SCOPE_WORKGROUP);
}
__device__ __forceinline__ void lds_wait(LAS unsigned* c, unsigned want) {
#pragma nounroll
  for (unsigned spins = 0; spins < (SPIN_CAP << 3); ++spins) {
    asm volatile("" ::: "memory");
    if (__builtin_amdgcn_readfirstlane(*(volatile LAS unsigned*)c) >= want) break;
    __builtin_amdgcn_s_sleep(1);
  }
  asm volatile("" ::: "memory");
}

__global__ void __launch_bounds__(NTHR) fused_decode(Params p) {
  __shared__ __attribute__((aligned(16))) unsigned char smem[L_BYTES];
  LAS unsigned char* L = (LAS unsigned char*)smem;
  LAS unsigned* FL = (LAS unsigned*)(L + L_FL);
  const int tid = (int)threadIdx.x - 64, lane = threadIdx.x & 63;
  const int w = __builtin_amdgcn_readfirstlane(threadIdx.x >> 6) - 1;
  const int bid = blockIdx.x;
  const int grp = bid >> 5, xq = bid & 7;
  const int cg = (bid >> 3) & 3, rg = grp * 8 + xq;
  const int hkv = bid >> 6, bb = grp & 1, sp = bid & 31;
  const bool has_ffn = xq != grp;
  const int fi = has_ffn ? grp * 7 + (xq < grp ? xq : xq - 1) : 0;
  const bool reducer = rg >= 48;
  constexpr int OOB = 0x7FFF0000;
  const __amdgpu_buffer_rsrc_t rs = __builtin_amdgcn_make_buffer_rsrc((void*)p.ws, 0, WS_ALL, 0x00020000);
  const int p1own = (bid & 7) * RSP + WS_P1 + (bid >> 3) * 4096;
  const int s2own = (rg & 7) * RSS + (cg * 8 + (rg >> 3)) * 2048;
  const GAS v4u* xg = (const GAS v4u*)p.x;
  STAMP(0);

  if (w < 0) {
    __builtin_amdgcn_s_setprio(3);
    const v4u xa0 = xg[lane * 2], xa1 = xg[lane * 2 + 1], xb0 = xg[128 + lane * 2], xb1 = xg[128 + lane * 2 + 1];
    const v2u ev = __builtin_amdgcn_raw_buffer_load_b64(rs, WS_E, 0, AUX_SC1);
    if (lane < 6) FL[lane] = 0u;
    float rr;
    {
      float va_[16], vb_[16];
#pragma unroll
      for (int k = 0; k < 4; ++k) {
        va_[2 * k] = bflo(xa0[k]); va_[2 * k + 1] = bfhi(xa0[k]); va_[8 + 2 * k] = bflo(xa1[k]); va_[8 + 2 * k + 1] = bfhi(xa1[k]);
        vb_[2 * k] = bflo(xb0[k]); vb_[2 * k + 1] = bfhi(xb0[k]); vb_[8 + 2 * k] = bflo(xb1[k]); vb_[8 + 2 * k + 1] = bfhi(xb1[k]);
      }
      float sa = 0.f, sb = 0.f;
#pragma unroll
      for (int k = 0; k < 16; ++k) { sa += va_[k]; sb += vb_[k]; }
      const float ma = wave_sum(sa) * (1.0f / D), mb = wave_sum(sb) * (1.0f / D);
      if (lane == 0) *(LAS f32x2v*)(L + L_FL + 32) = f32x2v{ma, mb};
      WG_BARRIER();
      float qa = 0.f, qb = 0.f;
#pragma unroll
      for (int k = 0; k < 16; ++k) { const float da = va_[k] - ma, db = vb_[k] - mb; qa = fmaf(da, da, qa); qb = fmaf(db, db, qb); }
      const float ta = wave_sum(qa), tb = wave_sum(qb);
      rr = 1.0f / sqrtf((lane < 32 ? ta : tb) * (1.0f / D) + 1e-5f);
    }
    const u64 e1 = ((((u64)__builtin_amdgcn_readfirstlane(ev.y)) << 32) | (u64)(unsigned)__builtin_amdgcn_readfirstlane(ev.x)) + 1ull;
    const unsigned tag_hi = (unsigned)(e1 >> 8), tag_lo = (unsigned)(e1 & 0xFFull);
    if (lane == 0) { FL[6] = (unsigned)e1; FL[7] = (unsigned)(e1 >> 32); }
    lds_wait(FL + 0, 8);
    {
      f32x2v a = {0.f, 0.f};
#pragma unroll
      for (int ww = 0; ww < 8; ++ww) a += *(LAS f32x2v*)(L + L_RED + ww * 512 + lane * 8);
      const v2u g0 = granule(a.x * rr, tag_lo, tag_hi), g1 = granule(a.y * rr, tag_lo, tag_hi);
      __builtin_amdgcn_raw_buffer_store_b128(v4u{g0.x, g0.y, g1.x, g1.y}, rs,
          ((rg & 15) >> 1) * RSQ + WS_QP + ((hkv * 2 + (lane >> 5)) * 4 + cg) * 1024 + (rg & 1) * 512 + (lane & 31) * 16, 0, AUX_SC1);
    }
    STAMP_S(1);
    DRAIN();
    {
      const int qoff = WS_QP + (hkv * 2 + bb) * 4096 + lane * 16;
      v4u qg[32];
#pragma nounroll
      for (unsigned spins = 0; spins < SPIN_CAP; ++spins) {
        asm volatile("" ::: "memory");
        bool ok = true;
#pragma unroll
        for (int j = 0; j < 32; ++j) qg[j] = __builtin_amdgcn_raw_buffer_load_b128(rs, qoff + (j >> 3) * 1024, (j & 7) * RSQ, AUX_SC1);
#pragma unroll
        for (int j = 0; j < 32; ++j) ok &= tag2(qg[j], tag_lo, tag_hi);
        if (__all(ok)) break;
        __builtin_amdgcn_s_sleep(4);
      }
#pragma unroll
      for (int j = 0; j < 8; ++j) {
        const float q0 = (u2f(qg[j].x << 8) + u2f(qg[8 + j].x << 8)) + (u2f(qg[16 + j].x << 8) + u2f(qg[24 + j].x << 8));
        const float q1 = (u2f(qg[j].z << 8) + u2f(qg[8 + j].z << 8)) + (u2f(qg[16 + j].z << 8) + u2f(qg[24 + j].z << 8));
        const unsigned hi = cvtpk(q0, q1);
        const unsigned lo = cvtpk(q0 - bflo(hi), q1 - bfhi(hi));
        *(LAS unsigned*)(L + L_Q + j * 272 + lane * 4) = hi;
        *(LAS unsigned*)(L + L_Q + (j + 8) * 272 + lane * 4) = lo;
      }
      lds_signal(FL + 3);
    }
    const int coff = ((lane & 15) >> 1) * RSQ + WS_QP + (((lane >> 4) * 2) * 4 + cg) * 1024 + (lane & 1) * 512;
    STAMP_S(2);
    if (reducer) {
#pragma nounroll
      for (unsigned spins = 0; spins < SPIN_CAP; ++spins) {
        asm volatile("" ::: "memory");
        const v4u ck = __builtin_amdgcn_raw_buffer_load_b128(rs, coff, 0, AUX_SC1);
        if (__all(tag2(ck, tag_lo, tag_hi))) break;
        __builtin_amdgcn_s_sleep(4);
      }
    }
    if (has_ffn) {
      lds_wait(FL + 1, 8);
      v4u gr[2];
#pragma unroll
      for (int m = 0; m < 2; ++m) {
        f32x2v a = {0.f, 0.f};
#pragma unroll
        for (int ww = 0; ww < 8; ++ww) a += *(LAS f32x2v*)(L + L_RED + 4096 + m * 4096 + ww * 512 + lane * 8);
        const v2u g0 = granule(a.x * rr, tag_lo, tag_hi), g1 = granule(a.y * rr, tag_lo, tag_hi);
        gr[m] = v4u{g0.x, g0.y, g1.x, g1.y};
      }
      lds_wait(FL + 2, GUWAIT);
#pragma unroll
      for (int m = 0; m < 2; ++m) __builtin_amdgcn_raw_buffer_store_b128(gr[m], rs, (cg * 2 + m) * RSG + WS_GU + rg * 1024 + lane * 16, 0, AUX_SC1);
    }
    STAMP_S(4);
    WG_BARRIER();
    return;
  }

  {
    const v4u s4 = {SENT, SENT, SENT, SENT};
    const int off = w < 4 ? p1own + w * 1024 : (w < 6 ? WS_S2 - 4096 : WS_F2 - 6144) + s2own + w * 1024;
    __builtin_amdgcn_raw_buffer_store_b128(s4, rs, off + lane * 16, 0, AUX_SC1);
    SB();
  }
  const int r8 = lane >> 3, c8 = lane & 7;
  const int krow = cg * 256 + w * 32 + r8 * 4;
  const v2u xr0 = *(const GAS v2u*)(p.x + krow), xr1 = *(const GAS v2u*)(p.x + D + krow);
  v4u wqr[4], wgr[4], wur[4], wdr[4], wor[4];
#pragma unroll
  for (int i = 0; i < 4; ++i) wqr[i] = ldnt((const GAS v4u*)(p.wq + (size_t)(krow + i) * HD + rg * 64 + c8 * 8));
  SB();
  const int fbytes = has_ffn ? D * FF * 2 : 0;
  const __amdgpu_buffer_rsrc_t rwg = __builtin_amdgcn_make_buffer_rsrc((void*)p.wg, 0, fbytes, 0x00020000);
  const __amdgpu_buffer_rsrc_t rwu = __builtin_amdgcn_make_buffer_rsrc((void*)p.wu, 0, fbytes, 0x00020000);
  const __amdgpu_buffer_rsrc_t rwd = __builtin_amdgcn_make_buffer_rsrc((void*)p.wd, 0, fbytes, 0x00020000);
  const __amdgpu_buffer_rsrc_t rwo = __builtin_amdgcn_make_buffer_rsrc((void*)p.wo, 0, HD * D * 2, 0x00020000);
  const int guoff = (krow * FF + fi * 64 + c8 * 8) * 2;
#pragma unroll
  for (int i = 0; i < 4; ++i) wgr[i] = __builtin_amdgcn_raw_buffer_load_b128(rwg, guoff + i * FF * 2, 0, AUX_NT);
  SB();
  const int tile = ((w * 8 + (lane >> 5)) * D + cg * 256 + (lane & 31) * 8) * 2;
  const int i16 = lane & 15, g4 = lane >> 4;
  const __amdgpu_buffer_rsrc_t rk = __builtin_amdgcn_make_buffer_rsrc((void*)p.kc, 0, 8 * SK * 256, 0x00020000);
  const __amdgpu_buffer_rsrc_t rv = __builtin_amdgcn_make_buffer_rsrc((void*)p.vc, 0, 8 * SK * 256, 0x00020000);
  const int klane = (4 * (i16 & 3) + (i16 >> 2)) * 256 + g4 * 16, vlane = lane * 16;
  const int fj = xq < grp ? xq : xq - 1;
  const int nunit = has_ffn ? 64 - BAL / 7 - (fj < BAL % 7 ? 1 : 0) : 64 + BAL;
  const int obase = ((bb * 4 + hkv) * 2048 + cg * 512) * 4096;
  const int ubase = obase + xq * 262144;
#define UOFF(t_) (ubase + (t_) * 4096)
#define UOFFX(t_) ((t_) < 64 ? UOFF(t_) : obase + ((((t_) - 64) % 7) + ((((t_) - 64) % 7) >= grp ? 1 : 0)) * 262144 + (63 - ((t_) - 64) / 7) * 4096)
  v4u ka[4], va[4], kb[4], vb[4], kc[4], vc[4];
  WG_BARRIER();
  float x0[4], x1[4];
  {
    const f32x2v mm = *(LAS f32x2v*)(L + L_FL + 32);
    x0[0] = bflo(xr0.x) - mm.x; x0[1] = bfhi(xr0.x) - mm.x; x0[2] = bflo(xr0.y) - mm.x; x0[3] = bfhi(xr0.y) - mm.x;
    x1[0] = bflo(xr1.x) - mm.y; x1[1] = bfhi(xr1.x) - mm.y; x1[2] = bflo(xr1.y) - mm.y; x1[3] = bfhi(xr1.y) - mm.y;
  }
  const int redoff = ((g4 & 1) * 64 + c8 * 8 + (g4 >> 1) * 4) * 4;

  {
    const f32x4 r = gemv64(wqr, x0, x1);
    if ((lane & 8) == 0) *(LAS f32x4*)(L + L_RED + w * 512 + redoff) = r;
  }
  lds_signal(FL + 0);
#pragma unroll
  for (int i = 0; i < 4; ++i) wur[i] = __builtin_amdgcn_raw_buffer_load_b128(rwu, guoff + i * FF * 2, 0, AUX_NT);
  SB();
  {
    const f32x4 r = gemv64(wgr, x0, x1);
    if ((lane & 8) == 0) *(LAS f32x4*)(L + L_RED + 4096 + w * 512 + redoff) = r;
  }
#pragma unroll
  for (int i = 0; i < 4; ++i) wdr[i] = __builtin_amdgcn_raw_buffer_load_b128(rwd, tile + i * 4 * D, fi * 64 * D * 2, AUX_NT);
  SB();
  {
    const f32x4 r = gemv64(wur, x0, x1);
    if ((lane & 8) == 0) *(LAS f32x4*)(L + L_RED + 8192 + w * 512 + redoff) = r;
  }
  lds_signal(FL + 1);
#pragma unroll
  for (int i = 0; i < 4; ++i) wor[i] = __builtin_amdgcn_raw_buffer_load_b128(rwo, tile + i * 4 * D, rg * 64 * D * 2, AUX_NT);
  SB();
#pragma unroll
  for (int i = 0; i < 4; ++i) *(LAS v4u*)(L + L_WD + (w * 4 + i) * 1024 + lane * 16) = wdr[i];
  SB();
  LDK(ka, UOFF(w));
#pragma unroll
  for (int i = 0; i < 4; ++i) *(LAS v4u*)(L + L_WO + (w * 4 + i) * 1024 + lane * 16) = wor[i];
  SB();
  LDV(va, UOFF(w));
  TOUCH4(ka);
  LDK(kb, UOFF(w + 8));
  TOUCH4(va);
  LDV(vb, UOFF(w + 8));
  TOUCH4(kb);
  LDK(kc, UOFF(w + 16));
  TOUCH4(vb);
  LDV(vc, UOFF(w + 16));
  lds_wait(FL + 3, 1);
  v4u qf[4];
#pragma unroll
  for (int kk = 0; kk < 4; ++kk) qf[kk] = *(LAS v4u*)(L + L_Q + i16 * 272 + kk * 64 + g4 * 16);
  STAMP(3);

  AttnState st;
  st.m = -INFINITY;
  st.l = 0.f;
#pragma unroll
  for (int e = 0; e < 8; ++e) st.o[e] = f32x4{0.f, 0.f, 0.f, 0.f};
  attn_unit(st, ka, va, qf);
  attn_unit(st, kb, vb, qf);
#define PA_K kc
#define PA_V vc
#define PB_K kb
#define PB_V vb
  int t = w + 24;
  for (;;) {
    const int o0 = t < nunit ? UOFFX(t) : OOB;
    const int ak0 = klane + o0, av0 = vlane + o0;
    LDK2(PB_K, ak0);
    LDV2(PB_V, av0);
    asm volatile("" ::"v"(ak0), "v"(av0));
    attn_unit(st, PA_K, PA_V, qf);
    if (t >= nunit) break;
    const int o1 = t + 8 < nunit ? UOFFX(t + 8) : OOB;
    const int ak1 = klane + o1, av1 = vlane + o1;
    LDK2(PA_K, ak1);
    LDV2(PA_V, av1);
    asm volatile("" ::"v"(ak1), "v"(av1));
    attn_unit(st, PB_K, PB_V, qf);
    if (t + 8 >= nunit) break;
    t += 16;
  }
  lds_signal(FL + 2);
  STAMP(5);
  const int sfoff = ((tid >> 2) & 1) * 1024 + ((tid & 127) >> 3) * 64 + (tid & 3) * 16;
  const int oidx = ((tid >> 4) & 1) * D + cg * 256 + (rg & 15) * 16 + (tid & 15);
  unsigned xo = 0;
  if (w == 0) xo = *(const GAS unsigned short*)(p.x + oidx);

  {
    const float l = x32_sum(x16_sum(st.l));
    if (i16 < 8) {
#pragma unroll
      for (int e = 0; e < 8; ++e) *(LAS f32x4*)(L + L_O + (w * 256 + (i16 * 4 + g4) * 8 + (e ^ i16)) * 16) = st.o[e];
      if (g4 == 0) *(LAS v2u*)(L + L_ML + (i16 * 8 + w) * 8) = v2u{f2u(st.m), f2u(l)};
    }
  }
  int poff = g4 * RSP + WS_P1 + (hkv * 8 + w) * 4096 + (rg & 15) * 256 + i16 * 16;
  int goff = (lane >> 3) * RSG + WS_GU + rg * 1024 + ((lane >> 2) & 1) * 512 + (w * 8 + (lane & 3) * 2) * 8;
  int roff = ((tid >> 3) & 7) * RSS + (cg * 8 + (tid >> 6)) * 2048 + (rg & 15) * 128 + (tid & 7) * 16;
  int p1st = p1own + tid * 16, s2st = (w < 2 ? WS_S2 : WS_F2) + s2own + (tid & 127) * 16;
  __builtin_amdgcn_s_waitcnt(0x0F70);
  asm volatile("" : "+v"(poff), "+v"(goff), "+v"(roff), "+v"(p1st), "+v"(s2st));
  WG_BARRIER();
  if (w < 4) {
    const int j = tid >> 5;
    v4u ml[4];
    f32x4 ov[8];
#pragma unroll
    for (int k = 0; k < 4; ++k) ml[k] = *(LAS v4u*)(L + L_ML + j * 64 + k * 16);
#pragma unroll
    for (int ww = 0; ww < 8; ++ww) ov[ww] = *(LAS f32x4*)(L + L_O + (ww * 256 + (tid ^ j)) * 16);
    SB();
    float M = fmaxf(u2f(ml[0].x), u2f(ml[0].z));
#pragma unroll
    for (int k = 1; k < 4; ++k) M = fmaxf(M, fmaxf(u2f(ml[k].x), u2f(ml[k].z)));
    float Ls = 0.f;
    f32x4 acc = {0.f, 0.f, 0.f, 0.f};
#pragma unroll
    for (int ww = 0; ww < 8; ++ww) {
      const float wt = __builtin_amdgcn_exp2f(u2f(ml[ww >> 1][(ww & 1) * 2]) - M);
      Ls = fmaf(wt, u2f(ml[ww >> 1][(ww & 1) * 2 + 1]), Ls);
      acc += ov[ww] * wt;
    }
    acc *= rcp(Ls);
    const unsigned lse = f2u(M + __builtin_amdgcn_logf(Ls));
    v4u pk;
#pragma unroll
    for (int r = 0; r < 4; ++r) {
      const unsigned t = ((f2u(acc[r]) + 0x80u) & 0xFFFFFF00u) | ((lse >> (8 * r)) & 0xFFu);
      pk[r] = t == SENT ? 0x7FC000FFu : t;
    }
    __builtin_amdgcn_raw_buffer_store_b128(pk, rs, p1st, 0, AUX_SC1);
  }
  STAMP(6);
  SB();

  f32x4 wof[8];
  {
    v4u gu = __builtin_amdgcn_raw_buffer_load_b128(rs, goff, 0, AUX_SC1);
    v4u pa = __builtin_amdgcn_raw_buffer_load_b128(rs, poff, 0, AUX_SC1);
    v4u pb = __builtin_amdgcn_raw_buffer_load_b128(rs, poff, 4 * RSP, AUX_SC1);
    SB();
    if (has_ffn) {
      const unsigned e_lo = FL[6], e_hi = FL[7];
      const unsigned tag_hi = (e_lo >> 8) | (e_hi << 24), tag_lo = e_lo & 0xFFu;
      v2u wt[8];
#pragma unroll
      for (int k = 0; k < 8; ++k) wt[k] = *(LAS v2u*)(L + L_WD + (w * 8 + k) * 512 + lane * 8);
      if (!__all(tag2(gu, tag_lo, tag_hi))) {
#pragma nounroll
        for (unsigned spins = 0; spins < SPIN_CAP; ++spins) {
          asm volatile("" ::: "memory");
          __builtin_amdgcn_s_sleep(1);
          gu = __builtin_amdgcn_raw_buffer_load_b128(rs, goff, 0, AUX_SC1);
          if (__all(tag2(gu, tag_lo, tag_hi))) break;
        }
      }
      const float ga = x32_sum(x16_sum(u2f(gu.x << 8))), gb = x32_sum(x16_sum(u2f(gu.z << 8)));
      const float hx = silu(ga) * dpp<XOR8>(ga), hy = silu(gb) * dpp<XOR8>(gb);
      float h0[8], h1[8];
#pragma unroll
      for (int pr = 0; pr < 4; ++pr) {
        h0[2 * pr] = rdl(hx, pr); h0[2 * pr + 1] = rdl(hy, pr);
        h1[2 * pr] = rdl(hx, 4 + pr); h1[2 * pr + 1] = rdl(hy, 4 + pr);
      }
      f32x4 s0, s1;
      gemv256(wt, h0, h1, s0, s1);
      *(LAS f32x4*)(L + L_F + w * 2048 + lane * 16) = s0;
      *(LAS f32x4*)(L + L_F + w * 2048 + 1024 + lane * 16) = s1;
    }
#pragma unroll
    for (int k = 0; k < 8; ++k) {
      const v2u t = *(LAS v2u*)(L + L_WO + (w * 8 + k) * 512 + lane * 8);
      wof[k] = f32x4{bflo(t.x), bfhi(t.x), bflo(t.y), bfhi(t.y)};
      asm volatile("" : "+v"(wof[k]));
    }
    bool ha = full4(pa), hb = full4(pb);
#pragma nounroll
    for (unsigned spins = 0; spins < SPIN_CAP; ++spins) {
      asm volatile("" ::: "memory");
      if (__all(ha & hb)) break;
      if (!ha) pa = __builtin_amdgcn_raw_buffer_load_b128(rs, poff, 0, AUX_SC1);
      if (!hb) pb = __builtin_amdgcn_raw_buffer_load_b128(rs, poff, 4 * RSP, AUX_SC1);
      ha = full4(pa);
      hb = full4(pb);
    }
    const float la = u2f(__builtin_amdgcn_perm(__builtin_amdgcn_perm(pa.w, pa.z, 0x0c0c0400u), __builtin_amdgcn_perm(pa.y, pa.x, 0x0c0c0400u), 0x05040100u));
    const float lb = u2f(__builtin_amdgcn_perm(__builtin_amdgcn_perm(pb.w, pb.z, 0x0c0c0400u), __builtin_amdgcn_perm(pb.y, pb.x, 0x0c0c0400u), 0x05040100u));
    const float Mw = x32_max(x16_max(fmaxf(la, lb)));
    const float wa = __builtin_amdgcn_exp2f(la - Mw), wb = __builtin_amdgcn_exp2f(lb - Mw);
    const float Ww = x32_sum(x16_sum(wa + wb));
    float a[4];
#pragma unroll
    for (int r = 0; r < 4; ++r) a[r] = fmaf(u2f(pa[r] & 0xFFFFFF00u), wa, u2f(pb[r] & 0xFFFFFF00u) * wb);
    const float v = tr32(tr16(a[0], a[1]), tr16(a[2], a[3]));
    *(LAS float*)(L + L_C + (((w >> 2) * 64 + (i16 >> 3) * 32 + g4 * 8 + (i16 & 7)) * 4 + (w & 3)) * 4) = v;
    if (lane == 0) *(LAS v2u*)(L + L_C + 2048 + w * 8) = v2u{f2u(Mw), f2u(Ww)};
  }
  STAMP(7);
  WG_BARRIER();
  {
    const int b = (lane >> 3) & 1, k = lane & 7;
    const v4u m0 = *(LAS v4u*)(L + L_C + 2048 + b * 32), m1 = *(LAS v4u*)(L + L_C + 2048 + b * 32 + 16);
    const f32x4 av = *(LAS f32x4*)(L + L_C + (b * 64 + w * 8 + k) * 16);
    const float M = fmaxf(fmaxf(u2f(m0.x), u2f(m0.z)), fmaxf(u2f(m1.x), u2f(m1.z)));
    const float f0 = __builtin_amdgcn_exp2f(u2f(m0.x) - M), f1 = __builtin_amdgcn_exp2f(u2f(m0.z) - M);
    const float f2 = __builtin_amdgcn_exp2f(u2f(m1.x) - M), f3 = __builtin_amdgcn_exp2f(u2f(m1.z) - M);
    const float num = fmaf(f0, av.x, f1 * av.y) + fmaf(f2, av.z, f3 * av.w);
    const float den = fmaf(f0, u2f(m0.y), f1 * u2f(m0.w)) + fmaf(f2, u2f(m1.y), f3 * u2f(m1.w));
    const float ov = num * rcp(den);
    f32x4 s0 = {0.f, 0.f, 0.f, 0.f}, s1 = {0.f, 0.f, 0.f, 0.f};
#pragma unroll
    for (int kk = 0; kk < 8; ++kk) { s0 += wof[kk] * rdl(ov, kk); s1 += wof[kk] * rdl(ov, 8 + kk); }
    *(LAS f32x4*)(L + L_S + w * 2048 + lane * 16) = s0;
    *(LAS f32x4*)(L + L_S + w * 2048 + 1024 + lane * 16) = s1;
  }
  WG_BARRIER();
  if (w < 2 || (w < 4 && has_ffn)) {
    const int lb = (w < 2 ? L_S : L_F) + sfoff;
    f32x4 t[8];
#pragma unroll
    for (int ww = 0; ww < 8; ++ww) t[ww] = *(LAS f32x4*)(L + lb + ww * 2048);
    SB();
    const f32x4 s4 = ((t[0] + t[1]) + (t[2] + t[3])) + ((t[4] + t[5]) + (t[6] + t[7]));
    __builtin_amdgcn_raw_buffer_store_b128(clean4(s4), rs, s2st, 0, AUX_SC1);
  }
  STAMP(8);

  if (reducer) {
    v4u a = __builtin_amdgcn_raw_buffer_load_b128(rs, roff, WS_S2, AUX_SC1);
    v4u c = {0u, 0u, 0u, 0u};
    if (((tid >> 3) & 7) != w) c = __builtin_amdgcn_raw_buffer_load_b128(rs, roff, WS_F2, AUX_SC1);
    bool ha = full4(a), hc = full4(c);
#pragma nounroll
    for (unsigned spins = 0; spins < SPIN_CAP; ++spins) {
      asm volatile("" ::: "memory");
      if (__all(ha & hc)) break;
      if (!ha) a = __builtin_amdgcn_raw_buffer_load_b128(rs, roff, WS_S2, AUX_SC1);
      if (!hc) c = __builtin_amdgcn_raw_buffer_load_b128(rs, roff, WS_F2, AUX_SC1);
      ha = full4(a);
      hc = full4(c);
    }
    float vs = tr32(tr16(u2f(a.x), u2f(a.y)), tr16(u2f(a.z), u2f(a.w)));
    float vf = tr32(tr16(u2f(c.x), u2f(c.y)), tr16(u2f(c.z), u2f(c.w)));
    vs += dpp<XOR8>(vs);
    vf += dpp<XOR8>(vf);
    *(LAS float*)(L + L_R + ((lane >> 3) & 1) * 1024 + ((lane & 7) * 4 + (lane >> 4)) * 32 + w * 4) = (lane & 8) ? vs : vf;
    WG_BARRIER();
    if (tid < 32) {
      const f32x4 f0 = *(LAS f32x4*)(L + L_R + tid * 32), f1 = *(LAS f32x4*)(L + L_R + tid * 32 + 16);
      const f32x4 s0 = *(LAS f32x4*)(L + L_R + 1024 + tid * 32), s1 = *(LAS f32x4*)(L + L_R + 1024 + tid * 32 + 16);
      const f32x4 f4 = f0 + f1, s4 = s0 + s1;
      const float f = (f4.x + f4.y) + (f4.z + f4.w), sv = (s4.x + s4.y) + (s4.z + s4.w);
      const float r = fmaf(sv, silu(f), bflo(xo));
      p.out[oidx] = (unsigned short)(cvtpk(r, 0.f) & 0xFFFFu);
    }
    if (bid == NWG - 1 && tid == 0) {
      const v2u ne = {FL[6], FL[7]};
      __builtin_amdgcn_raw_buffer_store_b64(ne, rs, WS_E, 0, AUX_SC1);
    }
  }
  STAMP(9);
}

}

extern "C" void kernel_launch(void* const* d_in, const int* in_sizes, int n_in, void* d_out, int out_size, void* d_ws,
                              size_t ws_size, hipStream_t stream) {
  Params p;
  p.x = (const unsigned short*)d_in[0];
  p.kc = (const unsigned short*)d_in[1];
  p.vc = (const unsigned short*)d_in[2];
  p.wq = (const unsigned short*)d_in[3];
  p.wo = (const unsigned short*)d_in[4];
  p.wg = (const unsigned short*)d_in[6];
  p.wu = (const unsigned short*)d_in[7];
  p.wd = (const unsigned short*)d_in[8];
  p.out = (unsigned short*)d_out;
  p.ws = (unsigned char*)d_ws;
  hipLaunchKernelGGL(fused_decode, dim3(NWG), dim3(NTHR), 0, stream, p);
}
```

```cpp
#include <hip/hip_runtime.h>
#include <hip/hip_bf16.h>
#include <stdint.h>

#define GAS __attribute__((address_space(1)))
#define LAS __attribute__((address_space(3)))
typedef unsigned v4u __attribute__((ext_vector_type(4)));
typedef unsigned v2u __attribute__((ext_vector_type(2)));
typedef float f32x4 __attribute__((ext_vector_type(4)));
typedef short bf16x8 __attribute__((ext_vector_type(8)));
typedef short bf16x4 __attribute__((ext_vector_type(4)));
typedef unsigned long long u64;

namespace {

constexpr int NWG = 256, NTHR = 576;
constexpr int D = 1024, HD = 4096, FF = 3584, SK = 32768;
constexpr int AUX_SC1 = 16;
constexpr unsigned SENT = 0xFFFFFFFFu;
constexpr unsigned SPIN_CAP = 1u << 22;

constexpr int RSG = 65536, RSP = 131072, RSS = 65536;
constexpr int WS_E = 0;
constexpr int WS_QP = 4096;
constexpr int WS_GU = WS_QP + 262144;
constexpr int WS_P1 = WS_GU + 8 * RSG;
constexpr int WS_S2 = WS_P1 + 8 * RSP;
constexpr int WS_F2 = WS_S2 + 8 * RSS;
constexpr int WS_ALL = WS_F2 + 8 * RSS;

constexpr int L_RED = 0;
constexpr int L_Q = 12288;
constexpr int L_ML = L_Q + 16 * 272;
constexpr int L_O = L_ML + 512;
constexpr int L_S = L_O + 32768;
constexpr int L_C = L_S + 16384;
constexpr int L_R = L_C + 2048 + 64;
constexpr int L_FL = L_R + 2048 + 512;
constexpr int L_WO = L_FL + 64;
constexpr int L_WD = L_WO + 32768;
constexpr int L_F = L_WD + 32768;
constexpr int L_USED = L_F + 16384;
constexpr int L_BYTES = 152 * 1024;
static_assert(L_USED <= L_BYTES, "lds");

struct Params {
  const unsigned short* x;
  const unsigned short* wq;
  unsigned char* ws;
  const unsigned short* wg;
  const unsigned short* wu;
  const unsigned short* wd;
  const unsigned short* wo;
  const unsigned short* kc;
  const unsigned short* vc;
  unsigned short* out;
};

__device__ __forceinline__ float bflo(unsigned u) { return __builtin_bit_cast(float, u << 16); }
__device__ __forceinline__ float bfhi(unsigned u) { return __builtin_bit_cast(float, u & 0xFFFF0000u); }
__device__ __forceinline__ unsigned f2u(float f) { return __builtin_bit_cast(unsigned, f); }
__device__ __forceinline__ float u2f(unsigned u) { return __builtin_bit_cast(float, u); }
typedef __bf16 bf16p __attribute__((ext_vector_type(2)));
typedef float f32x2 __attribute__((ext_vector_type(2)));
__device__ __forceinline__ unsigned cvtpk(float lo, float hi) {
  return __builtin_bit_cast(unsigned, __builtin_convertvector(f32x2{lo, hi}, bf16p));
}
template <int CTRL> __device__ __forceinline__ float dpp(float x) {
  return __builtin_bit_cast(float, __builtin_amdgcn_mov_dpp(__builtin_bit_cast(int, x), CTRL, 0xf, 0xf, true));
}
constexpr int XOR1 = 0xB1, XOR2 = 0x4E, XOR7 = 0x141, XOR8 = 0x128;
__device__ __forceinline__ float x16_sum(float x) {
  auto s = __builtin_amdgcn_permlane16_swap(f2u(x), f2u(x), false, false);
  return u2f(s[0]) + u2f(s[1]);
}
__device__ __forceinline__ float x32_sum(float x) {
  auto s = __builtin_amdgcn_permlane32_swap(f2u(x), f2u(x), false, false);
  return u2f(s[0]) + u2f(s[1]);
}
__device__ __forceinline__ float x16_max(float x) {
  auto s = __builtin_amdgcn_permlane16_swap(f2u(x), f2u(x), false, false);
  return fmaxf(u2f(s[0]), u2f(s[1]));
}
__device__ __forceinline__ float x32_max(float x) {
  auto s = __builtin_amdgcn_permlane32_swap(f2u(x), f2u(x), false, false);
  return fmaxf(u2f(s[0]), u2f(s[1]));
}
__device__ __forceinline__ float row16_sum(float x) {
  x += dpp<XOR1>(x);
  x += dpp<XOR2>(x);
  x += dpp<XOR7>(x);
  x += dpp<XOR8>(x);
  return x;
}
__device__ __forceinline__ float wave_sum(float x) { return x32_sum(x16_sum(row16_sum(x))); }
__device__ __forceinline__ float tr16(float a, float b) {
  auto s = __builtin_amdgcn_permlane16_swap(f2u(a), f2u(b), false, false);
  return u2f(s[0]) + u2f(s[1]);
}
__device__ __forceinline__ float tr32(float a, float b) {
  auto s = __builtin_amdgcn_permlane32_swap(f2u(a), f2u(b), false, false);
  return u2f(s[0]) + u2f(s[1]);
}
__device__ __forceinline__ v4u ldnt(const GAS v4u* p) { return __builtin_nontemporal_load(p); }
__device__ __forceinline__ unsigned clean(float f) {
  const unsigned u = f2u(f);
  return u == SENT ? 0x7FC00000u : u;
}
__device__ __forceinline__ v4u clean4(f32x4 v) { return v4u{clean(v.x), clean(v.y), clean(v.z), clean(v.w)}; }
__device__ __forceinline__ bool full4(v4u v) { return v.x != SENT && v.y != SENT && v.z != SENT && v.w != SENT; }
__device__ __forceinline__ float rdl(float v, int lane) { return u2f(__builtin_amdgcn_readlane(f2u(v), lane)); }
__device__ __forceinline__ float rcp(float v) { return __builtin_amdgcn_rcpf(v); }
__device__ __forceinline__ float silu(float v) { return v * rcp(1.0f + __builtin_amdgcn_exp2f(v * -1.4426950408889634f)); }

#define SB() __builtin_amdgcn_sched_barrier(0)
#define WG_BARRIER()                                        \
  do {                                                      \
    asm volatile("s_waitcnt lgkmcnt(0)" ::: "memory");      \
    __builtin_amdgcn_s_barrier();                           \
    asm volatile("" ::: "memory");                          \
  } while (0)

__device__ __forceinline__ f32x4 gemv64(const v4u (&w)[4], const float (&x0)[4], const float (&x1)[4]) {
  float a0[8], a1[8];
#pragma unroll
  for (int c = 0; c < 8; ++c) { a0[c] = 0.f; a1[c] = 0.f; }
#pragma unroll
  for (int i = 0; i < 4; ++i) {
#pragma unroll
    for (int k = 0; k < 4; ++k) {
      const float lo = bflo(w[i][k]), hi = bfhi(w[i][k]);
      a0[2 * k] = fmaf(lo, x0[i], a0[2 * k]);
      a0[2 * k + 1] = fmaf(hi, x0[i], a0[2 * k + 1]);
      a1[2 * k] = fmaf(lo, x1[i], a1[2 * k]);
      a1[2 * k + 1] = fmaf(hi, x1[i], a1[2 * k + 1]);
    }
  }
  float t[8];
#pragma unroll
  for (int c = 0; c < 8; ++c) t[c] = tr16(a0[c], a1[c]);
  f32x4 r;
#pragma unroll
  for (int c = 0; c < 4; ++c) {
    float v = tr32(t[c], t[c + 4]);
    v += dpp<XOR8>(v);
    r[c] = v;
  }
  return r;
}

__device__ __forceinline__ void gemv256(const v2u (&w)[8], const float (&c0)[8], const float (&c1)[8], f32x4& s0, f32x4& s1) {
  s0 = f32x4{0.f, 0.f, 0.f, 0.f};
  s1 = f32x4{0.f, 0.f, 0.f, 0.f};
#pragma unroll
  for (int k = 0; k < 8; ++k) {
    const f32x4 wf = {bflo(w[k].x), bfhi(w[k].x), bflo(w[k].y), bfhi(w[k].y)};
#pragma unroll
    for (int c = 0; c < 4; ++c) {
      s0[c] = fmaf(wf[c], c0[k], s0[c]);
      s1[c] = fmaf(wf[c], c1[k], s1[c]);
    }
  }
}

__device__ __forceinline__ v2u granule(float v, unsigned tag_lo, unsigned tag_hi) {
  return v2u{(tag_lo << 24) | ((f2u(v) + 0x80u) >> 8), tag_hi};
}
__device__ __forceinline__ bool tag2(v4u g, unsigned tag_lo, unsigned tag_hi) {
  return (g.y == tag_hi) & ((g.x >> 24) == tag_lo) & (g.w == tag_hi) & ((g.z >> 24) == tag_lo);
}
#define TOUCH4(a) asm volatile("" : "+v"(a[0]), "+v"(a[1]), "+v"(a[2]), "+v"(a[3]))
constexpr int AUX_NT = 2;
#define LDK(kb_, off_) do { _Pragma("unroll") for (int j = 0; j < 4; ++j) kb_[j] = __builtin_amdgcn_raw_buffer_load_b128(rk, (klane + (off_)) + j * 64, 0, 0); SB(); } while (0)
#define LDV(vb_, off_) do { _Pragma("unroll") for (int j = 0; j < 4; ++j) vb_[j] = __builtin_amdgcn_raw_buffer_load_b128(rv, (vlane + (off_)) + j * 1024, 0, AUX_NT); SB(); } while (0)
#define LDK2(kb_, a_) do { _Pragma("unroll") for (int j = 0; j < 4; ++j) kb_[j] = __builtin_amdgcn_raw_buffer_load_b128(rk, (a_) + j * 64, 0, 0); SB(); } while (0)
#define LDV2(vb_, a_) do { _Pragma("unroll") for (int j = 0; j < 4; ++j) vb_[j] = __builtin_amdgcn_raw_buffer_load_b128(rv, (a_) + j * 1024, 0, AUX_NT); SB(); } while (0)

struct AttnState {
  float m, l;
  f32x4 o[8];
};
__device__ __forceinline__ v2u attn_qk(AttnState& st, const v4u (&kc)[4], const v4u (&qf)[4]) {
  constexpr float C2 = 0.088388347648318440f * 1.4426950408889634f;
  f32x4 s = {0.f, 0.f, 0.f, 0.f};
#pragma unroll
  for (int kk = 0; kk < 4; ++kk)
    s = __builtin_amdgcn_mfma_f32_16x16x32_bf16(__builtin_bit_cast(bf16x8, kc[kk]), __builtin_bit_cast(bf16x8, qf[kk]), s, 0, 0, 0);
#pragma unroll
  for (int r = 0; r < 4; ++r) s[r] = (s[r] + dpp<XOR8>(s[r])) * C2;
  float mx = fmaxf(fmaxf(s[0], s[1]), fmaxf(s[2], s[3]));
  mx = x32_max(x16_max(mx));
  const float mn = fmaxf(st.m, mx);
  const float alpha = __builtin_amdgcn_exp2f(st.m - mn);
  st.m = mn;
  const float p0 = __builtin_amdgcn_exp2f(s[0] - mn), p1 = __builtin_amdgcn_exp2f(s[1] - mn);
  const float p2 = __builtin_amdgcn_exp2f(s[2] - mn), p3 = __builtin_amdgcn_exp2f(s[3] - mn);
  st.l = fmaf(st.l, alpha, (p0 + p1) + (p2 + p3));
  const v2u pb = {cvtpk(p0, p1), cvtpk(p2, p3)};
#pragma unroll
  for (int e = 0; e < 8; ++e) st.o[e] *= alpha;
  SB();
  return pb;
}
__device__ __forceinline__ void attn_pv(AttnState& st, const v4u (&vc)[4], v2u pb) {
#pragma unroll
  for (int e = 0; e < 8; ++e) {
    const unsigned sel = (e & 1) ? 0x07060302u : 0x05040100u;
    const v2u a = {__builtin_amdgcn_perm(vc[1][e >> 1], vc[0][e >> 1], sel), __builtin_amdgcn_perm(vc[3][e >> 1], vc[2][e >> 1], sel)};
    st.o[e] = __builtin_amdgcn_mfma_f32_16x16x16bf16_1k(__builtin_bit_cast(bf16x4, a), __builtin_bit_cast(bf16x4, pb), st.o[e], 0, 0, 0);
  }
  SB();
}
__device__ __forceinline__ void attn_unit(AttnState& st, const v4u (&kc)[4], const v4u (&vc)[4], const v4u (&qf)[4]) {
  attn_pv(st, vc, attn_qk(st, kc, qf));
}

#define DRAIN() asm volatile("s_waitcnt vmcnt(0)" ::: "memory")
constexpr int MV = 3;
typedef float f32x2v __attribute__((ext_vector_type(2)));
__device__ __forceinline__ void lds_signal(LAS unsigned* c) {
  asm volatile("s_waitcnt lgkmcnt(0)" ::: "memory");
  if ((threadIdx.x & 63) == 0) __hip_atomic_fetch_add(c, 1u, __ATOMIC_RELAXED, __HIP_MEMORY_SCOPE_WORKGROUP);
}
__device__ __forceinline__ void lds_wait(LAS unsigned* c, unsigned want) {
#pragma nounroll
  for (unsigned spins = 0; spins < (SPIN_CAP << 3); ++spins) {
    asm volatile("" ::: "memory");
    if (__builtin_amdgcn_readfirstlane(*(volatile LAS unsigned*)c) >= want) break;
    __builtin_amdgcn_s_sleep(1);
  }
  asm volatile("" ::: "memory");
}

__global__ void __launch_bounds__(NTHR) fused_decode(Params p) {
  __shared__ __attribute__((aligned(16))) unsigned char smem[L_BYTES];
  LAS unsigned char* L = (LAS unsigned char*)smem;
  LAS unsigned* FL = (LAS unsigned*)(L + L_FL);
  const int tid = (int)threadIdx.x - 64, lane = threadIdx.x & 63;
  const int w = __builtin_amdgcn_readfirstlane(threadIdx.x >> 6) - 1;
  const int bid = blockIdx.x;
  const int grp = bid >> 5, xq = bid & 7;
  const int cg = (bid >> 3) & 3, rg = grp * 8 + xq;
  const int hkv = bid >> 6, bb = grp & 1;
  const int xl = (3 - (int)((size_t)p.wq >> 7)) & 7;
  const bool has_ffn = xq != xl;
  const int fi = has_ffn ? grp * 7 + (xq < xl ? xq : xq - 1) : 0;
  const bool reducer = rg >= 48;
  constexpr int OOB = 0x7FFF0000;
  const __amdgpu_buffer_rsrc_t rs = __builtin_amdgcn_make_buffer_rsrc((void*)p.ws, 0, WS_ALL, 0x00020000);
  const int p1own = (bid & 7) * RSP + WS_P1 + (bid >> 3) * 4096;
  const int s2own = (rg & 7) * RSS + (cg * 8 + (rg >> 3)) * 2048;
  const GAS v4u* xg = (const GAS v4u*)p.x;

  if (w < 0) {
    __builtin_amdgcn_s_setprio(3);
    const v4u xa0 = xg[lane * 2], xa1 = xg[lane * 2 + 1], xb0 = xg[128 + lane * 2], xb1 = xg[128 + lane * 2 + 1];
    const v2u ev = __builtin_amdgcn_raw_buffer_load_b64(rs, WS_E, 0, AUX_SC1);
    if (lane < 6) FL[lane] = 0u;
    float rr;
    {
      float va_[16], vb_[16];
#pragma unroll
      for (int k = 0; k < 4; ++k) {
        va_[2 * k] = bflo(xa0[k]); va_[2 * k + 1] = bfhi(xa0[k]); va_[8 + 2 * k] = bflo(xa1[k]); va_[8 + 2 * k + 1] = bfhi(xa1[k]);
        vb_[2 * k] = bflo(xb0[k]); vb_[2 * k + 1] = bfhi(xb0[k]); vb_[8 + 2 * k] = bflo(xb1[k]); vb_[8 + 2 * k + 1] = bfhi(xb1[k]);
      }
      float sa = 0.f, sb = 0.f;
#pragma unroll
      for (int k = 0; k < 16; ++k) { sa += va_[k]; sb += vb_[k]; }
      const float ma = wave_sum(sa) * (1.0f / D), mb = wave_sum(sb) * (1.0f / D);
      if (lane == 0) *(LAS f32x2v*)(L + L_FL + 32) = f32x2v{ma, mb};
      WG_BARRIER();
      float qa = 0.f, qb = 0.f;
#pragma unroll
      for (int k = 0; k < 16; ++k) { const float da = va_[k] - ma, db = vb_[k] - mb; qa = fmaf(da, da, qa); qb = fmaf(db, db, qb); }
      const float ta = wave_sum(qa), tb = wave_sum(qb);
      rr = 1.0f / sqrtf((lane < 32 ? ta : tb) * (1.0f / D) + 1e-5f);
    }
    const u64 e1 = ((((u64)__builtin_amdgcn_readfirstlane(ev.y)) << 32) | (u64)(unsigned)__builtin_amdgcn_readfirstlane(ev.x)) + 1ull;
    const unsigned tag_hi = (unsigned)(e1 >> 8), tag_lo = (unsigned)(e1 & 0xFFull);
    const unsigned tq = (unsigned)e1 & 0xFFFFFFu;
    if (lane == 0) { FL[6] = (unsigned)e1; FL[7] = (unsigned)(e1 >> 32); }
    lds_wait(FL + 0, 8);
    {
      f32x2v a = {0.f, 0.f};
#pragma unroll
      for (int ww = 0; ww < 8; ++ww) a += *(LAS f32x2v*)(L + L_RED + ww * 512 + lane * 8);
      const unsigned v0 = (f2u(a.x * rr) + 0x800u) >> 12, v1 = (f2u(a.y * rr) + 0x800u) >> 12;
      __builtin_amdgcn_raw_buffer_store_b64(v2u{v0 | (v1 << 20), (v1 >> 12) | (tq << 8)}, rs,
          WS_QP + ((hkv * 2 + (lane >> 5)) * 4 + cg) * 4096 + (rg & 15) * 256 + (lane & 31) * 8, 0, AUX_SC1);
    }
    DRAIN();
    {
      const int qoff = WS_QP + (hkv * 2 + bb) * 16384 + lane * 16;
      v4u qg[16];
#pragma nounroll
      for (unsigned spins = 0; spins < SPIN_CAP; ++spins) {
        asm volatile("" ::: "memory");
        bool ok = true;
#pragma unroll
        for (int j = 0; j < 16; ++j) qg[j] = __builtin_amdgcn_raw_buffer_load_b128(rs, qoff + (j & 3) * 1024, (j >> 2) * 4096, AUX_SC1);
#pragma unroll
        for (int j = 0; j < 16; ++j) ok &= ((qg[j].y >> 8) == tq) & ((qg[j].w >> 8) == tq);
        if (__all(ok)) break;
        __builtin_amdgcn_s_sleep(4);
      }
#pragma unroll
      for (int j = 0; j < 4; ++j) {
        f32x4 q = {0.f, 0.f, 0.f, 0.f};
#pragma unroll
        for (int ks = 0; ks < 4; ++ks) {
          const v4u g = qg[ks * 4 + j];
          q += f32x4{u2f(g.x << 12), u2f((g.y << 24) | ((g.x >> 20) << 12)), u2f(g.z << 12), u2f((g.w << 24) | ((g.z >> 20) << 12))};
        }
        const v2u hi = {cvtpk(q.x, q.y), cvtpk(q.z, q.w)};
        const v2u lo = {cvtpk(q.x - bflo(hi.x), q.y - bfhi(hi.x)), cvtpk(q.z - bflo(hi.y), q.w - bfhi(hi.y))};
        *(LAS v2u*)(L + L_Q + (2 * j + (lane >> 5)) * 272 + (lane & 31) * 8) = hi;
        *(LAS v2u*)(L + L_Q + (2 * j + 8 + (lane >> 5)) * 272 + (lane & 31) * 8) = lo;
      }
      lds_signal(FL + 3);
    }
    const int coff = WS_QP + (((lane >> 4) * 2) * 4 + cg) * 4096 + (lane & 15) * 256;
    if (reducer) {
#pragma nounroll
      for (unsigned spins = 0; spins < SPIN_CAP; ++spins) {
        asm volatile("" ::: "memory");
        const v4u ck = __builtin_amdgcn_raw_buffer_load_b128(rs, coff, 0, AUX_SC1);
        if (__all(((ck.y >> 8) == tq) & ((ck.w >> 8) == tq))) break;
        __builtin_amdgcn_s_sleep(4);
      }
    }
    if (has_ffn) {
      lds_wait(FL + 1, 8);
      v4u gr[2];
#pragma unroll
      for (int m = 0; m < 2; ++m) {
        f32x2v a = {0.f, 0.f};
#pragma unroll
        for (int ww = 0; ww < 8; ++ww) a += *(LAS f32x2v*)(L + L_RED + 4096 + m * 4096 + ww * 512 + lane * 8);
        const v2u g0 = granule(a.x * rr, tag_lo, tag_hi), g1 = granule(a.y * rr, tag_lo, tag_hi);
        gr[m] = v4u{g0.x, g0.y, g1.x, g1.y};
      }
      lds_wait(FL + 2, 4);
#pragma unroll
      for (int m = 0; m < 2; ++m) __builtin_amdgcn_raw_buffer_store_b128(gr[m], rs, (cg * 2 + m) * RSG + WS_GU + rg * 1024 + lane * 16, 0, AUX_SC1);
    }
    WG_BARRIER();
    return;
  }

  {
    const v4u s4 = {SENT, SENT, SENT, SENT};
    const int off = w < 4 ? p1own + w * 1024 : (w < 6 ? WS_S2 - 4096 : WS_F2 - 6144) + s2own + w * 1024;
    __builtin_amdgcn_raw_buffer_store_b128(s4, rs, off + lane * 16, 0, AUX_SC1);
    SB();
  }
  const int r8 = lane >> 3, c8 = lane & 7;
  const int krow = cg * 256 + w * 32 + r8 * 4;
  const v2u xr0 = *(const GAS v2u*)(p.x + krow), xr1 = *(const GAS v2u*)(p.x + D + krow);
  v4u wqr[4], wgr[4], wur[4], wdr[4], wor[4];
#pragma unroll
  for (int i = 0; i < 4; ++i) wqr[i] = ldnt((const GAS v4u*)(p.wq + (size_t)(krow + i) * HD + rg * 64 + c8 * 8));
  SB();
  const int fbytes = has_ffn ? D * FF * 2 : 0;
  const __amdgpu_buffer_rsrc_t rwg = __builtin_amdgcn_make_buffer_rsrc((void*)p.wg, 0, fbytes, 0x00020000);
  const __amdgpu_buffer_rsrc_t rwu = __builtin_amdgcn_make_buffer_rsrc((void*)p.wu, 0, fbytes, 0x00020000);
  const __amdgpu_buffer_rsrc_t rwd = __builtin_amdgcn_make_buffer_rsrc((void*)p.wd, 0, fbytes, 0x00020000);
  const __amdgpu_buffer_rsrc_t rwo = __builtin_amdgcn_make_buffer_rsrc((void*)p.wo, 0, HD * D * 2, 0x00020000);
  const int guoff = (krow * FF + fi * 64 + c8 * 8) * 2;
  const int tile = ((w * 8 + (lane >> 5)) * D + cg * 256 + (lane & 31) * 8) * 2;
#define LDWG() do { _Pragma("unroll") for (int i = 0; i < 4; ++i) wgr[i] = __builtin_amdgcn_raw_buffer_load_b128(rwg, guoff + i * FF * 2, 0, AUX_NT); SB(); } while (0)
#define LDWU() do { _Pragma("unroll") for (int i = 0; i < 4; ++i) wur[i] = __builtin_amdgcn_raw_buffer_load_b128(rwu, guoff + i * FF * 2, 0, AUX_NT); SB(); } while (0)
#define LDWD() do { _Pragma("unroll") for (int i = 0; i < 4; ++i) wdr[i] = __builtin_amdgcn_raw_buffer_load_b128(rwd, tile + i * 4 * D, fi * 64 * D * 2, AUX_NT); SB(); } while (0)
#define LDWO() do { _Pragma("unroll") for (int i = 0; i < 4; ++i) wor[i] = __builtin_amdgcn_raw_buffer_load_b128(rwo, tile + i * 4 * D, rg * 64 * D * 2, AUX_NT); SB(); } while (0)
#define USEQ() do { const f32x4 r = gemv64(wqr, x0, x1); if ((lane & 8) == 0) *(LAS f32x4*)(L + L_RED + w * 512 + redoff) = r; lds_signal(FL + 0); } while (0)
#define USEG() do { const f32x4 r = gemv64(wgr, x0, x1); if ((lane & 8) == 0) *(LAS f32x4*)(L + L_RED + 4096 + w * 512 + redoff) = r; SB(); } while (0)
#define USEU() do { const f32x4 r = gemv64(wur, x0, x1); if ((lane & 8) == 0) *(LAS f32x4*)(L + L_RED + 8192 + w * 512 + redoff) = r; lds_signal(FL + 1); } while (0)
#define USED() do { _Pragma("unroll") for (int i = 0; i < 4; ++i) *(LAS v4u*)(L + L_WD + (w * 4 + i) * 1024 + lane * 16) = wdr[i]; SB(); } while (0)
#define USEO() do { _Pragma("unroll") for (int i = 0; i < 4; ++i) *(LAS v4u*)(L + L_WO + (w * 4 + i) * 1024 + lane * 16) = wor[i]; SB(); } while (0)
  LDWO();
  const int i16 = lane & 15, g4 = lane >> 4;
  const __amdgpu_buffer_rsrc_t rk = __builtin_amdgcn_make_buffer_rsrc((void*)p.kc, 0, 8 * SK * 256, 0x00020000);
  const __amdgpu_buffer_rsrc_t rv = __builtin_amdgcn_make_buffer_rsrc((void*)p.vc, 0, 8 * SK * 256, 0x00020000);
  const int klane = (4 * (i16 & 3) + (i16 >> 2)) * 256 + g4 * 16, vlane = lane * 16;
  const int rsl = (((3 - (int)((size_t)p.wg >> 7)) & 7) + grp) & 7;
  const int xs = rsl < xl ? rsl : rsl + 1;
  const int nunit = rsl == 7 ? 64 : (!has_ffn ? 64 + MV : (xq == xs ? 64 - MV : 64));
  const int obase = ((bb * 4 + hkv) * 2048 + cg * 512) * 4096;
  const int ubase = obase + xq * 262144, xbase = obase + xs * 262144 - MV * 4096;
#define UOFF(t_) (ubase + (t_) * 4096)
#define UOFFX(t_) (((t_) < 64 ? ubase : xbase) + (t_) * 4096)
  v4u ka[4], va[4], kb[4], vb[4], kc[4], vc[4];
  WG_BARRIER();
  float x0[4], x1[4];
  {
    const f32x2v mm = *(LAS f32x2v*)(L + L_FL + 32);
    x0[0] = bflo(xr0.x) - mm.x; x0[1] = bfhi(xr0.x) - mm.x; x0[2] = bflo(xr0.y) - mm.x; x0[3] = bfhi(xr0.y) - mm.x;
    x1[0] = bflo(xr1.x) - mm.y; x1[1] = bfhi(xr1.x) - mm.y; x1[2] = bflo(xr1.y) - mm.y; x1[3] = bfhi(xr1.y) - mm.y;
  }
  const int redoff = ((g4 & 1) * 64 + c8 * 8 + (g4 >> 1) * 4) * 4;

  USEQ(); LDWG();
  USEO(); LDWD();
  USEG(); LDWU();
  USED(); LDK(ka, UOFF(w));
  USEU(); LDV(va, UOFF(w));
  TOUCH4(ka);
  LDK(kb, UOFF(w + 8));
  TOUCH4(va);
  LDV(vb, UOFF(w + 8));
  TOUCH4(kb);
  LDK(kc, UOFF(w + 16));
  TOUCH4(vb);
  LDV(vc, UOFF(w + 16));
  lds_wait(FL + 3, 1);
  v4u qf[4];
#pragma unroll
  for (int kk = 0; kk < 4; ++kk) qf[kk] = *(LAS v4u*)(L + L_Q + i16 * 272 + kk * 64 + g4 * 16);

  AttnState st;
  st.m = -INFINITY;
  st.l = 0.f;
#pragma unroll
  for (int e = 0; e < 8; ++e) st.o[e] = f32x4{0.f, 0.f, 0.f, 0.f};
  attn_unit(st, ka, va, qf);
  attn_unit(st, kb, vb, qf);
  int t = w + 24;
  for (;;) {
    const int o0 = t < nunit ? UOFFX(t) : OOB;
    const int ak0 = klane + o0, av0 = vlane + o0;
    LDK2(kb, ak0);
    LDV2(vb, av0);
    asm volatile("" ::"v"(ak0), "v"(av0));
    attn_unit(st, kc, vc, qf);
    if (t >= nunit) break;
    const int o1 = t + 8 < nunit ? UOFFX(t + 8) : OOB;
    const int ak1 = klane + o1, av1 = vlane + o1;
    LDK2(kc, ak1);
    LDV2(vc, av1);
    asm volatile("" ::"v"(ak1), "v"(av1));
    attn_unit(st, kb, vb, qf);
    if (t + 8 >= nunit) break;
    t += 16;
  }
  lds_signal(FL + 2);
  const int sfoff = ((tid >> 2) & 1) * 1024 + ((tid & 127) >> 3) * 64 + (tid & 3) * 16;
  const int oidx = ((tid >> 4) & 1) * D + cg * 256 + (rg & 15) * 16 + (tid & 15);
  unsigned xo = 0;
  if (w == 0) xo = *(const GAS unsigned short*)(p.x + oidx);

  {
    const float l = x32_sum(x16_sum(st.l));
    if (i16 < 8) {
#pragma unroll
      for (int e = 0; e < 8; ++e) *(LAS f32x4*)(L + L_O + (w * 256 + (i16 * 4 + g4) * 8 + (e ^ i16)) * 16) = st.o[e];
      if (g4 == 0) *(LAS v2u*)(L + L_ML + (i16 * 8 + w) * 8) = v2u{f2u(st.m), f2u(l)};
    }
  }
  int poff = g4 * RSP + WS_P1 + (hkv * 8 + w) * 4096 + (rg & 15) * 256 + i16 * 16;
  int goff = (lane >> 3) * RSG + WS_GU + rg * 1024 + ((lane >> 2) & 1) * 512 + (w * 8 + (lane & 3) * 2) * 8;
  int roff = ((tid >> 3) & 7) * RSS + (cg * 8 + (tid >> 6)) * 2048 + (rg & 15) * 128 + (tid & 7) * 16;
  int p1st = p1own + tid * 16;
  int s2st = (w < 2 ? WS_S2 : WS_F2) + s2own + (tid & 127) * 16;
  __builtin_amdgcn_s_waitcnt(0x0F70);
  asm volatile("" : "+v"(poff), "+v"(goff), "+v"(roff), "+v"(p1st), "+v"(s2st));
  WG_BARRIER();
  if (w < 4) {
    const int j = tid >> 5;
    v4u ml[4];
    f32x4 ov[8];
#pragma unroll
    for (int k = 0; k < 4; ++k) ml[k] = *(LAS v4u*)(L + L_ML + j * 64 + k * 16);
#pragma unroll
    for (int ww = 0; ww < 8; ++ww) ov[ww] = *(LAS f32x4*)(L + L_O + (ww * 256 + (tid ^ j)) * 16);
    SB();
    float M = fmaxf(u2f(ml[0].x), u2f(ml[0].z));
#pragma unroll
    for (int k = 1; k < 4; ++k) M = fmaxf(M, fmaxf(u2f(ml[k].x), u2f(ml[k].z)));
    float Ls = 0.f;
    f32x4 acc = {0.f, 0.f, 0.f, 0.f};
#pragma unroll
    for (int ww = 0; ww < 8; ++ww) {
      const float wt = __builtin_amdgcn_exp2f(u2f(ml[ww >> 1][(ww & 1) * 2]) - M);
      Ls = fmaf(wt, u2f(ml[ww >> 1][(ww & 1) * 2 + 1]), Ls);
      acc += ov[ww] * wt;
    }
    acc *= rcp(Ls);
    const unsigned lse = f2u(M + __builtin_amdgcn_logf(Ls));
    v4u pk;
#pragma unroll
    for (int r = 0; r < 4; ++r) {
      const unsigned t = ((f2u(acc[r]) + 0x80u) & 0xFFFFFF00u) | ((lse >> (8 * r)) & 0xFFu);
      pk[r] = t == SENT ? 0x7FC000FFu : t;
    }
    __builtin_amdgcn_raw_buffer_store_b128(pk, rs, p1st, 0, AUX_SC1);
  }
  SB();

  f32x4 wof[8];
  {
    v4u gu = __builtin_amdgcn_raw_buffer_load_b128(rs, goff, 0, AUX_SC1);
    v4u pa = __builtin_amdgcn_raw_buffer_load_b128(rs, poff, 0, AUX_SC1);
    v4u pb = __builtin_amdgcn_raw_buffer_load_b128(rs, poff, 4 * RSP, AUX_SC1);
    SB();
    if (has_ffn) {
      const unsigned e_lo = FL[6], e_hi = FL[7];
      const unsigned tag_hi = (e_lo >> 8) | (e_hi << 24), tag_lo = e_lo & 0xFFu;
      v2u wt[8];
#pragma unroll
      for (int k = 0; k < 8; ++k) wt[k] = *(LAS v2u*)(L + L_WD + (w * 8 + k) * 512 + lane * 8);
      if (!__all(tag2(gu, tag_lo, tag_hi))) {
#pragma nounroll
        for (unsigned spins = 0; spins < SPIN_CAP; ++spins) {
          asm volatile("" ::: "memory");
          __builtin_amdgcn_s_sleep(1);
          gu = __builtin_amdgcn_raw_buffer_load_b128(rs, goff, 0, AUX_SC1);
          if (__all(tag2(gu, tag_lo, tag_hi))) break;
        }
      }
      const float ga = x32_sum(x16_sum(u2f(gu.x << 8))), gb = x32_sum(x16_sum(u2f(gu.z << 8)));
      const float hx = silu(ga) * dpp<XOR8>(ga), hy = silu(gb) * dpp<XOR8>(gb);
      float h0[8], h1[8];
#pragma unroll
      for (int pr = 0; pr < 4; ++pr) {
        h0[2 * pr] = rdl(hx, pr); h0[2 * pr + 1] = rdl(hy, pr);
        h1[2 * pr] = rdl(hx, 4 + pr); h1[2 * pr + 1] = rdl(hy, 4 + pr);
      }
      f32x4 s0, s1;
      gemv256(wt, h0, h1, s0, s1);
      *(LAS f32x4*)(L + L_F + w * 2048 + lane * 16) = s0;
      *(LAS f32x4*)(L + L_F + w * 2048 + 1024 + lane * 16) = s1;
    }
#pragma unroll
    for (int k = 0; k < 8; ++k) {
      const v2u t = *(LAS v2u*)(L + L_WO + (w * 8 + k) * 512 + lane * 8);
      wof[k] = f32x4{bflo(t.x), bfhi(t.x), bflo(t.y), bfhi(t.y)};
      asm volatile("" : "+v"(wof[k]));
    }
    bool ha = full4(pa), hb = full4(pb);
#pragma nounroll
    for (unsigned spins = 0; spins < SPIN_CAP; ++spins) {
      asm volatile("" ::: "memory");
      if (__all(ha & hb)) break;
      if (!ha) pa = __builtin_amdgcn_raw_buffer_load_b128(rs, poff, 0, AUX_SC1);
      if (!hb) pb = __builtin_amdgcn_raw_buffer_load_b128(rs, poff, 4 * RSP, AUX_SC1);
      ha = full4(pa);
      hb = full4(pb);
    }
    const float la = u2f(__builtin_amdgcn_perm(__builtin_amdgcn_perm(pa.w, pa.z, 0x0c0c0400u), __builtin_amdgcn_perm(pa.y, pa.x, 0x0c0c0400u), 0x05040100u));
    const float lb = u2f(__builtin_amdgcn_perm(__builtin_amdgcn_perm(pb.w, pb.z, 0x0c0c0400u), __builtin_amdgcn_perm(pb.y, pb.x, 0x0c0c0400u), 0x05040100u));
    const float Mw = x32_max(x16_max(fmaxf(la, lb)));
    const float wa = __builtin_amdgcn_exp2f(la - Mw), wb = __builtin_amdgcn_exp2f(lb - Mw);
    const float Ww = x32_sum(x16_sum(wa + wb));
    float a[4];
#pragma unroll
    for (int r = 0; r < 4; ++r) a[r] = fmaf(u2f(pa[r] & 0xFFFFFF00u), wa, u2f(pb[r] & 0xFFFFFF00u) * wb);
    const float v = tr32(tr16(a[0], a[1]), tr16(a[2], a[3]));
    *(LAS float*)(L + L_C + (((w >> 2) * 64 + (i16 >> 3) * 32 + g4 * 8 + (i16 & 7)) * 4 + (w & 3)) * 4) = v;
    if (lane == 0) *(LAS v2u*)(L + L_C + 2048 + w * 8) = v2u{f2u(Mw), f2u(Ww)};
  }
  WG_BARRIER();
  {
    const int b = (lane >> 3) & 1, k = lane & 7;
    const v4u m0 = *(LAS v4u*)(L + L_C + 2048 + b * 32), m1 = *(LAS v4u*)(L + L_C + 2048 + b * 32 + 16);
    const f32x4 av = *(LAS f32x4*)(L + L_C + (b * 64 + w * 8 + k) * 16);
    const float M = fmaxf(fmaxf(u2f(m0.x), u2f(m0.z)), fmaxf(u2f(m1.x), u2f(m1.z)));
    const float f0 = __builtin_amdgcn_exp2f(u2f(m0.x) - M), f1 = __builtin_amdgcn_exp2f(u2f(m0.z) - M);
    const float f2 = __builtin_amdgcn_exp2f(u2f(m1.x) - M), f3 = __builtin_amdgcn_exp2f(u2f(m1.z) - M);
    const float num = fmaf(f0, av.x, f1 * av.y) + fmaf(f2, av.z, f3 * av.w);
    const float den = fmaf(f0, u2f(m0.y), f1 * u2f(m0.w)) + fmaf(f2, u2f(m1.y), f3 * u2f(m1.w));
    const float ov = num * rcp(den);
    f32x4 s0 = {0.f, 0.f, 0.f, 0.f}, s1 = {0.f, 0.f, 0.f, 0.f};
#pragma unroll
    for (int kk = 0; kk < 8; ++kk) { s0 += wof[kk] * rdl(ov, kk); s1 += wof[kk] * rdl(ov, 8 + kk); }
    *(LAS f32x4*)(L + L_S + w * 2048 + lane * 16) = s0;
    *(LAS f32x4*)(L + L_S + w * 2048 + 1024 + lane * 16) = s1;
  }
  WG_BARRIER();
  if (w < 2 || (w < 4 && has_ffn)) {
    const int lb = (w < 2 ? L_S : L_F) + sfoff;
    f32x4 t[8];
#pragma unroll
    for (int ww = 0; ww < 8; ++ww) t[ww] = *(LAS f32x4*)(L + lb + ww * 2048);
    SB();
    const f32x4 s4 = ((t[0] + t[1]) + (t[2] + t[3])) + ((t[4] + t[5]) + (t[6] + t[7]));
    __builtin_amdgcn_raw_buffer_store_b128(clean4(s4), rs, s2st, 0, AUX_SC1);
  }

  if (reducer) {
    v4u a = __builtin_amdgcn_raw_buffer_load_b128(rs, roff, WS_S2, AUX_SC1);
    v4u c = {0u, 0u, 0u, 0u};
    if (((tid >> 3) & 7) != xl) c = __builtin_amdgcn_raw_buffer_load_b128(rs, roff, WS_F2, AUX_SC1);
    bool ha = full4(a), hc = full4(c);
#pragma nounroll
    for (unsigned spins = 0; spins < SPIN_CAP; ++spins) {
      asm volatile("" ::: "memory");
      if (__all(ha & hc)) break;
      if (!ha) a = __builtin_amdgcn_raw_buffer_load_b128(rs, roff, WS_S2, AUX_SC1);
      if (!hc) c = __builtin_amdgcn_raw_buffer_load_b128(rs, roff, WS_F2, AUX_SC1);
      ha = full4(a);
      hc = full4(c);
    }
    float vs = tr32(tr16(u2f(a.x), u2f(a.y)), tr16(u2f(a.z), u2f(a.w)));
    float vf = tr32(tr16(u2f(c.x), u2f(c.y)), tr16(u2f(c.z), u2f(c.w)));
    vs += dpp<XOR8>(vs);
    vf += dpp<XOR8>(vf);
    *(LAS float*)(L + L_R + ((lane >> 3) & 1) * 1024 + ((lane & 7) * 4 + (lane >> 4)) * 32 + w * 4) = (lane & 8) ? vs : vf;
    WG_BARRIER();
    if (tid < 32) {
      const f32x4 f0 = *(LAS f32x4*)(L + L_R + tid * 32), f1 = *(LAS f32x4*)(L + L_R + tid * 32 + 16);
      const f32x4 s0 = *(LAS f32x4*)(L + L_R + 1024 + tid * 32), s1 = *(LAS f32x4*)(L + L_R + 1024 + tid * 32 + 16);
      const f32x4 f4 = f0 + f1, s4 = s0 + s1;
      const float f = (f4.x + f4.y) + (f4.z + f4.w), sv = (s4.x + s4.y) + (s4.z + s4.w);
      const float r = fmaf(sv, silu(f), bflo(xo));
      p.out[oidx] = (unsigned short)(cvtpk(r, 0.f) & 0xFFFFu);
    }
    if (bid == NWG - 1 && tid == 0) {
      const v2u ne = {FL[6], FL[7]};
      __builtin_amdgcn_raw_buffer_store_b64(ne, rs, WS_E, 0, AUX_SC1);
    }
  }
}

}

extern "C" void kernel_launch(void* const* d_in, const int* in_sizes, int n_in, void* d_out, int out_size, void* d_ws,
                              size_t ws_size, hipStream_t stream) {
  Params p;
  p.x = (const unsigned short*)d_in[0];
  p.kc = (const unsigned short*)d_in[1];
  p.vc = (const unsigned short*)d_in[2];
  p.wq = (const unsigned short*)d_in[3];
  p.wo = (const unsigned short*)d_in[4];
  p.wg = (const unsigned short*)d_in[6];
  p.wu = (const unsigned short*)d_in[7];
  p.wd = (const unsigned short*)d_in[8];
  p.out = (unsigned short*)d_out;
  p.ws = (unsigned char*)d_ws;
  hipLaunchKernelGGL(fused_decode, dim3(NWG), dim3(NTHR), 0, stream, p);
}
```

```cpp
#define DIAG 0
#include <hip/hip_runtime.h>
#include <hip/hip_bf16.h>
#include <stdint.h>

#define GAS __attribute__((address_space(1)))
#define LAS __attribute__((address_space(3)))
typedef unsigned v4u __attribute__((ext_vector_type(4)));
typedef unsigned v2u __attribute__((ext_vector_type(2)));
typedef float f32x4 __attribute__((ext_vector_type(4)));
typedef short bf16x8 __attribute__((ext_vector_type(8)));
typedef short bf16x4 __attribute__((ext_vector_type(4)));
typedef unsigned long long u64;

namespace {

constexpr int NWG = 256, NTHR = 576;
constexpr int D = 1024, HD = 4096, FF = 3584, SK = 32768;
constexpr int AUX_SC1 = 16;
constexpr unsigned SENT = 0xFFFFFFFFu;
constexpr unsigned SPIN_CAP = 1u << 22;

constexpr int RSG = 65536, RSP = 131072, RSS = 65536;
constexpr int WS_E = 0;
constexpr int WS_QP = 4096;
constexpr int WS_GU = WS_QP + 262144;
constexpr int WS_P1 = WS_GU + 8 * RSG;
constexpr int WS_S2 = WS_P1 + 8 * RSP;
constexpr int WS_F2 = WS_S2 + 8 * RSS;
constexpr int WS_DG = WS_F2 + 8 * RSS;
constexpr int WS_ALL = WS_DG + 8192;

constexpr int L_RED = 0;
constexpr int L_Q = 12288;
constexpr int L_ML = L_Q + 16 * 272;
constexpr int L_O = L_ML + 512;
constexpr int L_S = L_O + 32768;
constexpr int L_C = L_S + 16384;
constexpr int L_R = L_C + 2048 + 64;
constexpr int L_FL = L_R + 2048 + 512;
constexpr int L_WO = L_FL + 64;
constexpr int L_WD = L_WO + 32768;
constexpr int L_F = L_WD + 32768;
constexpr int L_USED = L_F + 16384;
constexpr int L_BYTES = 152 * 1024;
static_assert(L_USED <= L_BYTES, "lds");

struct Params {
  const unsigned short* x;
  const unsigned short* wq;
  unsigned char* ws;
  const unsigned short* wg;
  const unsigned short* wu;
  const unsigned short* wd;
  const unsigned short* wo;
  const unsigned short* kc;
  const unsigned short* vc;
  unsigned short* out;
};

__device__ __forceinline__ float bflo(unsigned u) { return __builtin_bit_cast(float, u << 16); }
__device__ __forceinline__ float bfhi(unsigned u) { return __builtin_bit_cast(float, u & 0xFFFF0000u); }
__device__ __forceinline__ unsigned f2u(float f) { return __builtin_bit_cast(unsigned, f); }
__device__ __forceinline__ float u2f(unsigned u) { return __builtin_bit_cast(float, u); }
typedef __bf16 bf16p __attribute__((ext_vector_type(2)));
typedef float f32x2 __attribute__((ext_vector_type(2)));
__device__ __forceinline__ unsigned cvtpk(float lo, float hi) {
  return __builtin_bit_cast(unsigned, __builtin_convertvector(f32x2{lo, hi}, bf16p));
}
template <int CTRL> __device__ __forceinline__ float dpp(float x) {
  return __builtin_bit_cast(float, __builtin_amdgcn_mov_dpp(__builtin_bit_cast(int, x), CTRL, 0xf, 0xf, true));
}
constexpr int XOR1 = 0xB1, XOR2 = 0x4E, XOR7 = 0x141, XOR8 = 0x128;
__device__ __forceinline__ float x16_sum(float x) {
  auto s = __builtin_amdgcn_permlane16_swap(f2u(x), f2u(x), false, false);
  return u2f(s[0]) + u2f(s[1]);
}
__device__ __forceinline__ float x32_sum(float x) {
  auto s = __builtin_amdgcn_permlane32_swap(f2u(x), f2u(x), false, false);
  return u2f(s[0]) + u2f(s[1]);
}
__device__ __forceinline__ float x16_max(float x) {
  auto s = __builtin_amdgcn_permlane16_swap(f2u(x), f2u(x), false, false);
  return fmaxf(u2f(s[0]), u2f(s[1]));
}
__device__ __forceinline__ float x32_max(float x) {
  auto s = __builtin_amdgcn_permlane32_swap(f2u(x), f2u(x), false, false);
  return fmaxf(u2f(s[0]), u2f(s[1]));
}
__device__ __forceinline__ float row16_sum(float x) {
  x += dpp<XOR1>(x);
  x += dpp<XOR2>(x);
  x += dpp<XOR7>(x);
  x += dpp<XOR8>(x);
  return x;
}
__device__ __forceinline__ float wave_sum(float x) { return x32_sum(x16_sum(row16_sum(x))); }
__device__ __forceinline__ float tr16(float a, float b) {
  auto s = __builtin_amdgcn_permlane16_swap(f2u(a), f2u(b), false, false);
  return u2f(s[0]) + u2f(s[1]);
}
__device__ __forceinline__ float tr32(float a, float b) {
  auto s = __builtin_amdgcn_permlane32_swap(f2u(a), f2u(b), false, false);
  return u2f(s[0]) + u2f(s[1]);
}
__device__ __forceinline__ v4u ldnt(const GAS v4u* p) { return __builtin_nontemporal_load(p); }
__device__ __forceinline__ unsigned clean(float f) {
  const unsigned u = f2u(f);
  return u == SENT ? 0x7FC00000u : u;
}
__device__ __forceinline__ v4u clean4(f32x4 v) { return v4u{clean(v.x), clean(v.y), clean(v.z), clean(v.w)}; }
__device__ __forceinline__ bool full4(v4u v) { return v.x != SENT && v.y != SENT && v.z != SENT && v.w != SENT; }
__device__ __forceinline__ float rdl(float v, int lane) { return u2f(__builtin_amdgcn_readlane(f2u(v), lane)); }
__device__ __forceinline__ float rcp(float v) { return __builtin_amdgcn_rcpf(v); }
__device__ __forceinline__ float silu(float v) { return v * rcp(1.0f + __builtin_amdgcn_exp2f(v * -1.4426950408889634f)); }

#define SB() __builtin_amdgcn_sched_barrier(0)
#define WG_BARRIER()                                        \
  do {                                                      \
    asm volatile("s_waitcnt lgkmcnt(0)" ::: "memory");      \
    __builtin_amdgcn_s_barrier();                           \
    asm volatile("" ::: "memory");                          \
  } while (0)

__device__ __forceinline__ f32x4 gemv64(const v4u (&w)[4], const float (&x0)[4], const float (&x1)[4]) {
  float a0[8], a1[8];
#pragma unroll
  for (int c = 0; c < 8; ++c) { a0[c] = 0.f; a1[c] = 0.f; }
#pragma unroll
  for (int i = 0; i < 4; ++i) {
#pragma unroll
    for (int k = 0; k < 4; ++k) {
      const float lo = bflo(w[i][k]), hi = bfhi(w[i][k]);
      a0[2 * k] = fmaf(lo, x0[i], a0[2 * k]);
      a0[2 * k + 1] = fmaf(hi, x0[i], a0[2 * k + 1]);
      a1[2 * k] = fmaf(lo, x1[i], a1[2 * k]);
      a1[2 * k + 1] = fmaf(hi, x1[i], a1[2 * k + 1]);
    }
  }
  float t[8];
#pragma unroll
  for (int c = 0; c < 8; ++c) t[c] = tr16(a0[c], a1[c]);
  f32x4 r;
#pragma unroll
  for (int c = 0; c < 4; ++c) {
    float v = tr32(t[c], t[c + 4]);
    v += dpp<XOR8>(v);
    r[c] = v;
  }
  return r;
}

__device__ __forceinline__ void gemv256(const v2u (&w)[8], const float (&c0)[8], const float (&c1)[8], f32x4& s0, f32x4& s1) {
  s0 = f32x4{0.f, 0.f, 0.f, 0.f};
  s1 = f32x4{0.f, 0.f, 0.f, 0.f};
#pragma unroll
  for (int k = 0; k < 8; ++k) {
    const f32x4 wf = {bflo(w[k].x), bfhi(w[k].x), bflo(w[k].y), bfhi(w[k].y)};
#pragma unroll
    for (int c = 0; c < 4; ++c) {
      s0[c] = fmaf(wf[c], c0[k], s0[c]);
      s1[c] = fmaf(wf[c], c1[k], s1[c]);
    }
  }
}

__device__ __forceinline__ v2u granule(float v, unsigned tag_lo, unsigned tag_hi) {
  return v2u{(tag_lo << 24) | ((f2u(v) + 0x80u) >> 8), tag_hi};
}
__device__ __forceinline__ bool tag2(v4u g, unsigned tag_lo, unsigned tag_hi) {
  return (g.y == tag_hi) & ((g.x >> 24) == tag_lo) & (g.w == tag_hi) & ((g.z >> 24) == tag_lo);
}
#define TOUCH4(a) asm volatile("" : "+v"(a[0]), "+v"(a[1]), "+v"(a[2]), "+v"(a[3]))
constexpr int AUX_NT = 2;
#define LDK(kb_, off_) do { _Pragma("unroll") for (int j = 0; j < 4; ++j) kb_[j] = __builtin_amdgcn_raw_buffer_load_b128(rk, (klane + (off_)) + j * 64, 0, AUX_NT); SB(); } while (0)
#define LDV(vb_, off_) do { _Pragma("unroll") for (int j = 0; j < 4; ++j) vb_[j] = __builtin_amdgcn_raw_buffer_load_b128(rv, (vlane + (off_)) + j * 1024, 0, AUX_NT); SB(); } while (0)
#define LDK2(kb_, a_) do { _Pragma("unroll") for (int j = 0; j < 4; ++j) kb_[j] = __builtin_amdgcn_raw_buffer_load_b128(rk, (a_) + j * 64, 0, AUX_NT); SB(); } while (0)
#define LDV2(vb_, a_) do { _Pragma("unroll") for (int j = 0; j < 4; ++j) vb_[j] = __builtin_amdgcn_raw_buffer_load_b128(rv, (a_) + j * 1024, 0, AUX_NT); SB(); } while (0)

struct AttnState {
  float m, l;
  f32x4 o[8];
};
__device__ __forceinline__ v2u attn_qk(AttnState& st, const v4u (&kc)[4], const v4u (&qf)[4]) {
  constexpr float C2 = 0.088388347648318440f * 1.4426950408889634f;
  f32x4 s = {0.f, 0.f, 0.f, 0.f};
#pragma unroll
  for (int kk = 0; kk < 4; ++kk)
    s = __builtin_amdgcn_mfma_f32_16x16x32_bf16(__builtin_bit_cast(bf16x8, kc[kk]), __builtin_bit_cast(bf16x8, qf[kk]), s, 0, 0, 0);
#pragma unroll
  for (int r = 0; r < 4; ++r) s[r] = (s[r] + dpp<XOR8>(s[r])) * C2;
  float mx = fmaxf(fmaxf(s[0], s[1]), fmaxf(s[2], s[3]));
  mx = x32_max(x16_max(mx));
  const float mn = fmaxf(st.m, mx);
  const float alpha = __builtin_amdgcn_exp2f(st.m - mn);
  st.m = mn;
  const float p0 = __builtin_amdgcn_exp2f(s[0] - mn), p1 = __builtin_amdgcn_exp2f(s[1] - mn);
  const float p2 = __builtin_amdgcn_exp2f(s[2] - mn), p3 = __builtin_amdgcn_exp2f(s[3] - mn);
  st.l = fmaf(st.l, alpha, (p0 + p1) + (p2 + p3));
  const v2u pb = {cvtpk(p0, p1), cvtpk(p2, p3)};
#pragma unroll
  for (int e = 0; e < 8; ++e) st.o[e] *= alpha;
  SB();
  return pb;
}
__device__ __forceinline__ void attn_pv(AttnState& st, const v4u (&vc)[4], v2u pb) {
#pragma unroll
  for (int e = 0; e < 8; ++e) {
    const unsigned sel = (e & 1) ? 0x07060302u : 0x05040100u;
    const v2u a = {__builtin_amdgcn_perm(vc[1][e >> 1], vc[0][e >> 1], sel), __builtin_amdgcn_perm(vc[3][e >> 1], vc[2][e >> 1], sel)};
    st.o[e] = __builtin_amdgcn_mfma_f32_16x16x16bf16_1k(__builtin_bit_cast(bf16x4, a), __builtin_bit_cast(bf16x4, pb), st.o[e], 0, 0, 0);
  }
  SB();
}
__device__ __forceinline__ void attn_unit(AttnState& st, const v4u (&kc)[4], const v4u (&vc)[4], const v4u (&qf)[4]) {
  attn_pv(st, vc, attn_qk(st, kc, qf));
}

#define DRAIN() asm volatile("s_waitcnt vmcnt(0)" ::: "memory")
constexpr int MV = 3;
typedef float f32x2v __attribute__((ext_vector_type(2)));
__device__ __forceinline__ void lds_signal(LAS unsigned* c) {
  asm volatile("s_waitcnt lgkmcnt(0)" ::: "memory");
  if ((threadIdx.x & 63) == 0) __hip_atomic_fetch_add(c, 1u, __ATOMIC_RELAXED, __HIP_MEMORY_SCOPE_WORKGROUP);
}
__device__ __forceinline__ void lds_wait(LAS unsigned* c, unsigned want) {
#pragma nounroll
  for (unsigned spins = 0; spins < (SPIN_CAP << 3); ++spins) {
    asm volatile("" ::: "memory");
    if (__builtin_amdgcn_readfirstlane(*(volatile LAS unsigned*)c) >= want) break;
    __builtin_amdgcn_s_sleep(1);
  }
  asm volatile("" ::: "memory");
}

__global__ void __launch_bounds__(NTHR) fused_decode(Params p) {
  __shared__ __attribute__((aligned(16))) unsigned char smem[L_BYTES];
  LAS unsigned char* L = (LAS unsigned char*)smem;
  LAS unsigned* FL = (LAS unsigned*)(L + L_FL);
  const unsigned tstart = (unsigned)__builtin_amdgcn_s_memrealtime();
  SB();
  const int tid = (int)threadIdx.x - 64, lane = threadIdx.x & 63;
  const int w = __builtin_amdgcn_readfirstlane(threadIdx.x >> 6) - 1;
  const int bid = blockIdx.x;
  const int grp = bid >> 5, xq = bid & 7;
  const int cg = (bid >> 3) & 3, rg = grp * 8 + xq;
  const int hkv = bid >> 6, bb = grp & 1;
  const int xl = (3 - (int)((size_t)p.wq >> 7)) & 7;
  const bool has_ffn = xq != xl;
  const int fi = has_ffn ? grp * 7 + (xq < xl ? xq : xq - 1) : 0;
  const bool reducer = rg >= 48;
  constexpr int OOB = 0x7FFF0000;
  const __amdgpu_buffer_rsrc_t rs = __builtin_amdgcn_make_buffer_rsrc((void*)p.ws, 0, WS_ALL, 0x00020000);
  const int p1own = (bid & 7) * RSP + WS_P1 + (bid >> 3) * 4096;
  const int s2own = (rg & 7) * RSS + (cg * 8 + (rg >> 3)) * 2048;
  const GAS v4u* xg = (const GAS v4u*)p.x;

  if (w < 0) {
    __builtin_amdgcn_s_setprio(3);
    const v4u xa0 = xg[lane * 2], xa1 = xg[lane * 2 + 1], xb0 = xg[128 + lane * 2], xb1 = xg[128 + lane * 2 + 1];
    const v2u ev = __builtin_amdgcn_raw_buffer_load_b64(rs, WS_E, 0, AUX_SC1);
    if (lane < 6) FL[lane] = 0u;
    float rr;
    {
      float va_[16], vb_[16];
#pragma unroll
      for (int k = 0; k < 4; ++k) {
        va_[2 * k] = bflo(xa0[k]); va_[2 * k + 1] = bfhi(xa0[k]); va_[8 + 2 * k] = bflo(xa1[k]); va_[8 + 2 * k + 1] = bfhi(xa1[k]);
        vb_[2 * k] = bflo(xb0[k]); vb_[2 * k + 1] = bfhi(xb0[k]); vb_[8 + 2 * k] = bflo(xb1[k]); vb_[8 + 2 * k + 1] = bfhi(xb1[k]);
      }
      float sa = 0.f, sb = 0.f;
#pragma unroll
      for (int k = 0; k < 16; ++k) { sa += va_[k]; sb += vb_[k]; }
      const float ma = wave_sum(sa) * (1.0f / D), mb = wave_sum(sb) * (1.0f / D);
      if (lane == 0) *(LAS f32x2v*)(L + L_FL + 32) = f32x2v{ma, mb};
      WG_BARRIER();
      float qa = 0.f, qb = 0.f;
#pragma unroll
      for (int k = 0; k < 16; ++k) { const float da = va_[k] - ma, db = vb_[k] - mb; qa = fmaf(da, da, qa); qb = fmaf(db, db, qb); }
      const float ta = wave_sum(qa), tb = wave_sum(qb);
      rr = 1.0f / sqrtf((lane < 32 ? ta : tb) * (1.0f / D) + 1e-5f);
    }
    const u64 e1 = ((((u64)__builtin_amdgcn_readfirstlane(ev.y)) << 32) | (u64)(unsigned)__builtin_amdgcn_readfirstlane(ev.x)) + 1ull;
    const unsigned tag_hi = (unsigned)(e1 >> 8), tag_lo = (unsigned)(e1 & 0xFFull);
    const unsigned tq = (unsigned)e1 & 0xFFFFFFu;
    if (lane == 0) { FL[6] = (unsigned)e1; FL[7] = (unsigned)(e1 >> 32); }
    lds_wait(FL + 0, 8);
    {
      f32x2v a = {0.f, 0.f};
#pragma unroll
      for (int ww = 0; ww < 8; ++ww) a += *(LAS f32x2v*)(L + L_RED + ww * 512 + lane * 8);
      const unsigned v0 = (f2u(a.x * rr) + 0x800u) >> 12, v1 = (f2u(a.y * rr) + 0x800u) >> 12;
      __builtin_amdgcn_raw_buffer_store_b64(v2u{v0 | (v1 << 20), (v1 >> 12) | (tq << 8)}, rs,
          WS_QP + ((hkv * 2 + (lane >> 5)) * 4 + cg) * 4096 + (rg & 15) * 256 + (lane & 31) * 8, 0, AUX_SC1);
    }
    if (lane == 0) __builtin_amdgcn_raw_buffer_store_b32(tstart, rs, WS_DG + bid * 4, 0, AUX_SC1);
    DRAIN();
    {
      const int qoff = WS_QP + (hkv * 2 + bb) * 16384 + lane * 16;
      v4u qg[16];
#pragma nounroll
      for (unsigned spins = 0; spins < SPIN_CAP; ++spins) {
        asm volatile("" ::: "memory");
        bool ok = true;
#pragma unroll
        for (int j = 0; j < 16; ++j) qg[j] = __builtin_amdgcn_raw_buffer_load_b128(rs, qoff + (j & 3) * 1024, (j >> 2) * 4096, AUX_SC1);
#pragma unroll
        for (int j = 0; j < 16; ++j) ok &= ((qg[j].y >> 8) == tq) & ((qg[j].w >> 8) == tq);
        if (__all(ok)) break;
        __builtin_amdgcn_s_sleep(4);
      }
#pragma unroll
      for (int j = 0; j < 4; ++j) {
        f32x4 q = {0.f, 0.f, 0.f, 0.f};
#pragma unroll
        for (int ks = 0; ks < 4; ++ks) {
          const v4u g = qg[ks * 4 + j];
          q += f32x4{u2f(g.x << 12), u2f((g.y << 24) | ((g.x >> 20) << 12)), u2f(g.z << 12), u2f((g.w << 24) | ((g.z >> 20) << 12))};
        }
        const v2u hi = {cvtpk(q.x, q.y), cvtpk(q.z, q.w)};
        const v2u lo = {cvtpk(q.x - bflo(hi.x), q.y - bfhi(hi.x)), cvtpk(q.z - bflo(hi.y), q.w - bfhi(hi.y))};
        *(LAS v2u*)(L + L_Q + (2 * j + (lane >> 5)) * 272 + (lane & 31) * 8) = hi;
        *(LAS v2u*)(L + L_Q + (2 * j + 8 + (lane >> 5)) * 272 + (lane & 31) * 8) = lo;
      }
      lds_signal(FL + 3);
    }
    const int coff = WS_QP + (((lane >> 4) * 2) * 4 + cg) * 4096 + (lane & 15) * 256;
    if (reducer) {
#pragma nounroll
      for (unsigned spins = 0; spins < SPIN_CAP; ++spins) {
        asm volatile("" ::: "memory");
        const v4u ck = __builtin_amdgcn_raw_buffer_load_b128(rs, coff, 0, AUX_SC1);
        if (__all(((ck.y >> 8) == tq) & ((ck.w >> 8) == tq))) break;
        __builtin_amdgcn_s_sleep(4);
      }
    }
    if (has_ffn) {
      lds_wait(FL + 1, 8);
      v4u gr[2];
#pragma unroll
      for (int m = 0; m < 2; ++m) {
        f32x2v a = {0.f, 0.f};
#pragma unroll
        for (int ww = 0; ww < 8; ++ww) a += *(LAS f32x2v*)(L + L_RED + 4096 + m * 4096 + ww * 512 + lane * 8);
        const v2u g0 = granule(a.x * rr, tag_lo, tag_hi), g1 = granule(a.y * rr, tag_lo, tag_hi);
        gr[m] = v4u{g0.x, g0.y, g1.x, g1.y};
      }
      lds_wait(FL + 2, 4);
#pragma unroll
      for (int m = 0; m < 2; ++m) __builtin_amdgcn_raw_buffer_store_b128(gr[m], rs, (cg * 2 + m) * RSG + WS_GU + rg * 1024 + lane * 16, 0, AUX_SC1);
    }
    WG_BARRIER();
    return;
  }

  {
    const v4u s4 = {SENT, SENT, SENT, SENT};
    const int off = w < 4 ? p1own + w * 1024 : (w < 6 ? WS_S2 - 4096 : WS_F2 - 6144) + s2own + w * 1024;
    __builtin_amdgcn_raw_buffer_store_b128(s4, rs, off + lane * 16, 0, AUX_SC1);
    SB();
  }
  const int r8 = lane >> 3, c8 = lane & 7;
  const int krow = cg * 256 + w * 32 + r8 * 4;
  const v2u xr0 = *(const GAS v2u*)(p.x + krow), xr1 = *(const GAS v2u*)(p.x + D + krow);
  v4u wqr[4], wgr[4], wur[4], wdr[4], wor[4];
#pragma unroll
  for (int i = 0; i < 4; ++i) wqr[i] = ldnt((const GAS v4u*)(p.wq + (size_t)(krow + i) * HD + rg * 64 + c8 * 8));
  SB();
  const int fbytes = has_ffn ? D * FF * 2 : 0;
  const __amdgpu_buffer_rsrc_t rwg = __builtin_amdgcn_make_buffer_rsrc((void*)p.wg, 0, fbytes, 0x00020000);
  const __amdgpu_buffer_rsrc_t rwu = __builtin_amdgcn_make_buffer_rsrc((void*)p.wu, 0, fbytes, 0x00020000);
  const __amdgpu_buffer_rsrc_t rwd = __builtin_amdgcn_make_buffer_rsrc((void*)p.wd, 0, fbytes, 0x00020000);
  const __amdgpu_buffer_rsrc_t rwo = __builtin_amdgcn_make_buffer_rsrc((void*)p.wo, 0, HD * D * 2, 0x00020000);
  const int guoff = (krow * FF + fi * 64 + c8 * 8) * 2;
  const int tile = ((w * 8 + (lane >> 5)) * D + cg * 256 + (lane & 31) * 8) * 2;
#define LDWG() do { _Pragma("unroll") for (int i = 0; i < 4; ++i) wgr[i] = __builtin_amdgcn_raw_buffer_load_b128(rwg, guoff + i * FF * 2, 0, AUX_NT); SB(); } while (0)
#define LDWU() do { _Pragma("unroll") for (int i = 0; i < 4; ++i) wur[i] = __builtin_amdgcn_raw_buffer_load_b128(rwu, guoff + i * FF * 2, 0, AUX_NT); SB(); } while (0)
#define LDWD() do { _Pragma("unroll") for (int i = 0; i < 4; ++i) wdr[i] = __builtin_amdgcn_raw_buffer_load_b128(rwd, tile + i * 4 * D, fi * 64 * D * 2, AUX_NT); SB(); } while (0)
#define LDWO() do { _Pragma("unroll") for (int i = 0; i < 4; ++i) wor[i] = __builtin_amdgcn_raw_buffer_load_b128(rwo, tile + i * 4 * D, rg * 64 * D * 2, AUX_NT); SB(); } while (0)
#define USEQ() do { const f32x4 r = gemv64(wqr, x0, x1); if ((lane & 8) == 0) *(LAS f32x4*)(L + L_RED + w * 512 + redoff) = r; lds_signal(FL + 0); } while (0)
#define USEG() do { const f32x4 r = gemv64(wgr, x0, x1); if ((lane & 8) == 0) *(LAS f32x4*)(L + L_RED + 4096 + w * 512 + redoff) = r; SB(); } while (0)
#define USEU() do { const f32x4 r = gemv64(wur, x0, x1); if ((lane & 8) == 0) *(LAS f32x4*)(L + L_RED + 8192 + w * 512 + redoff) = r; lds_signal(FL + 1); } while (0)
#define USED() do { _Pragma("unroll") for (int i = 0; i < 4; ++i) *(LAS v4u*)(L + L_WD + (w * 4 + i) * 1024 + lane * 16) = wdr[i]; SB(); } while (0)
#define USEO() do { _Pragma("unroll") for (int i = 0; i < 4; ++i) *(LAS v4u*)(L + L_WO + (w * 4 + i) * 1024 + lane * 16) = wor[i]; SB(); } while (0)
  LDWO();
  const int i16 = lane & 15, g4 = lane >> 4;
  const __amdgpu_buffer_rsrc_t rk = __builtin_amdgcn_make_buffer_rsrc((void*)p.kc, 0, 8 * SK * 256, 0x00020000);
  const __amdgpu_buffer_rsrc_t rv = __builtin_amdgcn_make_buffer_rsrc((void*)p.vc, 0, 8 * SK * 256, 0x00020000);
  const int klane = (4 * (i16 & 3) + (i16 >> 2)) * 256 + g4 * 16, vlane = lane * 16;
  const int rsl = (((3 - (int)((size_t)p.wg >> 7)) & 7) + grp) & 7;
  const int xs = rsl < xl ? rsl : rsl + 1;
  const int nunit = rsl == 7 ? 64 : (!has_ffn ? 64 + MV : (xq == xs ? 64 - MV : 64));
  const int obase = ((bb * 4 + hkv) * 2048 + cg * 512) * 4096;
  const int ubase = obase + xq * 262144, xbase = obase + xs * 262144 - MV * 4096;
#define UOFF(t_) (ubase + (t_) * 4096)
#define UOFFX(t_) (((t_) < 64 ? ubase : xbase) + (t_) * 4096)
  v4u ka[4], va[4], kb[4], vb[4], kc[4], vc[4];
  WG_BARRIER();
  float x0[4], x1[4];
  {
    const f32x2v mm = *(LAS f32x2v*)(L + L_FL + 32);
    x0[0] = bflo(xr0.x) - mm.x; x0[1] = bfhi(xr0.x) - mm.x; x0[2] = bflo(xr0.y) - mm.x; x0[3] = bfhi(xr0.y) - mm.x;
    x1[0] = bflo(xr1.x) - mm.y; x1[1] = bfhi(xr1.x) - mm.y; x1[2] = bflo(xr1.y) - mm.y; x1[3] = bfhi(xr1.y) - mm.y;
  }
  const int redoff = ((g4 & 1) * 64 + c8 * 8 + (g4 >> 1) * 4) * 4;

  USEQ(); LDWG();
  USEO(); LDWD();
  USEG(); LDWU();
  USED(); LDK(ka, UOFF(w));
  USEU(); LDV(va, UOFF(w));
  TOUCH4(ka);
  LDK(kb, UOFF(w + 8));
  TOUCH4(va);
  LDV(vb, UOFF(w + 8));
  TOUCH4(kb);
  LDK(kc, UOFF(w + 16));
  TOUCH4(vb);
  LDV(vc, UOFF(w + 16));
  lds_wait(FL + 3, 1);
  v4u qf[4];
#pragma unroll
  for (int kk = 0; kk < 4; ++kk) qf[kk] = *(LAS v4u*)(L + L_Q + i16 * 272 + kk * 64 + g4 * 16);

  AttnState st;
  st.m = -INFINITY;
  st.l = 0.f;
#pragma unroll
  for (int e = 0; e < 8; ++e) st.o[e] = f32x4{0.f, 0.f, 0.f, 0.f};
  attn_unit(st, ka, va, qf);
  attn_unit(st, kb, vb, qf);
  int t = w + 24;
  for (;;) {
    const int o0 = t < nunit ? UOFFX(t) : OOB;
    const int ak0 = klane + o0, av0 = vlane + o0;
    LDK2(kb, ak0);
    LDV2(vb, av0);
    asm volatile("" ::"v"(ak0), "v"(av0));
    attn_unit(st, kc, vc, qf);
    if (t >= nunit) break;
    const int o1 = t + 8 < nunit ? UOFFX(t + 8) : OOB;
    const int ak1 = klane + o1, av1 = vlane + o1;
    LDK2(kc, ak1);
    LDV2(vc, av1);
    asm volatile("" ::"v"(ak1), "v"(av1));
    attn_unit(st, kb, vb, qf);
    if (t + 8 >= nunit) break;
    t += 16;
  }
  lds_signal(FL + 2);
  const int sfoff = ((tid >> 2) & 1) * 1024 + ((tid & 127) >> 3) * 64 + (tid & 3) * 16;
  const int oidx = ((tid >> 4) & 1) * D + cg * 256 + (rg & 15) * 16 + (tid & 15);
  unsigned xo = 0;
  if (w == 0) xo = *(const GAS unsigned short*)(p.x + oidx);

  {
    const float l = x32_sum(x16_sum(st.l));
    if (i16 < 8) {
#pragma unroll
      for (int e = 0; e < 8; ++e) *(LAS f32x4*)(L + L_O + (w * 256 + (i16 * 4 + g4) * 8 + (e ^ i16)) * 16) = st.o[e];
      if (g4 == 0) *(LAS v2u*)(L + L_ML + (i16 * 8 + w) * 8) = v2u{f2u(st.m), f2u(l)};
    }
  }
  int poff = g4 * RSP + WS_P1 + (hkv * 8 + w) * 4096 + (rg & 15) * 256 + i16 * 16;
  int goff = (lane >> 3) * RSG + WS_GU + rg * 1024 + ((lane >> 2) & 1) * 512 + (w * 8 + (lane & 3) * 2) * 8;
  int roff = ((tid >> 3) & 7) * RSS + (cg * 8 + (tid >> 6)) * 2048 + (rg & 15) * 128 + (tid & 7) * 16;
  int p1st = p1own + tid * 16;
  int s2st = (w < 2 ? WS_S2 : WS_F2) + s2own + (tid & 127) * 16;
  __builtin_amdgcn_s_waitcnt(0x0F70);
  asm volatile("" : "+v"(poff), "+v"(goff), "+v"(roff), "+v"(p1st), "+v"(s2st));
  WG_BARRIER();
  if (tid == 448) { __builtin_amdgcn_raw_buffer_store_b32((unsigned)__builtin_amdgcn_s_memrealtime(), rs, WS_DG + 1024 + bid * 4, 0, AUX_SC1); DRAIN(); }
  if (w < 4) {
    const int j = tid >> 5;
    v4u ml[4];
    f32x4 ov[8];
#pragma unroll
    for (int k = 0; k < 4; ++k) ml[k] = *(LAS v4u*)(L + L_ML + j * 64 + k * 16);
#pragma unroll
    for (int ww = 0; ww < 8; ++ww) ov[ww] = *(LAS f32x4*)(L + L_O + (ww * 256 + (tid ^ j)) * 16);
    SB();
    float M = fmaxf(u2f(ml[0].x), u2f(ml[0].z));
#pragma unroll
    for (int k = 1; k < 4; ++k) M = fmaxf(M, fmaxf(u2f(ml[k].x), u2f(ml[k].z)));
    float Ls = 0.f;
    f32x4 acc = {0.f, 0.f, 0.f, 0.f};
#pragma unroll
    for (int ww = 0; ww < 8; ++ww) {
      const float wt = __builtin_amdgcn_exp2f(u2f(ml[ww >> 1][(ww & 1) * 2]) - M);
      Ls = fmaf(wt, u2f(ml[ww >> 1][(ww & 1) * 2 + 1]), Ls);
      acc += ov[ww] * wt;
    }
    acc *= rcp(Ls);
    const unsigned lse = f2u(M + __builtin_amdgcn_logf(Ls));
    v4u pk;
#pragma unroll
    for (int r = 0; r < 4; ++r) {
      const unsigned t = ((f2u(acc[r]) + 0x80u) & 0xFFFFFF00u) | ((lse >> (8 * r)) & 0xFFu);
      pk[r] = t == SENT ? 0x7FC000FFu : t;
    }
    __builtin_amdgcn_raw_buffer_store_b128(pk, rs, p1st, 0, AUX_SC1);
  }
  SB();

  f32x4 wof[8];
  {
    v4u gu = __builtin_amdgcn_raw_buffer_load_b128(rs, goff, 0, AUX_SC1);
    v4u pa = __builtin_amdgcn_raw_buffer_load_b128(rs, poff, 0, AUX_SC1);
    v4u pb = __builtin_amdgcn_raw_buffer_load_b128(rs, poff, 4 * RSP, AUX_SC1);
    SB();
    if (has_ffn) {
      const unsigned e_lo = FL[6], e_hi = FL[7];
      const unsigned tag_hi = (e_lo >> 8) | (e_hi << 24), tag_lo = e_lo & 0xFFu;
      v2u wt[8];
#pragma unroll
      for (int k = 0; k < 8; ++k) wt[k] = *(LAS v2u*)(L + L_WD + (w * 8 + k) * 512 + lane * 8);
      if (!__all(tag2(gu, tag_lo, tag_hi))) {
#pragma nounroll
        for (unsigned spins = 0; spins < SPIN_CAP; ++spins) {
          asm volatile("" ::: "memory");
          __builtin_amdgcn_s_sleep(1);
          gu = __builtin_amdgcn_raw_buffer_load_b128(rs, goff, 0, AUX_SC1);
          if (__all(tag2(gu, tag_lo, tag_hi))) break;
        }
      }
      const float ga = x32_sum(x16_sum(u2f(gu.x << 8))), gb = x32_sum(x16_sum(u2f(gu.z << 8)));
      const float hx = silu(ga) * dpp<XOR8>(ga), hy = silu(gb) * dpp<XOR8>(gb);
      float h0[8], h1[8];
#pragma unroll
      for (int pr = 0; pr < 4; ++pr) {
        h0[2 * pr] = rdl(hx, pr); h0[2 * pr + 1] = rdl(hy, pr);
        h1[2 * pr] = rdl(hx, 4 + pr); h1[2 * pr + 1] = rdl(hy, 4 + pr);
      }
      f32x4 s0, s1;
      gemv256(wt, h0, h1, s0, s1);
      *(LAS f32x4*)(L + L_F + w * 2048 + lane * 16) = s0;
      *(LAS f32x4*)(L + L_F + w * 2048 + 1024 + lane * 16) = s1;
    }
#pragma unroll
    for (int k = 0; k < 8; ++k) {
      const v2u t = *(LAS v2u*)(L + L_WO + (w * 8 + k) * 512 + lane * 8);
      wof[k] = f32x4{bflo(t.x), bfhi(t.x), bflo(t.y), bfhi(t.y)};
      asm volatile("" : "+v"(wof[k]));
    }
    bool ha = full4(pa), hb = full4(pb);
#pragma nounroll
    for (unsigned spins = 0; spins < SPIN_CAP; ++spins) {
      asm volatile("" ::: "memory");
      if (__all(ha & hb)) break;
      if (!ha) pa = __builtin_amdgcn_raw_buffer_load_b128(rs, poff, 0, AUX_SC1);
      if (!hb) pb = __builtin_amdgcn_raw_buffer_load_b128(rs, poff, 4 * RSP, AUX_SC1);
      ha = full4(pa);
      hb = full4(pb);
    }
    const float la = u2f(__builtin_amdgcn_perm(__builtin_amdgcn_perm(pa.w, pa.z, 0x0c0c0400u), __builtin_amdgcn_perm(pa.y, pa.x, 0x0c0c0400u), 0x05040100u));
    const float lb = u2f(__builtin_amdgcn_perm(__builtin_amdgcn_perm(pb.w, pb.z, 0x0c0c0400u), __builtin_amdgcn_perm(pb.y, pb.x, 0x0c0c0400u), 0x05040100u));
    const float Mw = x32_max(x16_max(fmaxf(la, lb)));
    const float wa = __builtin_amdgcn_exp2f(la - Mw), wb = __builtin_amdgcn_exp2f(lb - Mw);
    const float Ww = x32_sum(x16_sum(wa + wb));
    float a[4];
#pragma unroll
    for (int r = 0; r < 4; ++r) a[r] = fmaf(u2f(pa[r] & 0xFFFFFF00u), wa, u2f(pb[r] & 0xFFFFFF00u) * wb);
    const float v = tr32(tr16(a[0], a[1]), tr16(a[2], a[3]));
    *(LAS float*)(L + L_C + (((w >> 2) * 64 + (i16 >> 3) * 32 + g4 * 8 + (i16 & 7)) * 4 + (w & 3)) * 4) = v;
    if (lane == 0) *(LAS v2u*)(L + L_C + 2048 + w * 8) = v2u{f2u(Mw), f2u(Ww)};
  }
  WG_BARRIER();
  {
    const int b = (lane >> 3) & 1, k = lane & 7;
    const v4u m0 = *(LAS v4u*)(L + L_C + 2048 + b * 32), m1 = *(LAS v4u*)(L + L_C + 2048 + b * 32 + 16);
    const f32x4 av = *(LAS f32x4*)(L + L_C + (b * 64 + w * 8 + k) * 16);
    const float M = fmaxf(fmaxf(u2f(m0.x), u2f(m0.z)), fmaxf(u2f(m1.x), u2f(m1.z)));
    const float f0 = __builtin_amdgcn_exp2f(u2f(m0.x) - M), f1 = __builtin_amdgcn_exp2f(u2f(m0.z) - M);
    const float f2 = __builtin_amdgcn_exp2f(u2f(m1.x) - M), f3 = __builtin_amdgcn_exp2f(u2f(m1.z) - M);
    const float num = fmaf(f0, av.x, f1 * av.y) + fmaf(f2, av.z, f3 * av.w);
    const float den = fmaf(f0, u2f(m0.y), f1 * u2f(m0.w)) + fmaf(f2, u2f(m1.y), f3 * u2f(m1.w));
    const float ov = num * rcp(den);
    f32x4 s0 = {0.f, 0.f, 0.f, 0.f}, s1 = {0.f, 0.f, 0.f, 0.f};
#pragma unroll
    for (int kk = 0; kk < 8; ++kk) { s0 += wof[kk] * rdl(ov, kk); s1 += wof[kk] * rdl(ov, 8 + kk); }
    *(LAS f32x4*)(L + L_S + w * 2048 + lane * 16) = s0;
    *(LAS f32x4*)(L + L_S + w * 2048 + 1024 + lane * 16) = s1;
  }
  WG_BARRIER();
  if (w < 2 || (w < 4 && has_ffn)) {
    const int lb = (w < 2 ? L_S : L_F) + sfoff;
    f32x4 t[8];
#pragma unroll
    for (int ww = 0; ww < 8; ++ww) t[ww] = *(LAS f32x4*)(L + lb + ww * 2048);
    SB();
    const f32x4 s4 = ((t[0] + t[1]) + (t[2] + t[3])) + ((t[4] + t[5]) + (t[6] + t[7]));
    __builtin_amdgcn_raw_buffer_store_b128(clean4(s4), rs, s2st, 0, AUX_SC1);
  }

  if (reducer) {
    v4u a = __builtin_amdgcn_raw_buffer_load_b128(rs, roff, WS_S2, AUX_SC1);
    v4u c = {0u, 0u, 0u, 0u};
    if (((tid >> 3) & 7) != xl) c = __builtin_amdgcn_raw_buffer_load_b128(rs, roff, WS_F2, AUX_SC1);
    bool ha = full4(a), hc = full4(c);
#pragma nounroll
    for (unsigned spins = 0; spins < SPIN_CAP; ++spins) {
      asm volatile("" ::: "memory");
      if (__all(ha & hc)) break;
      if (!ha) a = __builtin_amdgcn_raw_buffer_load_b128(rs, roff, WS_S2, AUX_SC1);
      if (!hc) c = __builtin_amdgcn_raw_buffer_load_b128(rs, roff, WS_F2, AUX_SC1);
      ha = full4(a);
      hc = full4(c);
    }
    float vs = tr32(tr16(u2f(a.x), u2f(a.y)), tr16(u2f(a.z), u2f(a.w)));
    float vf = tr32(tr16(u2f(c.x), u2f(c.y)), tr16(u2f(c.z), u2f(c.w)));
    vs += dpp<XOR8>(vs);
    vf += dpp<XOR8>(vf);
    *(LAS float*)(L + L_R + ((lane >> 3) & 1) * 1024 + ((lane & 7) * 4 + (lane >> 4)) * 32 + w * 4) = (lane & 8) ? vs : vf;
    WG_BARRIER();
    if (w == 0) {
      const v4u a4 = __builtin_amdgcn_raw_buffer_load_b128(rs, WS_DG + lane * 16, 0, AUX_SC1);
      const v4u b4 = __builtin_amdgcn_raw_buffer_load_b128(rs, WS_DG + 1024 + lane * 16, 0, AUX_SC1);
      const unsigned now = (unsigned)__builtin_amdgcn_s_memrealtime();
      float amx = 0.f, amn = 1e9f, bmx = 0.f, bmn = 1e9f, bs = 0.f;
      for (int k = 0; k < 4; ++k) { const float da = (float)(now - a4[k]), db = (float)(now - b4[k]); amx = fmaxf(amx, da); amn = fminf(amn, da); bmx = fmaxf(bmx, db); bmn = fminf(bmn, db); bs += db; }
      for (int o = 32; o >= 1; o >>= 1) { amx = fmaxf(amx, __shfl_xor(amx, o)); amn = fminf(amn, __shfl_xor(amn, o)); bmx = fmaxf(bmx, __shfl_xor(bmx, o)); bmn = fminf(bmn, __shfl_xor(bmn, o)); bs += __shfl_xor(bs, o); }
      const float val = DIAG == 0 ? 0.f : DIAG == 1 ? amx - amn : DIAG == 2 ? amx : DIAG == 3 ? bs / 256.f - bmn : DIAG == 4 ? bmx - bmn : bmn;
      const unsigned until = now + (unsigned)(val * (DIAG == 2 ? 1.f : 4.f));
      while ((int)((unsigned)__builtin_amdgcn_s_memrealtime() - until) < 0) {}
    }
    if (tid < 32) {
      const f32x4 f0 = *(LAS f32x4*)(L + L_R + tid * 32), f1 = *(LAS f32x4*)(L + L_R + tid * 32 + 16);
      const f32x4 s0 = *(LAS f32x4*)(L + L_R + 1024 + tid * 32), s1 = *(LAS f32x4*)(L + L_R + 1024 + tid * 32 + 16);
      const f32x4 f4 = f0 + f1, s4 = s0 + s1;
      const float f = (f4.x + f4.y) + (f4.z + f4.w), sv = (s4.x + s4.y) + (s4.z + s4.w);
      const float r = fmaf(sv, silu(f), bflo(xo));
      p.out[oidx] = (unsigned short)(cvtpk(r, 0.f) & 0xFFFFu);
    }
    if (bid == NWG - 1 && tid == 0) {
      const v2u ne = {FL[6], FL[7]};
      __builtin_amdgcn_raw_buffer_store_b64(ne, rs, WS_E, 0, AUX_SC1);
    }
  }
}

}

extern "C" void kernel_launch(void* const* d_in, const int* in_sizes, int n_in, void* d_out, int out_size, void* d_ws,
                              size_t ws_size, hipStream_t stream) {
  Params p;
  p.x = (const unsigned short*)d_in[0];
  p.kc = (const unsigned short*)d_in[1];
  p.vc = (const unsigned short*)d_in[2];
  p.wq = (const unsigned short*)d_in[3];
  p.wo = (const unsigned short*)d_in[4];
  p.wg = (const unsigned short*)d_in[6];
  p.wu = (const unsigned short*)d_in[7];
  p.wd = (const unsigned short*)d_in[8];
  p.out = (unsigned short*)d_out;
  p.ws = (unsigned char*)d_ws;
  hipLaunchKernelGGL(fused_decode, dim3(NWG), dim3(NTHR), 0, stream, p);
}
```

```cpp
#include <hip/hip_runtime.h>
#include <hip/hip_bf16.h>
#include <stdint.h>

#define GAS __attribute__((address_space(1)))
#define LAS __attribute__((address_space(3)))
typedef unsigned v4u __attribute__((ext_vector_type(4)));
typedef unsigned v2u __attribute__((ext_vector_type(2)));
typedef float f32x4 __attribute__((ext_vector_type(4)));
typedef short bf16x8 __attribute__((ext_vector_type(8)));
typedef short bf16x4 __attribute__((ext_vector_type(4)));
typedef unsigned long long u64;

namespace {

constexpr int NWG = 256, NTHR = 576;
constexpr int D = 1024, HD = 4096, FF = 3584, SK = 32768;
constexpr int AUX_SC1 = 16;
constexpr unsigned SENT = 0xFFFFFFFFu;
constexpr unsigned SPIN_CAP = 1u << 22;

constexpr int RSG = 65536, RSP = 131072, RSS = 65536;
constexpr int WS_E = 0;
constexpr int WS_QP = 4096;
constexpr int WS_GU = WS_QP + 262144;
constexpr int WS_P1 = WS_GU + 8 * RSG;
constexpr int WS_S2 = WS_P1 + 8 * RSP;
constexpr int WS_F2 = WS_S2 + 8 * RSS;
constexpr int WS_ALL = WS_F2 + 8 * RSS;

constexpr int L_RED = 0;
constexpr int L_Q = 12288;
constexpr int L_ML = L_Q + 16 * 272;
constexpr int L_O = L_ML + 512;
constexpr int L_S = L_O;
constexpr int L_C = L_O + 32768;
constexpr int L_R = L_C + 2048 + 64;
constexpr int L_FL = L_R + 2048;
constexpr int L_WD = L_FL + 64;
constexpr int L_F = L_WD;
constexpr int L_BYTES = L_WD + 32768;

struct Params {
  const unsigned short* x;
  const unsigned short* wq;
  unsigned char* ws;
  const unsigned short* wg;
  const unsigned short* wu;
  const unsigned short* wd;
  const unsigned short* wo;
  const unsigned short* kc;
  const unsigned short* vc;
  unsigned short* out;
};

__device__ __forceinline__ float bflo(unsigned u) { return __builtin_bit_cast(float, u << 16); }
__device__ __forceinline__ float bfhi(unsigned u) { return __builtin_bit_cast(float, u & 0xFFFF0000u); }
__device__ __forceinline__ unsigned f2u(float f) { return __builtin_bit_cast(unsigned, f); }
__device__ __forceinline__ float u2f(unsigned u) { return __builtin_bit_cast(float, u); }
typedef __bf16 bf16p __attribute__((ext_vector_type(2)));
typedef float f32x2 __attribute__((ext_vector_type(2)));
__device__ __forceinline__ unsigned cvtpk(float lo, float hi) {
  return __builtin_bit_cast(unsigned, __builtin_convertvector(f32x2{lo, hi}, bf16p));
}
template <int CTRL> __device__ __forceinline__ float dpp(float x) {
  return __builtin_bit_cast(float, __builtin_amdgcn_mov_dpp(__builtin_bit_cast(int, x), CTRL, 0xf, 0xf, true));
}
constexpr int XOR1 = 0xB1, XOR2 = 0x4E, XOR7 = 0x141, XOR8 = 0x128;
__device__ __forceinline__ float x16_sum(float x) {
  auto s = __builtin_amdgcn_permlane16_swap(f2u(x), f2u(x), false, false);
  return u2f(s[0]) + u2f(s[1]);
}
__device__ __forceinline__ float x32_sum(float x) {
  auto s = __builtin_amdgcn_permlane32_swap(f2u(x), f2u(x), false, false);
  return u2f(s[0]) + u2f(s[1]);
}
__device__ __forceinline__ float x16_max(float x) {
  auto s = __builtin_amdgcn_permlane16_swap(f2u(x), f2u(x), false, false);
  return fmaxf(u2f(s[0]), u2f(s[1]));
}
__device__ __forceinline__ float x32_max(float x) {
  auto s = __builtin_amdgcn_permlane32_swap(f2u(x), f2u(x), false, false);
  return fmaxf(u2f(s[0]), u2f(s[1]));
}
__device__ __forceinline__ float row16_sum(float x) {
  x += dpp<XOR1>(x);
  x += dpp<XOR2>(x);
  x += dpp<XOR7>(x);
  x += dpp<XOR8>(x);
  return x;
}
__device__ __forceinline__ float wave_sum(float x) { return x32_sum(x16_sum(row16_sum(x))); }
__device__ __forceinline__ float tr16(float a, float b) {
  auto s = __builtin_amdgcn_permlane16_swap(f2u(a), f2u(b), false, false);
  return u2f(s[0]) + u2f(s[1]);
}
__device__ __forceinline__ float tr32(float a, float b) {
  auto s = __builtin_amdgcn_permlane32_swap(f2u(a), f2u(b), false, false);
  return u2f(s[0]) + u2f(s[1]);
}
__device__ __forceinline__ v4u ldnt(const GAS v4u* p) { return __builtin_nontemporal_load(p); }
__device__ __forceinline__ unsigned clean(float f) {
  const unsigned u = f2u(f);
  return u == SENT ? 0x7FC00000u : u;
}
__device__ __forceinline__ v4u clean4(f32x4 v) { return v4u{clean(v.x), clean(v.y), clean(v.z), clean(v.w)}; }
__device__ __forceinline__ bool full4(v4u v) { return v.x != SENT && v.y != SENT && v.z != SENT && v.w != SENT; }
__device__ __forceinline__ float rdl(float v, int lane) { return u2f(__builtin_amdgcn_readlane(f2u(v), lane)); }
__device__ __forceinline__ float rcp(float v) { return __builtin_amdgcn_rcpf(v); }
__device__ __forceinline__ float silu(float v) { return v * rcp(1.0f + __builtin_amdgcn_exp2f(v * -1.4426950408889634f)); }

#define SB() __builtin_amdgcn_sched_barrier(0)
#define WG_BARRIER()                                        \
  do {                                                      \
    asm volatile("s_waitcnt lgkmcnt(0)" ::: "memory");      \
    __builtin_amdgcn_s_barrier();                           \
    asm volatile("" ::: "memory");                          \
  } while (0)

__device__ __forceinline__ f32x4 gemv64(const v4u (&w)[4], const float (&x0)[4], const float (&x1)[4]) {
  float a0[8], a1[8];
#pragma unroll
  for (int c = 0; c < 8; ++c) { a0[c] = 0.f; a1[c] = 0.f; }
#pragma unroll
  for (int i = 0; i < 4; ++i) {
#pragma unroll
    for (int k = 0; k < 4; ++k) {
      const float lo = bflo(w[i][k]), hi = bfhi(w[i][k]);
      a0[2 * k] = fmaf(lo, x0[i], a0[2 * k]);
      a0[2 * k + 1] = fmaf(hi, x0[i], a0[2 * k + 1]);
      a1[2 * k] = fmaf(lo, x1[i], a1[2 * k]);
      a1[2 * k + 1] = fmaf(hi, x1[i], a1[2 * k + 1]);
    }
  }
  float t[8];
#pragma unroll
  for (int c = 0; c < 8; ++c) t[c] = tr16(a0[c], a1[c]);
  f32x4 r;
#pragma unroll
  for (int c = 0; c < 4; ++c) {
    float v = tr32(t[c], t[c + 4]);
    v += dpp<XOR8>(v);
    r[c] = v;
  }
  return r;
}

__device__ __forceinline__ void gemv256(const v2u (&w)[8], const float (&c0)[8], const float (&c1)[8], f32x4& s0, f32x4& s1) {
  s0 = f32x4{0.f, 0.f, 0.f, 0.f};
  s1 = f32x4{0.f, 0.f, 0.f, 0.f};
#pragma unroll
  for (int k = 0; k < 8; ++k) {
    const f32x4 wf = {bflo(w[k].x), bfhi(w[k].x), bflo(w[k].y), bfhi(w[k].y)};
#pragma unroll
    for (int c = 0; c < 4; ++c) {
      s0[c] = fmaf(wf[c], c0[k], s0[c]);
      s1[c] = fmaf(wf[c], c1[k], s1[c]);
    }
  }
}

__device__ __forceinline__ v2u granule(float v, unsigned tag_lo, unsigned tag_hi) {
  return v2u{(tag_lo << 24) | ((f2u(v) + 0x80u) >> 8), tag_hi};
}
__device__ __forceinline__ bool tag2(v4u g, unsigned tag_lo, unsigned tag_hi) {
  return (g.y == tag_hi) & ((g.x >> 24) == tag_lo) & (g.w == tag_hi) & ((g.z >> 24) == tag_lo);
}
#define TOUCH4(a) asm volatile("" : "+v"(a[0]), "+v"(a[1]), "+v"(a[2]), "+v"(a[3]))
constexpr int AUX_NT = 2;
#define LDK(kb_, off_) do { _Pragma("unroll") for (int j = 0; j < 4; ++j) kb_[j] = __builtin_amdgcn_raw_buffer_load_b128(rk, (klane + (off_)) + j * 64, 0, AUX_NT); SB(); } while (0)
#define LDV(vb_, off_) do { _Pragma("unroll") for (int j = 0; j < 4; ++j) vb_[j] = __builtin_amdgcn_raw_buffer_load_b128(rv, (vlane + (off_)) + j * 1024, 0, AUX_NT); SB(); } while (0)
#define LDK2(kb_, a_) do { _Pragma("unroll") for (int j = 0; j < 4; ++j) kb_[j] = __builtin_amdgcn_raw_buffer_load_b128(rk, (a_) + j * 64, 0, AUX_NT); SB(); } while (0)
#define LDV2(vb_, a_) do { _Pragma("unroll") for (int j = 0; j < 4; ++j) vb_[j] = __builtin_amdgcn_raw_buffer_load_b128(rv, (a_) + j * 1024, 0, AUX_NT); SB(); } while (0)

struct AttnState {
  float m, l;
  f32x4 o[8];
};
__device__ __forceinline__ v2u attn_qk(AttnState& st, const v4u (&kc)[4], const v4u (&qf)[4]) {
  constexpr float C2 = 0.088388347648318440f * 1.4426950408889634f;
  f32x4 s = {0.f, 0.f, 0.f, 0.f};
#pragma unroll
  for (int kk = 0; kk < 4; ++kk)
    s = __builtin_amdgcn_mfma_f32_16x16x32_bf16(__builtin_bit_cast(bf16x8, kc[kk]), __builtin_bit_cast(bf16x8, qf[kk]), s, 0, 0, 0);
#pragma unroll
  for (int r = 0; r < 4; ++r) s[r] = (s[r] + dpp<XOR8>(s[r])) * C2;
  float mx = fmaxf(fmaxf(s[0], s[1]), fmaxf(s[2], s[3]));
  mx = x32_max(x16_max(mx));
  const float mn = fmaxf(st.m, mx);
  const float alpha = __builtin_amdgcn_exp2f(st.m - mn);
  st.m = mn;
  const float p0 = __builtin_amdgcn_exp2f(s[0] - mn), p1 = __builtin_amdgcn_exp2f(s[1] - mn);
  const float p2 = __builtin_amdgcn_exp2f(s[2] - mn), p3 = __builtin_amdgcn_exp2f(s[3] - mn);
  st.l = fmaf(st.l, alpha, (p0 + p1) + (p2 + p3));
  const v2u pb = {cvtpk(p0, p1), cvtpk(p2, p3)};
#pragma unroll
  for (int e = 0; e < 8; ++e) st.o[e] *= alpha;
  SB();
  return pb;
}
__device__ __forceinline__ void attn_pv(AttnState& st, const v4u (&vc)[4], v2u pb) {
#pragma unroll
  for (int e = 0; e < 8; ++e) {
    const unsigned sel = (e & 1) ? 0x07060302u : 0x05040100u;
    const v2u a = {__builtin_amdgcn_perm(vc[1][e >> 1], vc[0][e >> 1], sel), __builtin_amdgcn_perm(vc[3][e >> 1], vc[2][e >> 1], sel)};
    st.o[e] = __builtin_amdgcn_mfma_f32_16x16x16bf16_1k(__builtin_bit_cast(bf16x4, a), __builtin_bit_cast(bf16x4, pb), st.o[e], 0, 0, 0);
  }
  SB();
}
__device__ __forceinline__ void attn_unit(AttnState& st, const v4u (&kc)[4], const v4u (&vc)[4], const v4u (&qf)[4]) {
  attn_pv(st, vc, attn_qk(st, kc, qf));
}

#define DRAIN() asm volatile("s_waitcnt vmcnt(0)" ::: "memory")
constexpr int MV = 3;
typedef float f32x2v __attribute__((ext_vector_type(2)));
__device__ __forceinline__ void lds_signal(LAS unsigned* c) {
  asm volatile("s_waitcnt lgkmcnt(0)" ::: "memory");
  if ((threadIdx.x & 63) == 0) __hip_atomic_fetch_add(c, 1u, __ATOMIC_RELAXED, __HIP_MEMORY_SCOPE_WORKGROUP);
}
__device__ __forceinline__ void lds_wait(LAS unsigned* c, unsigned want) {
#pragma nounroll
  for (unsigned spins = 0; spins < (SPIN_CAP << 3); ++spins) {
    asm volatile("" ::: "memory");
    if (__builtin_amdgcn_readfirstlane(*(volatile LAS unsigned*)c) >= want) break;
    __builtin_amdgcn_s_sleep(1);
  }
  asm volatile("" ::: "memory");
}

__global__ void __launch_bounds__(NTHR) fused_decode(Params p) {
  __shared__ __attribute__((aligned(16))) unsigned char smem[L_BYTES];
  LAS unsigned char* L = (LAS unsigned char*)smem;
  LAS unsigned* FL = (LAS unsigned*)(L + L_FL);
  const int tid = (int)threadIdx.x - 64, lane = threadIdx.x & 63;
  const int w = __builtin_amdgcn_readfirstlane(threadIdx.x >> 6) - 1;
  const int bid = blockIdx.x;
  const int grp = bid >> 5, xq = bid & 7;
  const int cg = (bid >> 3) & 3, rg = grp * 8 + xq;
  const int hkv = bid >> 6, bb = grp & 1;
  const int xl = (3 - (int)((size_t)p.wq >> 7)) & 7;
  const bool has_ffn = xq != xl;
  const int fi = has_ffn ? grp * 7 + (xq < xl ? xq : xq - 1) : 0;
  const bool reducer = rg >= 48;
  constexpr int OOB = 0x7FFF0000;
  const __amdgpu_buffer_rsrc_t rs = __builtin_amdgcn_make_buffer_rsrc((void*)p.ws, 0, WS_ALL, 0x00020000);
  const int p1own = (bid & 7) * RSP + WS_P1 + (bid >> 3) * 4096;
  const int s2own = (rg & 7) * RSS + (cg * 8 + (rg >> 3)) * 2048;
  const GAS v4u* xg = (const GAS v4u*)p.x;

  if (w < 0) {
    __builtin_amdgcn_s_setprio(3);
    const v4u xa0 = xg[lane * 2], xa1 = xg[lane * 2 + 1], xb0 = xg[128 + lane * 2], xb1 = xg[128 + lane * 2 + 1];
    const v2u ev = __builtin_amdgcn_raw_buffer_load_b64(rs, WS_E, 0, AUX_SC1);
    if (lane < 6) FL[lane] = 0u;
    float rr;
    {
      float va_[16], vb_[16];
#pragma unroll
      for (int k = 0; k < 4; ++k) {
        va_[2 * k] = bflo(xa0[k]); va_[2 * k + 1] = bfhi(xa0[k]); va_[8 + 2 * k] = bflo(xa1[k]); va_[8 + 2 * k + 1] = bfhi(xa1[k]);
        vb_[2 * k] = bflo(xb0[k]); vb_[2 * k + 1] = bfhi(xb0[k]); vb_[8 + 2 * k] = bflo(xb1[k]); vb_[8 + 2 * k + 1] = bfhi(xb1[k]);
      }
      float sa = 0.f, sb = 0.f;
#pragma unroll
      for (int k = 0; k < 16; ++k) { sa += va_[k]; sb += vb_[k]; }
      const float ma = wave_sum(sa) * (1.0f / D), mb = wave_sum(sb) * (1.0f / D);
      if (lane == 0) *(LAS f32x2v*)(L + L_FL + 32) = f32x2v{ma, mb};
      WG_BARRIER();
      float qa = 0.f, qb = 0.f;
#pragma unroll
      for (int k = 0; k < 16; ++k) { const float da = va_[k] - ma, db = vb_[k] - mb; qa = fmaf(da, da, qa); qb = fmaf(db, db, qb); }
      const float ta = wave_sum(qa), tb = wave_sum(qb);
      rr = 1.0f / sqrtf((lane < 32 ? ta : tb) * (1.0f / D) + 1e-5f);
    }
    const u64 e1 = ((((u64)__builtin_amdgcn_readfirstlane(ev.y)) << 32) | (u64)(unsigned)__builtin_amdgcn_readfirstlane(ev.x)) + 1ull;
    const unsigned tag_hi = (unsigned)(e1 >> 8), tag_lo = (unsigned)(e1 & 0xFFull);
    const unsigned tq = (unsigned)e1 & 0xFFFFFFu;
    if (lane == 0) { FL[6] = (unsigned)e1; FL[7] = (unsigned)(e1 >> 32); }
    lds_wait(FL + 0, 8);
    {
      f32x2v a = {0.f, 0.f};
#pragma unroll
      for (int ww = 0; ww < 8; ++ww) a += *(LAS f32x2v*)(L + L_RED + ww * 512 + lane * 8);
      const unsigned v0 = (f2u(a.x * rr) + 0x800u) >> 12, v1 = (f2u(a.y * rr) + 0x800u) >> 12;
      __builtin_amdgcn_raw_buffer_store_b64(v2u{v0 | (v1 << 20), (v1 >> 12) | (tq << 8)}, rs,
          WS_QP + ((hkv * 2 + (lane >> 5)) * 4 + cg) * 4096 + (rg & 15) * 256 + (lane & 31) * 8, 0, AUX_SC1);
    }
    DRAIN();
    {
      const int qoff = WS_QP + (hkv * 2 + bb) * 16384 + lane * 16;
      v4u qg[16];
#pragma nounroll
      for (unsigned spins = 0; spins < SPIN_CAP; ++spins) {
        asm volatile("" ::: "memory");
        bool ok = true;
#pragma unroll
        for (int j = 0; j < 16; ++j) qg[j] = __builtin_amdgcn_raw_buffer_load_b128(rs, qoff + (j & 3) * 1024, (j >> 2) * 4096, AUX_SC1);
#pragma unroll
        for (int j = 0; j < 16; ++j) ok &= ((qg[j].y >> 8) == tq) & ((qg[j].w >> 8) == tq);
        if (__all(ok)) break;
        __builtin_amdgcn_s_sleep(4);
      }
#pragma unroll
      for (int j = 0; j < 4; ++j) {
        f32x4 q = {0.f, 0.f, 0.f, 0.f};
#pragma unroll
        for (int ks = 0; ks < 4; ++ks) {
          const v4u g = qg[ks * 4 + j];
          q += f32x4{u2f(g.x << 12), u2f((g.y << 24) | ((g.x >> 20) << 12)), u2f(g.z << 12), u2f((g.w << 24) | ((g.z >> 20) << 12))};
        }
        const v2u hi = {cvtpk(q.x, q.y), cvtpk(q.z, q.w)};
        const v2u lo = {cvtpk(q.x - bflo(hi.x), q.y - bfhi(hi.x)), cvtpk(q.z - bflo(hi.y), q.w - bfhi(hi.y))};
        *(LAS v2u*)(L + L_Q + (2 * j + (lane >> 5)) * 272 + (lane & 31) * 8) = hi;
        *(LAS v2u*)(L + L_Q + (2 * j + 8 + (lane >> 5)) * 272 + (lane & 31) * 8) = lo;
      }
      lds_signal(FL + 3);
    }
    const int coff = WS_QP + (((lane >> 4) * 2) * 4 + cg) * 4096 + (lane & 15) * 256;
    if (reducer) {
#pragma nounroll
      for (unsigned spins = 0; spins < SPIN_CAP; ++spins) {
        asm volatile("" ::: "memory");
        const v4u ck = __builtin_amdgcn_raw_buffer_load_b128(rs, coff, 0, AUX_SC1);
        if (__all(((ck.y >> 8) == tq) & ((ck.w >> 8) == tq))) break;
        __builtin_amdgcn_s_sleep(4);
      }
    }
    if (has_ffn) {
      lds_wait(FL + 1, 8);
      v4u gr[2];
#pragma unroll
      for (int m = 0; m < 2; ++m) {
        f32x2v a = {0.f, 0.f};
#pragma unroll
        for (int ww = 0; ww < 8; ++ww) a += *(LAS f32x2v*)(L + L_RED + 4096 + m * 4096 + ww * 512 + lane * 8);
        const v2u g0 = granule(a.x * rr, tag_lo, tag_hi), g1 = granule(a.y * rr, tag_lo, tag_hi);
        gr[m] = v4u{g0.x, g0.y, g1.x, g1.y};
      }
      lds_wait(FL + 2, 4);
#pragma unroll
      for (int m = 0; m < 2; ++m) __builtin_amdgcn_raw_buffer_store_b128(gr[m], rs, (cg * 2 + m) * RSG + WS_GU + rg * 1024 + lane * 16, 0, AUX_SC1);
    }
    WG_BARRIER();
    return;
  }

  {
    const v4u s4 = {SENT, SENT, SENT, SENT};
    const int off = w < 4 ? p1own + w * 1024 : (w < 6 ? WS_S2 - 4096 : WS_F2 - 6144) + s2own + w * 1024;
    __builtin_amdgcn_raw_buffer_store_b128(s4, rs, off + lane * 16, 0, AUX_SC1);
    SB();
  }
  const int r8 = lane >> 3, c8 = lane & 7;
  const int krow = cg * 256 + w * 32 + r8 * 4;
  const v2u xr0 = *(const GAS v2u*)(p.x + krow), xr1 = *(const GAS v2u*)(p.x + D + krow);
  v4u wqr[4], wgr[4], wur[4], wdr[4];
  v2u wor[8];
#pragma unroll
  for (int i = 0; i < 4; ++i) wqr[i] = ldnt((const GAS v4u*)(p.wq + (size_t)(krow + i) * HD + rg * 64 + c8 * 8));
  SB();
  const int fbytes = has_ffn ? D * FF * 2 : 0;
  const __amdgpu_buffer_rsrc_t rwg = __builtin_amdgcn_make_buffer_rsrc((void*)p.wg, 0, fbytes, 0x00020000);
  const __amdgpu_buffer_rsrc_t rwu = __builtin_amdgcn_make_buffer_rsrc((void*)p.wu, 0, fbytes, 0x00020000);
  const __amdgpu_buffer_rsrc_t rwd = __builtin_amdgcn_make_buffer_rsrc((void*)p.wd, 0, fbytes, 0x00020000);
  const __amdgpu_buffer_rsrc_t rwo = __builtin_amdgcn_make_buffer_rsrc((void*)p.wo, 0, HD * D * 2, 0x00020000);
  const int guoff = (krow * FF + fi * 64 + c8 * 8) * 2;
  const int tile = ((w * 8 + (lane >> 5)) * D + cg * 256 + (lane & 31) * 8) * 2;
#define LDWG() do { _Pragma("unroll") for (int i = 0; i < 4; ++i) wgr[i] = __builtin_amdgcn_raw_buffer_load_b128(rwg, guoff + i * FF * 2, 0, AUX_NT); SB(); } while (0)
#define LDWU() do { _Pragma("unroll") for (int i = 0; i < 4; ++i) wur[i] = __builtin_amdgcn_raw_buffer_load_b128(rwu, guoff + i * FF * 2, 0, AUX_NT); SB(); } while (0)
#define LDWD() do { _Pragma("unroll") for (int i = 0; i < 4; ++i) wdr[i] = __builtin_amdgcn_raw_buffer_load_b128(rwd, tile + i * 4 * D, fi * 64 * D * 2, AUX_NT); SB(); } while (0)
#define LDWO() do { _Pragma("unroll") for (int k = 0; k < 8; ++k) wor[k] = __builtin_amdgcn_raw_buffer_load_b64(rwo, lane * 8, ((rg * 64 + w * 8 + k) * D + cg * 256) * 2, AUX_NT); SB(); } while (0)
#define USEQ() do { const f32x4 r = gemv64(wqr, x0, x1); if ((lane & 8) == 0) *(LAS f32x4*)(L + L_RED + w * 512 + redoff) = r; lds_signal(FL + 0); } while (0)
#define USEG() do { const f32x4 r = gemv64(wgr, x0, x1); if ((lane & 8) == 0) *(LAS f32x4*)(L + L_RED + 4096 + w * 512 + redoff) = r; SB(); } while (0)
#define USEU() do { const f32x4 r = gemv64(wur, x0, x1); if ((lane & 8) == 0) *(LAS f32x4*)(L + L_RED + 8192 + w * 512 + redoff) = r; lds_signal(FL + 1); } while (0)
#define USED() do { _Pragma("unroll") for (int i = 0; i < 4; ++i) *(LAS v4u*)(L + L_WD + (w * 4 + i) * 1024 + lane * 16) = wdr[i]; SB(); } while (0)
#define USEO() asm volatile("" : "+v"(wor[0]), "+v"(wor[1]), "+v"(wor[2]), "+v"(wor[3]), "+v"(wor[4]), "+v"(wor[5]), "+v"(wor[6]), "+v"(wor[7]))
  LDWO();
  const int i16 = lane & 15, g4 = lane >> 4;
  const __amdgpu_buffer_rsrc_t rk = __builtin_amdgcn_make_buffer_rsrc((void*)p.kc, 0, 8 * SK * 256, 0x00020000);
  const __amdgpu_buffer_rsrc_t rv = __builtin_amdgcn_make_buffer_rsrc((void*)p.vc, 0, 8 * SK * 256, 0x00020000);
  const int klane = (4 * (i16 & 3) + (i16 >> 2)) * 256 + g4 * 16, vlane = lane * 16;
  const int rsl = (((3 - (int)((size_t)p.wg >> 7)) & 7) + grp) & 7;
  const int xs = rsl < xl ? rsl : rsl + 1;
  const int nunit = rsl == 7 ? 64 : (!has_ffn ? 64 + MV : (xq == xs ? 64 - MV : 64));
  const int obase = ((bb * 4 + hkv) * 2048 + cg * 512) * 4096;
  const int ubase = obase + xq * 262144, xbase = obase + xs * 262144 - MV * 4096;
#define UOFF(t_) (ubase + (t_) * 4096)
#define UOFFX(t_) (((t_) < 64 ? ubase : xbase) + (t_) * 4096)
  v4u ka[4], va[4], kb[4], vb[4], kc[4], vc[4];
  WG_BARRIER();
  float x0[4], x1[4];
  {
    const f32x2v mm = *(LAS f32x2v*)(L + L_FL + 32);
    x0[0] = bflo(xr0.x) - mm.x; x0[1] = bfhi(xr0.x) - mm.x; x0[2] = bflo(xr0.y) - mm.x; x0[3] = bfhi(xr0.y) - mm.x;
    x1[0] = bflo(xr1.x) - mm.y; x1[1] = bfhi(xr1.x) - mm.y; x1[2] = bflo(xr1.y) - mm.y; x1[3] = bfhi(xr1.y) - mm.y;
  }
  const int redoff = ((g4 & 1) * 64 + c8 * 8 + (g4 >> 1) * 4) * 4;

  USEQ(); LDWG();
  USEO(); LDWD();
  USEG(); LDWU();
  USED(); LDK(ka, UOFF(w));
  USEU(); LDV(va, UOFF(w));
  TOUCH4(ka);
  LDK(kb, UOFF(w + 8));
  TOUCH4(va);
  LDV(vb, UOFF(w + 8));
  TOUCH4(kb);
  LDK(kc, UOFF(w + 16));
  TOUCH4(vb);
  LDV(vc, UOFF(w + 16));
  lds_wait(FL + 3, 1);
  v4u qf[4];
#pragma unroll
  for (int kk = 0; kk < 4; ++kk) qf[kk] = *(LAS v4u*)(L + L_Q + i16 * 272 + kk * 64 + g4 * 16);

  AttnState st;
  st.m = -INFINITY;
  st.l = 0.f;
#pragma unroll
  for (int e = 0; e < 8; ++e) st.o[e] = f32x4{0.f, 0.f, 0.f, 0.f};
  attn_unit(st, ka, va, qf);
  attn_unit(st, kb, vb, qf);
  int t = w + 24;
  for (;;) {
    const int o0 = t < nunit ? UOFFX(t) : OOB;
    const int ak0 = klane + o0, av0 = vlane + o0;
    LDK2(kb, ak0);
    LDV2(vb, av0);
    asm volatile("" ::"v"(ak0), "v"(av0));
    attn_unit(st, kc, vc, qf);
    if (t >= nunit) break;
    const int o1 = t + 8 < nunit ? UOFFX(t + 8) : OOB;
    const int ak1 = klane + o1, av1 = vlane + o1;
    LDK2(kc, ak1);
    LDV2(vc, av1);
    asm volatile("" ::"v"(ak1), "v"(av1));
    attn_unit(st, kb, vb, qf);
    if (t + 8 >= nunit) break;
    t += 16;
  }
  lds_signal(FL + 2);
  const int sfoff = ((tid >> 2) & 1) * 1024 + ((tid & 127) >> 3) * 64 + (tid & 3) * 16;
  const int oidx = ((tid >> 4) & 1) * D + cg * 256 + (rg & 15) * 16 + (tid & 15);
  unsigned xo = 0;
  if (w == 0) xo = *(const GAS unsigned short*)(p.x + oidx);

  {
    const float l = x32_sum(x16_sum(st.l));
    if (i16 < 8) {
#pragma unroll
      for (int e = 0; e < 8; ++e) *(LAS f32x4*)(L + L_O + (w * 256 + (i16 * 4 + g4) * 8 + (e ^ i16)) * 16) = st.o[e];
      if (g4 == 0) *(LAS v2u*)(L + L_ML + (i16 * 8 + w) * 8) = v2u{f2u(st.m), f2u(l)};
    }
  }
  int poff = g4 * RSP + WS_P1 + (hkv * 8 + w) * 4096 + (rg & 15) * 256 + i16 * 16;
  int goff = (lane >> 3) * RSG + WS_GU + rg * 1024 + ((lane >> 2) & 1) * 512 + (w * 8 + (lane & 3) * 2) * 8;
  int roff = ((tid >> 3) & 7) * RSS + (cg * 8 + (tid >> 6)) * 2048 + (rg & 15) * 128 + (tid & 7) * 16;
  int p1st = p1own + tid * 16;
  int s2st = (w < 2 ? WS_S2 : WS_F2) + s2own + (tid & 127) * 16;
  __builtin_amdgcn_s_waitcnt(0x0F70);
  asm volatile("" : "+v"(poff), "+v"(goff), "+v"(roff), "+v"(p1st), "+v"(s2st));
  WG_BARRIER();
  if (w < 4) {
    const int j = tid >> 5;
    v4u ml[4];
    f32x4 ov[8];
#pragma unroll
    for (int k = 0; k < 4; ++k) ml[k] = *(LAS v4u*)(L + L_ML + j * 64 + k * 16);
#pragma unroll
    for (int ww = 0; ww < 8; ++ww) ov[ww] = *(LAS f32x4*)(L + L_O + (ww * 256 + (tid ^ j)) * 16);
    SB();
    float M = fmaxf(u2f(ml[0].x), u2f(ml[0].z));
#pragma unroll
    for (int k = 1; k < 4; ++k) M = fmaxf(M, fmaxf(u2f(ml[k].x), u2f(ml[k].z)));
    float Ls = 0.f;
    f32x4 acc = {0.f, 0.f, 0.f, 0.f};
#pragma unroll
    for (int ww = 0; ww < 8; ++ww) {
      const float wt = __builtin_amdgcn_exp2f(u2f(ml[ww >> 1][(ww & 1) * 2]) - M);
      Ls = fmaf(wt, u2f(ml[ww >> 1][(ww & 1) * 2 + 1]), Ls);
      acc += ov[ww] * wt;
    }
    acc *= rcp(Ls);
    const unsigned lse = f2u(M + __builtin_amdgcn_logf(Ls));
    v4u pk;
#pragma unroll
    for (int r = 0; r < 4; ++r) {
      const unsigned t = ((f2u(acc[r]) + 0x80u) & 0xFFFFFF00u) | ((lse >> (8 * r)) & 0xFFu);
      pk[r] = t == SENT ? 0x7FC000FFu : t;
    }
    __builtin_amdgcn_raw_buffer_store_b128(pk, rs, p1st, 0, AUX_SC1);
  }
  SB();

  f32x4 wof[8];
  {
    v4u gu = __builtin_amdgcn_raw_buffer_load_b128(rs, goff, 0, AUX_SC1);
    v4u pa = __builtin_amdgcn_raw_buffer_load_b128(rs, poff, 0, AUX_SC1);
    v4u pb = __builtin_amdgcn_raw_buffer_load_b128(rs, poff, 4 * RSP, AUX_SC1);
    SB();
    if (has_ffn) {
      const unsigned e_lo = FL[6], e_hi = FL[7];
      const unsigned tag_hi = (e_lo >> 8) | (e_hi << 24), tag_lo = e_lo & 0xFFu;
      v2u wt[8];
#pragma unroll
      for (int k = 0; k < 8; ++k) wt[k] = *(LAS v2u*)(L + L_WD + (w * 8 + k) * 512 + lane * 8);
      if (!__all(tag2(gu, tag_lo, tag_hi))) {
#pragma nounroll
        for (unsigned spins = 0; spins < SPIN_CAP; ++spins) {
          asm volatile("" ::: "memory");
          __builtin_amdgcn_s_sleep(1);
          gu = __builtin_amdgcn_raw_buffer_load_b128(rs, goff, 0, AUX_SC1);
          if (__all(tag2(gu, tag_lo, tag_hi))) break;
        }
      }
      const float ga = x32_sum(x16_sum(u2f(gu.x << 8))), gb = x32_sum(x16_sum(u2f(gu.z << 8)));
      const float hx = silu(ga) * dpp<XOR8>(ga), hy = silu(gb) * dpp<XOR8>(gb);
      float h0[8], h1[8];
#pragma unroll
      for (int pr = 0; pr < 4; ++pr) {
        h0[2 * pr] = rdl(hx, pr); h0[2 * pr + 1] = rdl(hy, pr);
        h1[2 * pr] = rdl(hx, 4 + pr); h1[2 * pr + 1] = rdl(hy, 4 + pr);
      }
      f32x4 s0, s1;
      gemv256(wt, h0, h1, s0, s1);
      *(LAS f32x4*)(L + L_F + w * 4096 + lane * 16) = s0;
      *(LAS f32x4*)(L + L_F + w * 4096 + 1024 + lane * 16) = s1;
    }
#pragma unroll
    for (int k = 0; k < 8; ++k) {
      const v2u t = wor[k];
      wof[k] = f32x4{bflo(t.x), bfhi(t.x), bflo(t.y), bfhi(t.y)};
      asm volatile("" : "+v"(wof[k]));
    }
    bool ha = full4(pa), hb = full4(pb);
#pragma nounroll
    for (unsigned spins = 0; spins < SPIN_CAP; ++spins) {
      asm volatile("" ::: "memory");
      if (__all(ha & hb)) break;
      if (!ha) pa = __builtin_amdgcn_raw_buffer_load_b128(rs, poff, 0, AUX_SC1);
      if (!hb) pb = __builtin_amdgcn_raw_buffer_load_b128(rs, poff, 4 * RSP, AUX_SC1);
      ha = full4(pa);
      hb = full4(pb);
    }
    const float la = u2f(__builtin_amdgcn_perm(__builtin_amdgcn_perm(pa.w, pa.z, 0x0c0c0400u), __builtin_amdgcn_perm(pa.y, pa.x, 0x0c0c0400u), 0x05040100u));
    const float lb = u2f(__builtin_amdgcn_perm(__builtin_amdgcn_perm(pb.w, pb.z, 0x0c0c0400u), __builtin_amdgcn_perm(pb.y, pb.x, 0x0c0c0400u), 0x05040100u));
    const float Mw = x32_max(x16_max(fmaxf(la, lb)));
    const float wa = __builtin_amdgcn_exp2f(la - Mw), wb = __builtin_amdgcn_exp2f(lb - Mw);
    const float Ww = x32_sum(x16_sum(wa + wb));
    float a[4];
#pragma unroll
    for (int r = 0; r < 4; ++r) a[r] = fmaf(u2f(pa[r] & 0xFFFFFF00u), wa, u2f(pb[r] & 0xFFFFFF00u) * wb);
    const float v = tr32(tr16(a[0], a[1]), tr16(a[2], a[3]));
    *(LAS float*)(L + L_C + (((w >> 2) * 64 + (i16 >> 3) * 32 + g4 * 8 + (i16 & 7)) * 4 + (w & 3)) * 4) = v;
    if (lane == 0) *(LAS v2u*)(L + L_C + 2048 + w * 8) = v2u{f2u(Mw), f2u(Ww)};
  }
  WG_BARRIER();
  {
    const int b = (lane >> 3) & 1, k = lane & 7;
    const v4u m0 = *(LAS v4u*)(L + L_C + 2048 + b * 32), m1 = *(LAS v4u*)(L + L_C + 2048 + b * 32 + 16);
    const f32x4 av = *(LAS f32x4*)(L + L_C + (b * 64 + w * 8 + k) * 16);
    const float M = fmaxf(fmaxf(u2f(m0.x), u2f(m0.z)), fmaxf(u2f(m1.x), u2f(m1.z)));
    const float f0 = __builtin_amdgcn_exp2f(u2f(m0.x) - M), f1 = __builtin_amdgcn_exp2f(u2f(m0.z) - M);
    const float f2 = __builtin_amdgcn_exp2f(u2f(m1.x) - M), f3 = __builtin_amdgcn_exp2f(u2f(m1.z) - M);
    const float num = fmaf(f0, av.x, f1 * av.y) + fmaf(f2, av.z, f3 * av.w);
    const float den = fmaf(f0, u2f(m0.y), f1 * u2f(m0.w)) + fmaf(f2, u2f(m1.y), f3 * u2f(m1.w));
    const float ov = num * rcp(den);
    f32x4 s0 = {0.f, 0.f, 0.f, 0.f}, s1 = {0.f, 0.f, 0.f, 0.f};
#pragma unroll
    for (int kk = 0; kk < 8; ++kk) { s0 += wof[kk] * rdl(ov, kk); s1 += wof[kk] * rdl(ov, 8 + kk); }
    *(LAS f32x4*)(L + L_S + w * 4096 + lane * 16) = s0;
    *(LAS f32x4*)(L + L_S + w * 4096 + 1024 + lane * 16) = s1;
  }
  WG_BARRIER();
  if (w < 2 || (w < 4 && has_ffn)) {
    const int lb = (w < 2 ? L_S : L_F) + sfoff;
    f32x4 t[8];
#pragma unroll
    for (int ww = 0; ww < 8; ++ww) t[ww] = *(LAS f32x4*)(L + lb + ww * 4096);
    SB();
    const f32x4 s4 = ((t[0] + t[1]) + (t[2] + t[3])) + ((t[4] + t[5]) + (t[6] + t[7]));
    __builtin_amdgcn_raw_buffer_store_b128(clean4(s4), rs, s2st, 0, AUX_SC1);
  }

  if (reducer) {
    v4u a = __builtin_amdgcn_raw_buffer_load_b128(rs, roff, WS_S2, AUX_SC1);
    v4u c = {0u, 0u, 0u, 0u};
    if (((tid >> 3) & 7) != xl) c = __builtin_amdgcn_raw_buffer_load_b128(rs, roff, WS_F2, AUX_SC1);
    bool ha = full4(a), hc = full4(c);
#pragma nounroll
    for (unsigned spins = 0; spins < SPIN_CAP; ++spins) {
      asm volatile("" ::: "memory");
      if (__all(ha & hc)) break;
      if (!ha) a = __builtin_amdgcn_raw_buffer_load_b128(rs, roff, WS_S2, AUX_SC1);
      if (!hc) c = __builtin_amdgcn_raw_buffer_load_b128(rs, roff, WS_F2, AUX_SC1);
      ha = full4(a);
      hc = full4(c);
    }
    float vs = tr32(tr16(u2f(a.x), u2f(a.y)), tr16(u2f(a.z), u2f(a.w)));
    float vf = tr32(tr16(u2f(c.x), u2f(c.y)), tr16(u2f(c.z), u2f(c.w)));
    vs += dpp<XOR8>(vs);
    vf += dpp<XOR8>(vf);
    *(LAS float*)(L + L_R + ((lane >> 3) & 1) * 1024 + ((lane & 7) * 4 + (lane >> 4)) * 32 + w * 4) = (lane & 8) ? vs : vf;
    WG_BARRIER();
    if (tid < 32) {
      const f32x4 f0 = *(LAS f32x4*)(L + L_R + tid * 32), f1 = *(LAS f32x4*)(L + L_R + tid * 32 + 16);
      const f32x4 s0 = *(LAS f32x4*)(L + L_R + 1024 + tid * 32), s1 = *(LAS f32x4*)(L + L_R + 1024 + tid * 32 + 16);
      const f32x4 f4 = f0 + f1, s4 = s0 + s1;
      const float f = (f4.x + f4.y) + (f4.z + f4.w), sv = (s4.x + s4.y) + (s4.z + s4.w);
      const float r = fmaf(sv, silu(f), bflo(xo));
      p.out[oidx] = (unsigned short)(cvtpk(r, 0.f) & 0xFFFFu);
    }
    if (bid == NWG - 1 && tid == 0) {
      const v2u ne = {FL[6], FL[7]};
      __builtin_amdgcn_raw_buffer_store_b64(ne, rs, WS_E, 0, AUX_SC1);
    }
  }
}

}

extern "C" void kernel_launch(void* const* d_in, const int* in_sizes, int n_in, void* d_out, int out_size, void* d_ws,
                              size_t ws_size, hipStream_t stream) {
  Params p;
  p.x = (const unsigned short*)d_in[0];
  p.kc = (const unsigned short*)d_in[1];
  p.vc = (const unsigned short*)d_in[2];
  p.wq = (const unsigned short*)d_in[3];
  p.wo = (const unsigned short*)d_in[4];
  p.wg = (const unsigned short*)d_in[6];
  p.wu = (const unsigned short*)d_in[7];
  p.wd = (const unsigned short*)d_in[8];
  p.out = (unsigned short*)d_out;
  p.ws = (unsigned char*)d_ws;
  hipLaunchKernelGGL(fused_decode, dim3(NWG), dim3(NTHR), 0, stream, p);
}
```

```cpp
#define SKEW 0
#define KAUX 0
#include <hip/hip_runtime.h>
#include <hip/hip_bf16.h>
#include <stdint.h>

#define GAS __attribute__((address_space(1)))
#define LAS __attribute__((address_space(3)))
typedef unsigned v4u __attribute__((ext_vector_type(4)));
typedef unsigned v2u __attribute__((ext_vector_type(2)));
typedef float f32x4 __attribute__((ext_vector_type(4)));
typedef short bf16x8 __attribute__((ext_vector_type(8)));
typedef short bf16x4 __attribute__((ext_vector_type(4)));
typedef unsigned long long u64;

namespace {

constexpr int NWG = 256, NTHR = 576;
constexpr int D = 1024, HD = 4096, FF = 3584, SK = 32768;
constexpr int AUX_SC1 = 16;
constexpr unsigned SENT = 0xFFFFFFFFu;
constexpr unsigned SPIN_CAP = 1u << 22;

constexpr int RSG = 65536, RSP = 131072, RSS = 65536;
constexpr int WS_E = 0;
constexpr int WS_QP = 4096;
constexpr int WS_GU = WS_QP + 262144;
constexpr int WS_P1 = WS_GU + 8 * RSG;
constexpr int WS_S2 = WS_P1 + 8 * RSP;
constexpr int WS_F2 = WS_S2 + 8 * RSS;
constexpr int WS_ALL = WS_F2 + 8 * RSS;

constexpr int L_RED = 0;
constexpr int L_Q = 12288;
constexpr int L_F = 0;
constexpr int L_ML = L_Q + 16 * 272;
constexpr int L_O = L_ML + 512;
constexpr int L_S = L_O;
constexpr int L_C = L_O + 32768;
constexpr int L_R = L_C + 2048 + 64;
constexpr int L_FL = L_R + 2048;
constexpr int L_BYTES = L_FL + 64;
static_assert(L_F + 16384 <= L_ML, "lds");

struct Params {
  const unsigned short* x;
  const unsigned short* wq;
  unsigned char* ws;
  const unsigned short* wg;
  const unsigned short* wu;
  const unsigned short* wd;
  const unsigned short* wo;
  const unsigned short* kc;
  const unsigned short* vc;
  unsigned short* out;
};

__device__ __forceinline__ float bflo(unsigned u) { return __builtin_bit_cast(float, u << 16); }
__device__ __forceinline__ float bfhi(unsigned u) { return __builtin_bit_cast(float, u & 0xFFFF0000u); }
__device__ __forceinline__ unsigned f2u(float f) { return __builtin_bit_cast(unsigned, f); }
__device__ __forceinline__ float u2f(unsigned u) { return __builtin_bit_cast(float, u); }
typedef __bf16 bf16p __attribute__((ext_vector_type(2)));
typedef float f32x2 __attribute__((ext_vector_type(2)));
__device__ __forceinline__ unsigned cvtpk(float lo, float hi) {
  return __builtin_bit_cast(unsigned, __builtin_convertvector(f32x2{lo, hi}, bf16p));
}
template <int CTRL> __device__ __forceinline__ float dpp(float x) {
  return __builtin_bit_cast(float, __builtin_amdgcn_mov_dpp(__builtin_bit_cast(int, x), CTRL, 0xf, 0xf, true));
}
constexpr int XOR1 = 0xB1, XOR2 = 0x4E, XOR7 = 0x141, XOR8 = 0x128;
__device__ __forceinline__ float x16_sum(float x) {
  auto s = __builtin_amdgcn_permlane16_swap(f2u(x), f2u(x), false, false);
  return u2f(s[0]) + u2f(s[1]);
}
__device__ __forceinline__ float x32_sum(float x) {
  auto s = __builtin_amdgcn_permlane32_swap(f2u(x), f2u(x), false, false);
  return u2f(s[0]) + u2f(s[1]);
}
__device__ __forceinline__ float x16_max(float x) {
  auto s = __builtin_amdgcn_permlane16_swap(f2u(x), f2u(x), false, false);
  return fmaxf(u2f(s[0]), u2f(s[1]));
}
__device__ __forceinline__ float x32_max(float x) {
  auto s = __builtin_amdgcn_permlane32_swap(f2u(x), f2u(x), false, false);
  return fmaxf(u2f(s[0]), u2f(s[1]));
}
__device__ __forceinline__ float row16_sum(float x) {
  x += dpp<XOR1>(x);
  x += dpp<XOR2>(x);
  x += dpp<XOR7>(x);
  x += dpp<XOR8>(x);
  return x;
}
__device__ __forceinline__ float wave_sum(float x) { return x32_sum(x16_sum(row16_sum(x))); }
__device__ __forceinline__ float tr16(float a, float b) {
  auto s = __builtin_amdgcn_permlane16_swap(f2u(a), f2u(b), false, false);
  return u2f(s[0]) + u2f(s[1]);
}
__device__ __forceinline__ float tr32(float a, float b) {
  auto s = __builtin_amdgcn_permlane32_swap(f2u(a), f2u(b), false, false);
  return u2f(s[0]) + u2f(s[1]);
}
__device__ __forceinline__ v4u ldnt(const GAS v4u* p) { return __builtin_nontemporal_load(p); }
__device__ __forceinline__ unsigned clean(float f) {
  const unsigned u = f2u(f);
  return u == SENT ? 0x7FC00000u : u;
}
__device__ __forceinline__ v4u clean4(f32x4 v) { return v4u{clean(v.x), clean(v.y), clean(v.z), clean(v.w)}; }
__device__ __forceinline__ bool full4(v4u v) { return v.x != SENT && v.y != SENT && v.z != SENT && v.w != SENT; }
__device__ __forceinline__ float rdl(float v, int lane) { return u2f(__builtin_amdgcn_readlane(f2u(v), lane)); }
__device__ __forceinline__ float rcp(float v) { return __builtin_amdgcn_rcpf(v); }
__device__ __forceinline__ float silu(float v) { return v * rcp(1.0f + __builtin_amdgcn_exp2f(v * -1.4426950408889634f)); }

#define SB() __builtin_amdgcn_sched_barrier(0)
#define WG_BARRIER()                                        \
  do {                                                      \
    asm volatile("s_waitcnt lgkmcnt(0)" ::: "memory");      \
    __builtin_amdgcn_s_barrier();                           \
    asm volatile("" ::: "memory");                          \
  } while (0)

__device__ __forceinline__ f32x4 gemv64(const v4u (&w)[4], const float (&x0)[4], const float (&x1)[4]) {
  float a0[8], a1[8];
#pragma unroll
  for (int c = 0; c < 8; ++c) { a0[c] = 0.f; a1[c] = 0.f; }
#pragma unroll
  for (int i = 0; i < 4; ++i) {
#pragma unroll
    for (int k = 0; k < 4; ++k) {
      const float lo = bflo(w[i][k]), hi = bfhi(w[i][k]);
      a0[2 * k] = fmaf(lo, x0[i], a0[2 * k]);
      a0[2 * k + 1] = fmaf(hi, x0[i], a0[2 * k + 1]);
      a1[2 * k] = fmaf(lo, x1[i], a1[2 * k]);
      a1[2 * k + 1] = fmaf(hi, x1[i], a1[2 * k + 1]);
    }
  }
  float t[8];
#pragma unroll
  for (int c = 0; c < 8; ++c) t[c] = tr16(a0[c], a1[c]);
  f32x4 r;
#pragma unroll
  for (int c = 0; c < 4; ++c) {
    float v = tr32(t[c], t[c + 4]);
    v += dpp<XOR8>(v);
    r[c] = v;
  }
  return r;
}

__device__ __forceinline__ void gemv256(const v2u (&w)[8], const float (&c0)[8], const float (&c1)[8], f32x4& s0, f32x4& s1) {
  s0 = f32x4{0.f, 0.f, 0.f, 0.f};
  s1 = f32x4{0.f, 0.f, 0.f, 0.f};
#pragma unroll
  for (int k = 0; k < 8; ++k) {
    const f32x4 wf = {bflo(w[k].x), bfhi(w[k].x), bflo(w[k].y), bfhi(w[k].y)};
#pragma unroll
    for (int c = 0; c < 4; ++c) {
      s0[c] = fmaf(wf[c], c0[k], s0[c]);
      s1[c] = fmaf(wf[c], c1[k], s1[c]);
    }
  }
}

__device__ __forceinline__ v2u granule(float v, unsigned tag_lo, unsigned tag_hi) {
  return v2u{(tag_lo << 24) | ((f2u(v) + 0x80u) >> 8), tag_hi};
}
__device__ __forceinline__ bool tag2(v4u g, unsigned tag_lo, unsigned tag_hi) {
  return (g.y == tag_hi) & ((g.x >> 24) == tag_lo) & (g.w == tag_hi) & ((g.z >> 24) == tag_lo);
}
#define TOUCH4(a) asm volatile("" : "+v"(a[0]), "+v"(a[1]), "+v"(a[2]), "+v"(a[3]))
constexpr int AUX_NT = 2;
#define LDK(kb_, off_) do { _Pragma("unroll") for (int j = 0; j < 4; ++j) kb_[j] = __builtin_amdgcn_raw_buffer_load_b128(rk, (klane + (off_)) + j * 64, 0, KAUX); SB(); } while (0)
#define LDV(vb_, off_) do { _Pragma("unroll") for (int j = 0; j < 4; ++j) vb_[j] = __builtin_amdgcn_raw_buffer_load_b128(rv, (vlane + (off_)) + j * 1024, 0, AUX_NT); SB(); } while (0)
#define LDK2(kb_, a_) do { _Pragma("unroll") for (int j = 0; j < 4; ++j) kb_[j] = __builtin_amdgcn_raw_buffer_load_b128(rk, (a_) + j * 64, 0, KAUX); SB(); } while (0)
#define LDV2(vb_, a_) do { _Pragma("unroll") for (int j = 0; j < 4; ++j) vb_[j] = __builtin_amdgcn_raw_buffer_load_b128(rv, (a_) + j * 1024, 0, AUX_NT); SB(); } while (0)

struct AttnState {
  float m, l;
  f32x4 o[8];
};
__device__ __forceinline__ v2u attn_qk(AttnState& st, const v4u (&kc)[4], const v4u (&qf)[4]) {
  constexpr float C2 = 0.088388347648318440f * 1.4426950408889634f;
  f32x4 s = {0.f, 0.f, 0.f, 0.f};
#pragma unroll
  for (int kk = 0; kk < 4; ++kk)
    s = __builtin_amdgcn_mfma_f32_16x16x32_bf16(__builtin_bit_cast(bf16x8, kc[kk]), __builtin_bit_cast(bf16x8, qf[kk]), s, 0, 0, 0);
#pragma unroll
  for (int r = 0; r < 4; ++r) s[r] = (s[r] + dpp<XOR8>(s[r])) * C2;
  float mx = fmaxf(fmaxf(s[0], s[1]), fmaxf(s[2], s[3]));
  mx = x32_max(x16_max(mx));
  const float mn = fmaxf(st.m, mx);
  const float alpha = __builtin_amdgcn_exp2f(st.m - mn);
  st.m = mn;
  const float p0 = __builtin_amdgcn_exp2f(s[0] - mn), p1 = __builtin_amdgcn_exp2f(s[1] - mn);
  const float p2 = __builtin_amdgcn_exp2f(s[2] - mn), p3 = __builtin_amdgcn_exp2f(s[3] - mn);
  st.l = fmaf(st.l, alpha, (p0 + p1) + (p2 + p3));
  const v2u pb = {cvtpk(p0, p1), cvtpk(p2, p3)};
#pragma unroll
  for (int e = 0; e < 8; ++e) st.o[e] *= alpha;
  SB();
  return pb;
}
__device__ __forceinline__ void attn_pv(AttnState& st, const v4u (&vc)[4], v2u pb) {
#pragma unroll
  for (int e = 0; e < 8; ++e) {
    const unsigned sel = (e & 1) ? 0x07060302u : 0x05040100u;
    const v2u a = {__builtin_amdgcn_perm(vc[1][e >> 1], vc[0][e >> 1], sel), __builtin_amdgcn_perm(vc[3][e >> 1], vc[2][e >> 1], sel)};
    st.o[e] = __builtin_amdgcn_mfma_f32_16x16x16bf16_1k(__builtin_bit_cast(bf16x4, a), __builtin_bit_cast(bf16x4, pb), st.o[e], 0, 0, 0);
  }
  SB();
}
__device__ __forceinline__ void attn_unit(AttnState& st, const v4u (&kc)[4], const v4u (&vc)[4], const v4u (&qf)[4]) {
  attn_pv(st, vc, attn_qk(st, kc, qf));
}

#define DRAIN() asm volatile("s_waitcnt vmcnt(0)" ::: "memory")
constexpr int MV = 3;
typedef float f32x2v __attribute__((ext_vector_type(2)));
__device__ __forceinline__ void lds_signal(LAS unsigned* c) {
  asm volatile("s_waitcnt lgkmcnt(0)" ::: "memory");
  if ((threadIdx.x & 63) == 0) __hip_atomic_fetch_add(c, 1u, __ATOMIC_RELAXED, __HIP_MEMORY_SCOPE_WORKGROUP);
}
__device__ __forceinline__ void lds_wait(LAS unsigned* c, unsigned want) {
#pragma nounroll
  for (unsigned spins = 0; spins < (SPIN_CAP << 3); ++spins) {
    asm volatile("" ::: "memory");
    if (__builtin_amdgcn_readfirstlane(*(volatile LAS unsigned*)c) >= want) break;
    __builtin_amdgcn_s_sleep(1);
  }
  asm volatile("" ::: "memory");
}

__global__ void __launch_bounds__(NTHR) fused_decode(Params p) {
  __shared__ __attribute__((aligned(16))) unsigned char smem[L_BYTES];
  LAS unsigned char* L = (LAS unsigned char*)smem;
  LAS unsigned* FL = (LAS unsigned*)(L + L_FL);
  const int tid = (int)threadIdx.x - 64, lane = threadIdx.x & 63;
  const int w = __builtin_amdgcn_readfirstlane(threadIdx.x >> 6) - 1;
  const int bid = blockIdx.x;
  const int grp = bid >> 5, xq = bid & 7;
  const int cg = (bid >> 3) & 3, rg = grp * 8 + xq;
  const int hkv = bid >> 6, bb = grp & 1;
  const int xl = (3 - (int)((size_t)p.wq >> 7)) & 7;
  const bool has_ffn = xq != xl;
  const int fi = has_ffn ? grp * 7 + (xq < xl ? xq : xq - 1) : 0;
  const bool reducer = rg >= 48;
  constexpr int OOB = 0x7FFF0000;
  const __amdgpu_buffer_rsrc_t rs = __builtin_amdgcn_make_buffer_rsrc((void*)p.ws, 0, WS_ALL, 0x00020000);
  const int p1own = (bid & 7) * RSP + WS_P1 + (bid >> 3) * 4096;
  const int s2own = (rg & 7) * RSS + (cg * 8 + (rg >> 3)) * 2048;
  const GAS v4u* xg = (const GAS v4u*)p.x;

  if (w < 0) {
    __builtin_amdgcn_s_setprio(3);
    const v4u xa0 = xg[lane * 2], xa1 = xg[lane * 2 + 1], xb0 = xg[128 + lane * 2], xb1 = xg[128 + lane * 2 + 1];
    const v2u ev = __builtin_amdgcn_raw_buffer_load_b64(rs, WS_E, 0, AUX_SC1);
    if (lane < 6) FL[lane] = 0u;
    float rr;
    {
      float va_[16], vb_[16];
#pragma unroll
      for (int k = 0; k < 4; ++k) {
        va_[2 * k] = bflo(xa0[k]); va_[2 * k + 1] = bfhi(xa0[k]); va_[8 + 2 * k] = bflo(xa1[k]); va_[8 + 2 * k + 1] = bfhi(xa1[k]);
        vb_[2 * k] = bflo(xb0[k]); vb_[2 * k + 1] = bfhi(xb0[k]); vb_[8 + 2 * k] = bflo(xb1[k]); vb_[8 + 2 * k + 1] = bfhi(xb1[k]);
      }
      float sa = 0.f, sb = 0.f;
#pragma unroll
      for (int k = 0; k < 16; ++k) { sa += va_[k]; sb += vb_[k]; }
      const float ma = wave_sum(sa) * (1.0f / D), mb = wave_sum(sb) * (1.0f / D);
      if (lane == 0) *(LAS f32x2v*)(L + L_FL + 32) = f32x2v{ma, mb};
      WG_BARRIER();
      float qa = 0.f, qb = 0.f;
#pragma unroll
      for (int k = 0; k < 16; ++k) { const float da = va_[k] - ma, db = vb_[k] - mb; qa = fmaf(da, da, qa); qb = fmaf(db, db, qb); }
      const float ta = wave_sum(qa), tb = wave_sum(qb);
      rr = 1.0f / sqrtf((lane < 32 ? ta : tb) * (1.0f / D) + 1e-5f);
    }
    const u64 e1 = ((((u64)__builtin_amdgcn_readfirstlane(ev.y)) << 32) | (u64)(unsigned)__builtin_amdgcn_readfirstlane(ev.x)) + 1ull;
    const unsigned tag_hi = (unsigned)(e1 >> 8), tag_lo = (unsigned)(e1 & 0xFFull);
    const unsigned tq = (unsigned)e1 & 0xFFFFFFu;
    if (lane == 0) { FL[6] = (unsigned)e1; FL[7] = (unsigned)(e1 >> 32); }
    lds_wait(FL + 0, 8);
    {
      f32x2v a = {0.f, 0.f};
#pragma unroll
      for (int ww = 0; ww < 8; ++ww) a += *(LAS f32x2v*)(L + L_RED + ww * 512 + lane * 8);
      const unsigned v0 = (f2u(a.x * rr) + 0x800u) >> 12, v1 = (f2u(a.y * rr) + 0x800u) >> 12;
      __builtin_amdgcn_raw_buffer_store_b64(v2u{v0 | (v1 << 20), (v1 >> 12) | (tq << 8)}, rs,
          WS_QP + ((hkv * 2 + (lane >> 5)) * 4 + cg) * 4096 + (rg & 15) * 256 + (lane & 31) * 8, 0, AUX_SC1);
    }
    DRAIN();
    {
      const int qoff = WS_QP + (hkv * 2 + bb) * 16384 + lane * 16;
      v4u qg[16];
#pragma nounroll
      for (unsigned spins = 0; spins < SPIN_CAP; ++spins) {
        asm volatile("" ::: "memory");
        bool ok = true;
#pragma unroll
        for (int j = 0; j < 16; ++j) qg[j] = __builtin_amdgcn_raw_buffer_load_b128(rs, qoff + (j & 3) * 1024, (j >> 2) * 4096, AUX_SC1);
#pragma unroll
        for (int j = 0; j < 16; ++j) ok &= ((qg[j].y >> 8) == tq) & ((qg[j].w >> 8) == tq);
        if (__all(ok)) break;
        __builtin_amdgcn_s_sleep(4);
      }
#pragma unroll
      for (int j = 0; j < 4; ++j) {
        f32x4 q = {0.f, 0.f, 0.f, 0.f};
#pragma unroll
        for (int ks = 0; ks < 4; ++ks) {
          const v4u g = qg[ks * 4 + j];
          q += f32x4{u2f(g.x << 12), u2f((g.y << 24) | ((g.x >> 20) << 12)), u2f(g.z << 12), u2f((g.w << 24) | ((g.z >> 20) << 12))};
        }
        const v2u hi = {cvtpk(q.x, q.y), cvtpk(q.z, q.w)};
        const v2u lo = {cvtpk(q.x - bflo(hi.x), q.y - bfhi(hi.x)), cvtpk(q.z - bflo(hi.y), q.w - bfhi(hi.y))};
        *(LAS v2u*)(L + L_Q + (2 * j + (lane >> 5)) * 272 + (lane & 31) * 8) = hi;
        *(LAS v2u*)(L + L_Q + (2 * j + 8 + (lane >> 5)) * 272 + (lane & 31) * 8) = lo;
      }
      lds_signal(FL + 3);
    }
    const int coff = WS_QP + (((lane >> 4) * 2) * 4 + cg) * 4096 + (lane & 15) * 256;
    if (reducer) {
#pragma nounroll
      for (unsigned spins = 0; spins < SPIN_CAP; ++spins) {
        asm volatile("" ::: "memory");
        const v4u ck = __builtin_amdgcn_raw_buffer_load_b128(rs, coff, 0, AUX_SC1);
        if (__all(((ck.y >> 8) == tq) & ((ck.w >> 8) == tq))) break;
        __builtin_amdgcn_s_sleep(4);
      }
    }
    if (has_ffn) {
      lds_wait(FL + 1, 8);
      v4u gr[2];
#pragma unroll
      for (int m = 0; m < 2; ++m) {
        f32x2v a = {0.f, 0.f};
#pragma unroll
        for (int ww = 0; ww < 8; ++ww) a += *(LAS f32x2v*)(L + L_RED + 4096 + m * 4096 + ww * 512 + lane * 8);
        const v2u g0 = granule(a.x * rr, tag_lo, tag_hi), g1 = granule(a.y * rr, tag_lo, tag_hi);
        gr[m] = v4u{g0.x, g0.y, g1.x, g1.y};
      }
      lds_wait(FL + 2, 4);
#pragma unroll
      for (int m = 0; m < 2; ++m) __builtin_amdgcn_raw_buffer_store_b128(gr[m], rs, (cg * 2 + m) * RSG + WS_GU + rg * 1024 + lane * 16, 0, AUX_SC1);
    }
    WG_BARRIER();
    return;
  }

  {
    const v4u s4 = {SENT, SENT, SENT, SENT};
    const int off = w < 4 ? p1own + w * 1024 : (w < 6 ? WS_S2 - 4096 : WS_F2 - 6144) + s2own + w * 1024;
    __builtin_amdgcn_raw_buffer_store_b128(s4, rs, off + lane * 16, 0, AUX_SC1);
    SB();
  }
  const int r8 = lane >> 3, c8 = lane & 7;
  const int krow = cg * 256 + w * 32 + r8 * 4;
  const v2u xr0 = *(const GAS v2u*)(p.x + krow), xr1 = *(const GAS v2u*)(p.x + D + krow);
  v4u wqr[4], wgr[4], wur[4];
#pragma unroll
  for (int i = 0; i < 4; ++i) wqr[i] = ldnt((const GAS v4u*)(p.wq + (size_t)(krow + i) * HD + rg * 64 + c8 * 8));
  SB();
  const int fbytes = has_ffn ? D * FF * 2 : 0;
  const __amdgpu_buffer_rsrc_t rwg = __builtin_amdgcn_make_buffer_rsrc((void*)p.wg, 0, fbytes, 0x00020000);
  const __amdgpu_buffer_rsrc_t rwu = __builtin_amdgcn_make_buffer_rsrc((void*)p.wu, 0, fbytes, 0x00020000);
  const __amdgpu_buffer_rsrc_t rwd = __builtin_amdgcn_make_buffer_rsrc((void*)p.wd, 0, fbytes, 0x00020000);
  const __amdgpu_buffer_rsrc_t rwo = __builtin_amdgcn_make_buffer_rsrc((void*)p.wo, 0, HD * D * 2, 0x00020000);
  const int guoff = (krow * FF + fi * 64 + c8 * 8) * 2;
#define LDWG() do { _Pragma("unroll") for (int i = 0; i < 4; ++i) wgr[i] = __builtin_amdgcn_raw_buffer_load_b128(rwg, guoff + i * FF * 2, 0, AUX_NT); SB(); } while (0)
#define LDWU() do { _Pragma("unroll") for (int i = 0; i < 4; ++i) wur[i] = __builtin_amdgcn_raw_buffer_load_b128(rwu, guoff + i * FF * 2, 0, AUX_NT); SB(); } while (0)
#define USEQ() do { const f32x4 r = gemv64(wqr, x0, x1); if ((lane & 8) == 0) *(LAS f32x4*)(L + L_RED + w * 512 + redoff) = r; \
    asm volatile("s_waitcnt vmcnt(4)" ::: "memory"); lds_signal(FL + 0); } while (0)
#define USEG() do { const f32x4 r = gemv64(wgr, x0, x1); if ((lane & 8) == 0) *(LAS f32x4*)(L + L_RED + 4096 + w * 512 + redoff) = r; SB(); } while (0)
#define USEU() do { const f32x4 r = gemv64(wur, x0, x1); if ((lane & 8) == 0) *(LAS f32x4*)(L + L_RED + 8192 + w * 512 + redoff) = r; lds_signal(FL + 1); } while (0)
  LDWG();
  const int i16 = lane & 15, g4 = lane >> 4;
  const __amdgpu_buffer_rsrc_t rk = __builtin_amdgcn_make_buffer_rsrc((void*)p.kc, 0, 8 * SK * 256, 0x00020000);
  const __amdgpu_buffer_rsrc_t rv = __builtin_amdgcn_make_buffer_rsrc((void*)p.vc, 0, 8 * SK * 256, 0x00020000);
  const int klane = (4 * (i16 & 3) + (i16 >> 2)) * 256 + g4 * 16, vlane = lane * 16;
  const int rsl = (((3 - (int)((size_t)p.wg >> 7)) & 7) + grp) & 7;
  const int xs = rsl < xl ? rsl : rsl + 1;
  const int nunit = rsl == 7 ? 64 : (!has_ffn ? 64 + MV : (xq == xs ? 64 - MV : 64));
  const int obase = ((bb * 4 + hkv) * 2048 + cg * 512) * 4096;
  const int ubase = obase + xq * 262144, xbase = obase + xs * 262144 - MV * 4096;
#define UOFF(t_) (ubase + (t_) * 4096)
#define UOFFX(t_) (((t_) < 64 ? ubase : xbase) + (t_) * 4096)
  v4u ka[4], va[4], kb[4], vb[4], kc[4], vc[4];
  WG_BARRIER();
  float x0[4], x1[4];
  {
    const f32x2v mm = *(LAS f32x2v*)(L + L_FL + 32);
    x0[0] = bflo(xr0.x) - mm.x; x0[1] = bfhi(xr0.x) - mm.x; x0[2] = bflo(xr0.y) - mm.x; x0[3] = bfhi(xr0.y) - mm.x;
    x1[0] = bflo(xr1.x) - mm.y; x1[1] = bfhi(xr1.x) - mm.y; x1[2] = bflo(xr1.y) - mm.y; x1[3] = bfhi(xr1.y) - mm.y;
  }
  const int redoff = ((g4 & 1) * 64 + c8 * 8 + (g4 >> 1) * 4) * 4;

  USEQ(); LDWU();
  USEG(); LDK(ka, UOFF(w));
  USEU(); LDV(va, UOFF(w));
  TOUCH4(ka);
  LDK(kb, UOFF(w + 8));
  TOUCH4(va);
  LDV(vb, UOFF(w + 8));
  TOUCH4(kb);
  LDK(kc, UOFF(w + 16));
  TOUCH4(vb);
  LDV(vc, UOFF(w + 16));
  lds_wait(FL + 3, 1);
  v4u qf[4];
#pragma unroll
  for (int kk = 0; kk < 4; ++kk) qf[kk] = *(LAS v4u*)(L + L_Q + i16 * 272 + kk * 64 + g4 * 16);

  AttnState st;
  st.m = -INFINITY;
  st.l = 0.f;
#pragma unroll
  for (int e = 0; e < 8; ++e) st.o[e] = f32x4{0.f, 0.f, 0.f, 0.f};
  attn_unit(st, ka, va, qf);
  attn_unit(st, kb, vb, qf);
  int t = w + 24;
  for (;;) {
    const int o0 = t < nunit ? UOFFX(t) : OOB;
    const int ak0 = klane + o0, av0 = vlane + o0;
    LDK2(kb, ak0);
    LDV2(vb, av0);
    asm volatile("" ::"v"(ak0), "v"(av0));
    attn_unit(st, kc, vc, qf);
    if (t >= nunit) break;
    const int o1 = t + 8 < nunit ? UOFFX(t + 8) : OOB;
    const int ak1 = klane + o1, av1 = vlane + o1;
    LDK2(kc, ak1);
    LDV2(vc, av1);
    asm volatile("" ::"v"(ak1), "v"(av1));
    attn_unit(st, kb, vb, qf);
    if (t + 8 >= nunit) break;
    t += 16;
  }
  lds_signal(FL + 2);
  const int sfoff = ((tid >> 2) & 1) * 1024 + ((tid & 127) >> 3) * 64 + (tid & 3) * 16;
  const int oidx = ((tid >> 4) & 1) * D + cg * 256 + (rg & 15) * 16 + (tid & 15);
  unsigned xo = 0;
  if (w == 0) xo = *(const GAS unsigned short*)(p.x + oidx);

  {
    const float l = x32_sum(x16_sum(st.l));
    if (i16 < 8) {
#pragma unroll
      for (int e = 0; e < 8; ++e) *(LAS f32x4*)(L + L_O + (w * 256 + (i16 * 4 + g4) * 8 + (e ^ i16)) * 16) = st.o[e];
      if (g4 == 0) *(LAS v2u*)(L + L_ML + (i16 * 8 + w) * 8) = v2u{f2u(st.m), f2u(l)};
    }
  }
  int poff = g4 * RSP + WS_P1 + (hkv * 8 + w) * 4096 + (rg & 15) * 256 + i16 * 16;
  int goff = (lane >> 3) * RSG + WS_GU + rg * 1024 + ((lane >> 2) & 1) * 512 + (w * 8 + (lane & 3) * 2) * 8;
  int roff = ((tid >> 3) & 7) * RSS + (cg * 8 + (tid >> 6)) * 2048 + (rg & 15) * 128 + (tid & 7) * 16;
  int p1st = p1own + tid * 16;
  int s2st = (w < 2 ? WS_S2 : WS_F2) + s2own + (tid & 127) * 16;
  __builtin_amdgcn_s_waitcnt(0x0F70);
  asm volatile("" : "+v"(poff), "+v"(goff), "+v"(roff), "+v"(p1st), "+v"(s2st));
  WG_BARRIER();
  if (w < 4) {
    const int j = tid >> 5;
    v4u ml[4];
    f32x4 ov[8];
#pragma unroll
    for (int k = 0; k < 4; ++k) ml[k] = *(LAS v4u*)(L + L_ML + j * 64 + k * 16);
#pragma unroll
    for (int ww = 0; ww < 8; ++ww) ov[ww] = *(LAS f32x4*)(L + L_O + (ww * 256 + (tid ^ j)) * 16);
    SB();
    float M = fmaxf(u2f(ml[0].x), u2f(ml[0].z));
#pragma unroll
    for (int k = 1; k < 4; ++k) M = fmaxf(M, fmaxf(u2f(ml[k].x), u2f(ml[k].z)));
    float Ls = 0.f;
    f32x4 acc = {0.f, 0.f, 0.f, 0.f};
#pragma unroll
    for (int ww = 0; ww < 8; ++ww) {
      const float wt = __builtin_amdgcn_exp2f(u2f(ml[ww >> 1][(ww & 1) * 2]) - M);
      Ls = fmaf(wt, u2f(ml[ww >> 1][(ww & 1) * 2 + 1]), Ls);
      acc += ov[ww] * wt;
    }
    acc *= rcp(Ls);
    const unsigned lse = f2u(M + __builtin_amdgcn_logf(Ls));
    v4u pk;
#pragma unroll
    for (int r = 0; r < 4; ++r) {
      const unsigned t = ((f2u(acc[r]) + 0x80u) & 0xFFFFFF00u) | ((lse >> (8 * r)) & 0xFFu);
      pk[r] = t == SENT ? 0x7FC000FFu : t;
    }
    __builtin_amdgcn_raw_buffer_store_b128(pk, rs, p1st, 0, AUX_SC1);
  }
  SB();

  f32x4 wof[8];
  {
    v4u gu = __builtin_amdgcn_raw_buffer_load_b128(rs, goff, 0, AUX_SC1);
    SB();
    v2u wt[8], wor[8];
#pragma unroll
    for (int k = 0; k < 8; ++k) wt[k] = __builtin_amdgcn_raw_buffer_load_b64(rwd, lane * 8, ((fi * 64 + w * 8 + k) * D + cg * 256) * 2, AUX_NT);
    SB();
#pragma unroll
    for (int k = 0; k < 8; ++k) wor[k] = __builtin_amdgcn_raw_buffer_load_b64(rwo, lane * 8, ((rg * 64 + w * 8 + k) * D + cg * 256) * 2, AUX_NT);
    SB();
    v4u pa = __builtin_amdgcn_raw_buffer_load_b128(rs, poff, 0, AUX_SC1);
    v4u pb = __builtin_amdgcn_raw_buffer_load_b128(rs, poff, 4 * RSP, AUX_SC1);
    SB();
    if (has_ffn) {
      const unsigned e_lo = FL[6], e_hi = FL[7];
      const unsigned tag_hi = (e_lo >> 8) | (e_hi << 24), tag_lo = e_lo & 0xFFu;
      if (!__all(tag2(gu, tag_lo, tag_hi))) {
#pragma nounroll
        for (unsigned spins = 0; spins < SPIN_CAP; ++spins) {
          asm volatile("" ::: "memory");
          __builtin_amdgcn_s_sleep(1);
          gu = __builtin_amdgcn_raw_buffer_load_b128(rs, goff, 0, AUX_SC1);
          if (__all(tag2(gu, tag_lo, tag_hi))) break;
        }
      }
      const float ga = x32_sum(x16_sum(u2f(gu.x << 8))), gb = x32_sum(x16_sum(u2f(gu.z << 8)));
      const float hx = silu(ga) * dpp<XOR8>(ga), hy = silu(gb) * dpp<XOR8>(gb);
      float h0[8], h1[8];
#pragma unroll
      for (int pr = 0; pr < 4; ++pr) {
        h0[2 * pr] = rdl(hx, pr); h0[2 * pr + 1] = rdl(hy, pr);
        h1[2 * pr] = rdl(hx, 4 + pr); h1[2 * pr + 1] = rdl(hy, 4 + pr);
      }
      f32x4 s0, s1;
      gemv256(wt, h0, h1, s0, s1);
      *(LAS f32x4*)(L + L_F + w * 2048 + lane * 16) = s0;
      *(LAS f32x4*)(L + L_F + w * 2048 + 1024 + lane * 16) = s1;
    }
#pragma unroll
    for (int k = 0; k < 8; ++k) {
      const v2u t = wor[k];
      wof[k] = f32x4{bflo(t.x), bfhi(t.x), bflo(t.y), bfhi(t.y)};
      asm volatile("" : "+v"(wof[k]));
    }
    bool ha = full4(pa), hb = full4(pb);
#pragma nounroll
    for (unsigned spins = 0; spins < SPIN_CAP; ++spins) {
      asm volatile("" ::: "memory");
      if (__all(ha & hb)) break;
      if (!ha) pa = __builtin_amdgcn_raw_buffer_load_b128(rs, poff, 0, AUX_SC1);
      if (!hb) pb = __builtin_amdgcn_raw_buffer_load_b128(rs, poff, 4 * RSP, AUX_SC1);
      ha = full4(pa);
      hb = full4(pb);
    }
    const float la = u2f(__builtin_amdgcn_perm(__builtin_amdgcn_perm(pa.w, pa.z, 0x0c0c0400u), __builtin_amdgcn_perm(pa.y, pa.x, 0x0c0c0400u), 0x05040100u));
    const float lb = u2f(__builtin_amdgcn_perm(__builtin_amdgcn_perm(pb.w, pb.z, 0x0c0c0400u), __builtin_amdgcn_perm(pb.y, pb.x, 0x0c0c0400u), 0x05040100u));
    const float Mw = x32_max(x16_max(fmaxf(la, lb)));
    const float wa = __builtin_amdgcn_exp2f(la - Mw), wb = __builtin_amdgcn_exp2f(lb - Mw);
    const float Ww = x32_sum(x16_sum(wa + wb));
    float a[4];
#pragma unroll
    for (int r = 0; r < 4; ++r) a[r] = fmaf(u2f(pa[r] & 0xFFFFFF00u), wa, u2f(pb[r] & 0xFFFFFF00u) * wb);
    const float v = tr32(tr16(a[0], a[1]), tr16(a[2], a[3]));
    *(LAS float*)(L + L_C + (((w >> 2) * 64 + (i16 >> 3) * 32 + g4 * 8 + (i16 & 7)) * 4 + (w & 3)) * 4) = v;
    if (lane == 0) *(LAS v2u*)(L + L_C + 2048 + w * 8) = v2u{f2u(Mw), f2u(Ww)};
  }
  WG_BARRIER();
  {
    const int b = (lane >> 3) & 1, k = lane & 7;
    const v4u m0 = *(LAS v4u*)(L + L_C + 2048 + b * 32), m1 = *(LAS v4u*)(L + L_C + 2048 + b * 32 + 16);
    const f32x4 av = *(LAS f32x4*)(L + L_C + (b * 64 + w * 8 + k) * 16);
    const float M = fmaxf(fmaxf(u2f(m0.x), u2f(m0.z)), fmaxf(u2f(m1.x), u2f(m1.z)));
    const float f0 = __builtin_amdgcn_exp2f(u2f(m0.x) - M), f1 = __builtin_amdgcn_exp2f(u2f(m0.z) - M);
    const float f2 = __builtin_amdgcn_exp2f(u2f(m1.x) - M), f3 = __builtin_amdgcn_exp2f(u2f(m1.z) - M);
    const float num = fmaf(f0, av.x, f1 * av.y) + fmaf(f2, av.z, f3 * av.w);
    const float den = fmaf(f0, u2f(m0.y), f1 * u2f(m0.w)) + fmaf(f2, u2f(m1.y), f3 * u2f(m1.w));
    const float ov = num * rcp(den);
    f32x4 s0 = {0.f, 0.f, 0.f, 0.f}, s1 = {0.f, 0.f, 0.f, 0.f};
#pragma unroll
    for (int kk = 0; kk < 8; ++kk) { s0 += wof[kk] * rdl(ov, kk); s1 += wof[kk] * rdl(ov, 8 + kk); }
    *(LAS f32x4*)(L + L_S + w * 2048 + lane * 16) = s0;
    *(LAS f32x4*)(L + L_S + w * 2048 + 1024 + lane * 16) = s1;
  }
  WG_BARRIER();
  if (w < 2 || (w < 4 && has_ffn)) {
    const int lb = (w < 2 ? L_S : L_F) + sfoff;
    f32x4 t[8];
#pragma unroll
    for (int ww = 0; ww < 8; ++ww) t[ww] = *(LAS f32x4*)(L + lb + ww * 2048);
    SB();
    const f32x4 s4 = ((t[0] + t[1]) + (t[2] + t[3])) + ((t[4] + t[5]) + (t[6] + t[7]));
    __builtin_amdgcn_raw_buffer_store_b128(clean4(s4), rs, s2st, 0, AUX_SC1);
  }

  if (reducer) {
    v4u a = __builtin_amdgcn_raw_buffer_load_b128(rs, roff, WS_S2, AUX_SC1);
    v4u c = {0u, 0u, 0u, 0u};
    if (((tid >> 3) & 7) != xl) c = __builtin_amdgcn_raw_buffer_load_b128(rs, roff, WS_F2, AUX_SC1);
    bool ha = full4(a), hc = full4(c);
#pragma nounroll
    for (unsigned spins = 0; spins < SPIN_CAP; ++spins) {
      asm volatile("" ::: "memory");
      if (__all(ha & hc)) break;
      if (!ha) a = __builtin_amdgcn_raw_buffer_load_b128(rs, roff, WS_S2, AUX_SC1);
      if (!hc) c = __builtin_amdgcn_raw_buffer_load_b128(rs, roff, WS_F2, AUX_SC1);
      ha = full4(a);
      hc = full4(c);
    }
    float vs = tr32(tr16(u2f(a.x), u2f(a.y)), tr16(u2f(a.z), u2f(a.w)));
    float vf = tr32(tr16(u2f(c.x), u2f(c.y)), tr16(u2f(c.z), u2f(c.w)));
    vs += dpp<XOR8>(vs);
    vf += dpp<XOR8>(vf);
    *(LAS float*)(L + L_R + ((lane >> 3) & 1) * 1024 + ((lane & 7) * 4 + (lane >> 4)) * 32 + w * 4) = (lane & 8) ? vs : vf;
    WG_BARRIER();
    if (tid < 32) {
      const f32x4 f0 = *(LAS f32x4*)(L + L_R + tid * 32), f1 = *(LAS f32x4*)(L + L_R + tid * 32 + 16);
      const f32x4 s0 = *(LAS f32x4*)(L + L_R + 1024 + tid * 32), s1 = *(LAS f32x4*)(L + L_R + 1024 + tid * 32 + 16);
      const f32x4 f4 = f0 + f1, s4 = s0 + s1;
      const float f = (f4.x + f4.y) + (f4.z + f4.w), sv = (s4.x + s4.y) + (s4.z + s4.w);
      const float r = fmaf(sv, silu(f), bflo(xo));
      p.out[oidx] = (unsigned short)(cvtpk(r, 0.f) & 0xFFFFu);
    }
    if (bid == NWG - 1 && tid == 0) {
      const v2u ne = {FL[6], FL[7]};
      __builtin_amdgcn_raw_buffer_store_b64(ne, rs, WS_E, 0, AUX_SC1);
    }
  }
}

}

extern "C" void kernel_launch(void* const* d_in, const int* in_sizes, int n_in, void* d_out, int out_size, void* d_ws,
                              size_t ws_size, hipStream_t stream) {
  Params p;
  p.x = (const unsigned short*)d_in[0];
  p.kc = (const unsigned short*)d_in[1];
  p.vc = (const unsigned short*)d_in[2];
  p.wq = (const unsigned short*)d_in[3];
  p.wo = (const unsigned short*)d_in[4];
  p.wg = (const unsigned short*)d_in[6];
  p.wu = (const unsigned short*)d_in[7];
  p.wd = (const unsigned short*)d_in[8];
  p.out = (unsigned short*)d_out;
  p.ws = (unsigned char*)d_ws;
  hipLaunchKernelGGL(fused_decode, dim3(NWG), dim3(NTHR), 0, stream, p);
}
```

```cpp
#define KAUX 0
#define VAUX 2
#define WAUX 0
#include <hip/hip_runtime.h>
#include <hip/hip_bf16.h>
#include <stdint.h>

#define GAS __attribute__((address_space(1)))
#define LAS __attribute__((address_space(3)))
typedef unsigned v4u __attribute__((ext_vector_type(4)));
typedef unsigned v2u __attribute__((ext_vector_type(2)));
typedef float f32x4 __attribute__((ext_vector_type(4)));
typedef short bf16x8 __attribute__((ext_vector_type(8)));
typedef short bf16x4 __attribute__((ext_vector_type(4)));
typedef unsigned long long u64;

namespace {

constexpr int NWG = 256, NTHR = 576;
constexpr int D = 1024, HD = 4096, FF = 3584, SK = 32768;
constexpr int AUX_SC1 = 16;
constexpr unsigned SENT = 0xFFFFFFFFu;
constexpr unsigned SPIN_CAP = 1u << 22;

constexpr int RSG = 65536, RSP = 131072, RSS = 65536;
constexpr int WS_E = 0;
constexpr int WS_QP = 4096;
constexpr int WS_GU = WS_QP + 262144;
constexpr int WS_P1 = WS_GU + 8 * RSG;
constexpr int WS_S2 = WS_P1 + 8 * RSP;
constexpr int WS_F2 = WS_S2 + 8 * RSS;
constexpr int WS_ALL = WS_F2 + 8 * RSS;

constexpr int L_RED = 0;
constexpr int L_Q = 12288;
constexpr int L_F = 0;
constexpr int L_ML = L_Q + 16 * 272;
constexpr int L_O = L_ML + 512;
constexpr int L_S = L_O;
constexpr int L_C = L_O + 32768;
constexpr int L_R = L_C + 2048 + 64;
constexpr int L_FL = L_R + 2048;
constexpr int L_BYTES = L_FL + 64;
static_assert(L_F + 16384 <= L_ML, "lds");

struct Params {
  const unsigned short* x;
  const unsigned short* wq;
  unsigned char* ws;
  const unsigned short* wg;
  const unsigned short* wu;
  const unsigned short* wd;
  const unsigned short* wo;
  const unsigned short* kc;
  const unsigned short* vc;
  unsigned short* out;
};

__device__ __forceinline__ float bflo(unsigned u) { return __builtin_bit_cast(float, u << 16); }
__device__ __forceinline__ float bfhi(unsigned u) { return __builtin_bit_cast(float, u & 0xFFFF0000u); }
__device__ __forceinline__ unsigned f2u(float f) { return __builtin_bit_cast(unsigned, f); }
__device__ __forceinline__ float u2f(unsigned u) { return __builtin_bit_cast(float, u); }
typedef __bf16 bf16p __attribute__((ext_vector_type(2)));
typedef float f32x2 __attribute__((ext_vector_type(2)));
__device__ __forceinline__ unsigned cvtpk(float lo, float hi) {
  return __builtin_bit_cast(unsigned, __builtin_convertvector(f32x2{lo, hi}, bf16p));
}
template <int CTRL> __device__ __forceinline__ float dpp(float x) {
  return __builtin_bit_cast(float, __builtin_amdgcn_mov_dpp(__builtin_bit_cast(int, x), CTRL, 0xf, 0xf, true));
}
constexpr int XOR1 = 0xB1, XOR2 = 0x4E, XOR7 = 0x141, XOR8 = 0x128;
__device__ __forceinline__ float x16_sum(float x) {
  auto s = __builtin_amdgcn_permlane16_swap(f2u(x), f2u(x), false, false);
  return u2f(s[0]) + u2f(s[1]);
}
__device__ __forceinline__ float x32_sum(float x) {
  auto s = __builtin_amdgcn_permlane32_swap(f2u(x), f2u(x), false, false);
  return u2f(s[0]) + u2f(s[1]);
}
__device__ __forceinline__ float x16_max(float x) {
  auto s = __builtin_amdgcn_permlane16_swap(f2u(x), f2u(x), false, false);
  return fmaxf(u2f(s[0]), u2f(s[1]));
}
__device__ __forceinline__ float x32_max(float x) {
  auto s = __builtin_amdgcn_permlane32_swap(f2u(x), f2u(x), false, false);
  return fmaxf(u2f(s[0]), u2f(s[1]));
}
__device__ __forceinline__ float row16_sum(float x) {
  x += dpp<XOR1>(x);
  x += dpp<XOR2>(x);
  x += dpp<XOR7>(x);
  x += dpp<XOR8>(x);
  return x;
}
__device__ __forceinline__ float wave_sum(float x) { return x32_sum(x16_sum(row16_sum(x))); }
__device__ __forceinline__ float tr16(float a, float b) {
  auto s = __builtin_amdgcn_permlane16_swap(f2u(a), f2u(b), false, false);
  return u2f(s[0]) + u2f(s[1]);
}
__device__ __forceinline__ float tr32(float a, float b) {
  auto s = __builtin_amdgcn_permlane32_swap(f2u(a), f2u(b), false, false);
  return u2f(s[0]) + u2f(s[1]);
}
__device__ __forceinline__ v4u ldnt(const GAS v4u* p) { if (WAUX) return __builtin_nontemporal_load(p); else return *p; }
__device__ __forceinline__ unsigned clean(float f) {
  const unsigned u = f2u(f);
  return u == SENT ? 0x7FC00000u : u;
}
__device__ __forceinline__ v4u clean4(f32x4 v) { return v4u{clean(v.x), clean(v.y), clean(v.z), clean(v.w)}; }
__device__ __forceinline__ bool full4(v4u v) { return v.x != SENT && v.y != SENT && v.z != SENT && v.w != SENT; }
__device__ __forceinline__ float rdl(float v, int lane) { return u2f(__builtin_amdgcn_readlane(f2u(v), lane)); }
__device__ __forceinline__ float rcp(float v) { return __builtin_amdgcn_rcpf(v); }
__device__ __forceinline__ float silu(float v) { return v * rcp(1.0f + __builtin_amdgcn_exp2f(v * -1.4426950408889634f)); }

#define SB() __builtin_amdgcn_sched_barrier(0)
#define WG_BARRIER()                                        \
  do {                                                      \
    asm volatile("s_waitcnt lgkmcnt(0)" ::: "memory");      \
    __builtin_amdgcn_s_barrier();                           \
    asm volatile("" ::: "memory");                          \
  } while (0)

__device__ __forceinline__ f32x4 gemv64(const v4u (&w)[4], const float (&x0)[4], const float (&x1)[4]) {
  float a0[8], a1[8];
#pragma unroll
  for (int c = 0; c < 8; ++c) { a0[c] = 0.f; a1[c] = 0.f; }
#pragma unroll
  for (int i = 0; i < 4; ++i) {
#pragma unroll
    for (int k = 0; k < 4; ++k) {
      const float lo = bflo(w[i][k]), hi = bfhi(w[i][k]);
      a0[2 * k] = fmaf(lo, x0[i], a0[2 * k]);
      a0[2 * k + 1] = fmaf(hi, x0[i], a0[2 * k + 1]);
      a1[2 * k] = fmaf(lo, x1[i], a1[2 * k]);
      a1[2 * k + 1] = fmaf(hi, x1[i], a1[2 * k + 1]);
    }
  }
  float t[8];
#pragma unroll
  for (int c = 0; c < 8; ++c) t[c] = tr16(a0[c], a1[c]);
  f32x4 r;
#pragma unroll
  for (int c = 0; c < 4; ++c) {
    float v = tr32(t[c], t[c + 4]);
    v += dpp<XOR8>(v);
    r[c] = v;
  }
  return r;
}

__device__ __forceinline__ void gemv256(const v2u (&w)[8], const float (&c0)[8], const float (&c1)[8], f32x4& s0, f32x4& s1) {
  s0 = f32x4{0.f, 0.f, 0.f, 0.f};
  s1 = f32x4{0.f, 0.f, 0.f, 0.f};
#pragma unroll
  for (int k = 0; k < 8; ++k) {
    const f32x4 wf = {bflo(w[k].x), bfhi(w[k].x), bflo(w[k].y), bfhi(w[k].y)};
#pragma unroll
    for (int c = 0; c < 4; ++c) {
      s0[c] = fmaf(wf[c], c0[k], s0[c]);
      s1[c] = fmaf(wf[c], c1[k], s1[c]);
    }
  }
}

__device__ __forceinline__ v2u granule(float v, unsigned tag_lo, unsigned tag_hi) {
  return v2u{(tag_lo << 24) | ((f2u(v) + 0x80u) >> 8), tag_hi};
}
__device__ __forceinline__ bool tag2(v4u g, unsigned tag_lo, unsigned tag_hi) {
  return (g.y == tag_hi) & ((g.x >> 24) == tag_lo) & (g.w == tag_hi) & ((g.z >> 24) == tag_lo);
}
#define TOUCH4(a) asm volatile("" : "+v"(a[0]), "+v"(a[1]), "+v"(a[2]), "+v"(a[3]))
constexpr int AUX_NT = WAUX;
#define LDK(kb_, off_) do { _Pragma("unroll") for (int j = 0; j < 4; ++j) kb_[j] = __builtin_amdgcn_raw_buffer_load_b128(rk, (klane + (off_)) + j * 64, 0, KAUX); SB(); } while (0)
#define LDV(vb_, off_) do { _Pragma("unroll") for (int j = 0; j < 4; ++j) vb_[j] = __builtin_amdgcn_raw_buffer_load_b128(rv, (vlane + (off_)) + j * 1024, 0, VAUX); SB(); } while (0)
#define LDK2(kb_, a_) do { _Pragma("unroll") for (int j = 0; j < 4; ++j) kb_[j] = __builtin_amdgcn_raw_buffer_load_b128(rk, (a_) + j * 64, 0, KAUX); SB(); } while (0)
#define LDV2(vb_, a_) do { _Pragma("unroll") for (int j = 0; j < 4; ++j) vb_[j] = __builtin_amdgcn_raw_buffer_load_b128(rv, (a_) + j * 1024, 0, VAUX); SB(); } while (0)

struct AttnState {
  float m, l;
  f32x4 o[8];
};
__device__ __forceinline__ v2u attn_qk(AttnState& st, const v4u (&kc)[4], const v4u (&qf)[4]) {
  constexpr float C2 = 0.088388347648318440f * 1.4426950408889634f;
  f32x4 s = {0.f, 0.f, 0.f, 0.f};
#pragma unroll
  for (int kk = 0; kk < 4; ++kk)
    s = __builtin_amdgcn_mfma_f32_16x16x32_bf16(__builtin_bit_cast(bf16x8, kc[kk]), __builtin_bit_cast(bf16x8, qf[kk]), s, 0, 0, 0);
#pragma unroll
  for (int r = 0; r < 4; ++r) s[r] = (s[r] + dpp<XOR8>(s[r])) * C2;
  float mx = fmaxf(fmaxf(s[0], s[1]), fmaxf(s[2], s[3]));
  mx = x32_max(x16_max(mx));
  const float mn = fmaxf(st.m, mx);
  const float alpha = __builtin_amdgcn_exp2f(st.m - mn);
  st.m = mn;
  const float p0 = __builtin_amdgcn_exp2f(s[0] - mn), p1 = __builtin_amdgcn_exp2f(s[1] - mn);
  const float p2 = __builtin_amdgcn_exp2f(s[2] - mn), p3 = __builtin_amdgcn_exp2f(s[3] - mn);
  st.l = fmaf(st.l, alpha, (p0 + p1) + (p2 + p3));
  const v2u pb = {cvtpk(p0, p1), cvtpk(p2, p3)};
#pragma unroll
  for (int e = 0; e < 8; ++e) st.o[e] *= alpha;
  SB();
  return pb;
}
__device__ __forceinline__ void attn_pv(AttnState& st, const v4u (&vc)[4], v2u pb) {
#pragma unroll
  for (int e = 0; e < 8; ++e) {
    const unsigned sel = (e & 1) ? 0x07060302u : 0x05040100u;
    const v2u a = {__builtin_amdgcn_perm(vc[1][e >> 1], vc[0][e >> 1], sel), __builtin_amdgcn_perm(vc[3][e >> 1], vc[2][e >> 1], sel)};
    st.o[e] = __builtin_amdgcn_mfma_f32_16x16x16bf16_1k(__builtin_bit_cast(bf16x4, a), __builtin_bit_cast(bf16x4, pb), st.o[e], 0, 0, 0);
  }
  SB();
}
__device__ __forceinline__ void attn_unit(AttnState& st, const v4u (&kc)[4], const v4u (&vc)[4], const v4u (&qf)[4]) {
  attn_pv(st, vc, attn_qk(st, kc, qf));
}

#define DRAIN() asm volatile("s_waitcnt vmcnt(0)" ::: "memory")
constexpr int MV = 3;
typedef float f32x2v __attribute__((ext_vector_type(2)));
__device__ __forceinline__ void lds_signal(LAS unsigned* c) {
  asm volatile("s_waitcnt lgkmcnt(0)" ::: "memory");
  if ((threadIdx.x & 63) == 0) __hip_atomic_fetch_add(c, 1u, __ATOMIC_RELAXED, __HIP_MEMORY_SCOPE_WORKGROUP);
}
__device__ __forceinline__ void lds_wait(LAS unsigned* c, unsigned want) {
#pragma nounroll
  for (unsigned spins = 0; spins < (SPIN_CAP << 3); ++spins) {
    asm volatile("" ::: "memory");
    if (__builtin_amdgcn_readfirstlane(*(volatile LAS unsigned*)c) >= want) break;
    __builtin_amdgcn_s_sleep(1);
  }
  asm volatile("" ::: "memory");
}

__global__ void __launch_bounds__(NTHR) fused_decode(Params p) {
  __shared__ __attribute__((aligned(16))) unsigned char smem[L_BYTES];
  LAS unsigned char* L = (LAS unsigned char*)smem;
  LAS unsigned* FL = (LAS unsigned*)(L + L_FL);
  const int tid = (int)threadIdx.x - 64, lane = threadIdx.x & 63;
  const int w = __builtin_amdgcn_readfirstlane(threadIdx.x >> 6) - 1;
  const int bid = blockIdx.x;
  const int grp = bid >> 5, xq = bid & 7;
  const int cg = (bid >> 3) & 3, rg = grp * 8 + xq;
  const int hkv = bid >> 6, bb = grp & 1;
  const int xl = (3 - (int)((size_t)p.wq >> 7)) & 7;
  const bool has_ffn = xq != xl;
  const int fi = has_ffn ? grp * 7 + (xq < xl ? xq : xq - 1) : 0;
  const bool reducer = rg >= 48;
  constexpr int OOB = 0x7FFF0000;
  const __amdgpu_buffer_rsrc_t rs = __builtin_amdgcn_make_buffer_rsrc((void*)p.ws, 0, WS_ALL, 0x00020000);
  const int p1own = (bid & 7) * RSP + WS_P1 + (bid >> 3) * 4096;
  const int s2own = (rg & 7) * RSS + (cg * 8 + (rg >> 3)) * 2048;
  const GAS v4u* xg = (const GAS v4u*)p.x;

  if (w < 0) {
    __builtin_amdgcn_s_setprio(3);
    const v4u xa0 = xg[lane * 2], xa1 = xg[lane * 2 + 1], xb0 = xg[128 + lane * 2], xb1 = xg[128 + lane * 2 + 1];
    const v2u ev = __builtin_amdgcn_raw_buffer_load_b64(rs, WS_E, 0, AUX_SC1);
    if (lane < 6) FL[lane] = 0u;
    float rr;
    {
      float va_[16], vb_[16];
#pragma unroll
      for (int k = 0; k < 4; ++k) {
        va_[2 * k] = bflo(xa0[k]); va_[2 * k + 1] = bfhi(xa0[k]); va_[8 + 2 * k] = bflo(xa1[k]); va_[8 + 2 * k + 1] = bfhi(xa1[k]);
        vb_[2 * k] = bflo(xb0[k]); vb_[2 * k + 1] = bfhi(xb0[k]); vb_[8 + 2 * k] = bflo(xb1[k]); vb_[8 + 2 * k + 1] = bfhi(xb1[k]);
      }
      float sa = 0.f, sb = 0.f;
#pragma unroll
      for (int k = 0; k < 16; ++k) { sa += va_[k]; sb += vb_[k]; }
      const float ma = wave_sum(sa) * (1.0f / D), mb = wave_sum(sb) * (1.0f / D);
      if (lane == 0) *(LAS f32x2v*)(L + L_FL + 32) = f32x2v{ma, mb};
      WG_BARRIER();
      float qa = 0.f, qb = 0.f;
#pragma unroll
      for (int k = 0; k < 16; ++k) { const float da = va_[k] - ma, db = vb_[k] - mb; qa = fmaf(da, da, qa); qb = fmaf(db, db, qb); }
      const float ta = wave_sum(qa), tb = wave_sum(qb);
      rr = 1.0f / sqrtf((lane < 32 ? ta : tb) * (1.0f / D) + 1e-5f);
    }
    const u64 e1 = ((((u64)__builtin_amdgcn_readfirstlane(ev.y)) << 32) | (u64)(unsigned)__builtin_amdgcn_readfirstlane(ev.x)) + 1ull;
    const unsigned tag_hi = (unsigned)(e1 >> 8), tag_lo = (unsigned)(e1 & 0xFFull);
    const unsigned tq = (unsigned)e1 & 0xFFFFFFu;
    if (lane == 0) { FL[6] = (unsigned)e1; FL[7] = (unsigned)(e1 >> 32); }
    lds_wait(FL + 0, 8);
    {
      f32x2v a = {0.f, 0.f};
#pragma unroll
      for (int ww = 0; ww < 8; ++ww) a += *(LAS f32x2v*)(L + L_RED + ww * 512 + lane * 8);
      const unsigned v0 = (f2u(a.x * rr) + 0x800u) >> 12, v1 = (f2u(a.y * rr) + 0x800u) >> 12;
      __builtin_amdgcn_raw_buffer_store_b64(v2u{v0 | (v1 << 20), (v1 >> 12) | (tq << 8)}, rs,
          WS_QP + ((hkv * 2 + (lane >> 5)) * 4 + cg) * 4096 + (rg & 15) * 256 + (lane & 31) * 8, 0, AUX_SC1);
    }
    DRAIN();
    {
      const int qoff = WS_QP + (hkv * 2 + bb) * 16384 + lane * 16;
      v4u qg[16];
#pragma nounroll
      for (unsigned spins = 0; spins < SPIN_CAP; ++spins) {
        asm volatile("" ::: "memory");
        bool ok = true;
#pragma unroll
        for (int j = 0; j < 16; ++j) qg[j] = __builtin_amdgcn_raw_buffer_load_b128(rs, qoff + (j & 3) * 1024, (j >> 2) * 4096, AUX_SC1);
#pragma unroll
        for (int j = 0; j < 16; ++j) ok &= ((qg[j].y >> 8) == tq) & ((qg[j].w >> 8) == tq);
        if (__all(ok)) break;
        __builtin_amdgcn_s_sleep(4);
      }
#pragma unroll
      for (int j = 0; j < 4; ++j) {
        f32x4 q = {0.f, 0.f, 0.f, 0.f};
#pragma unroll
        for (int ks = 0; ks < 4; ++ks) {
          const v4u g = qg[ks * 4 + j];
          q += f32x4{u2f(g.x << 12), u2f((g.y << 24) | ((g.x >> 20) << 12)), u2f(g.z << 12), u2f((g.w << 24) | ((g.z >> 20) << 12))};
        }
        const v2u hi = {cvtpk(q.x, q.y), cvtpk(q.z, q.w)};
        const v2u lo = {cvtpk(q.x - bflo(hi.x), q.y - bfhi(hi.x)), cvtpk(q.z - bflo(hi.y), q.w - bfhi(hi.y))};
        *(LAS v2u*)(L + L_Q + (2 * j + (lane >> 5)) * 272 + (lane & 31) * 8) = hi;
        *(LAS v2u*)(L + L_Q + (2 * j + 8 + (lane >> 5)) * 272 + (lane & 31) * 8) = lo;
      }
      lds_signal(FL + 3);
    }
    const int coff = WS_QP + (((lane >> 4) * 2) * 4 + cg) * 4096 + (lane & 15) * 256;
    if (reducer) {
#pragma nounroll
      for (unsigned spins = 0; spins < SPIN_CAP; ++spins) {
        asm volatile("" ::: "memory");
        const v4u ck = __builtin_amdgcn_raw_buffer_load_b128(rs, coff, 0, AUX_SC1);
        if (__all(((ck.y >> 8) == tq) & ((ck.w >> 8) == tq))) break;
        __builtin_amdgcn_s_sleep(4);
      }
    }
    if (has_ffn) {
      lds_wait(FL + 1, 8);
      v4u gr[2];
#pragma unroll
      for (int m = 0; m < 2; ++m) {
        f32x2v a = {0.f, 0.f};
#pragma unroll
        for (int ww = 0; ww < 8; ++ww) a += *(LAS f32x2v*)(L + L_RED + 4096 + m * 4096 + ww * 512 + lane * 8);
        const v2u g0 = granule(a.x * rr, tag_lo, tag_hi), g1 = granule(a.y * rr, tag_lo, tag_hi);
        gr[m] = v4u{g0.x, g0.y, g1.x, g1.y};
      }
      lds_wait(FL + 2, 4);
#pragma unroll
      for (int m = 0; m < 2; ++m) __builtin_amdgcn_raw_buffer_store_b128(gr[m], rs, (cg * 2 + m) * RSG + WS_GU + rg * 1024 + lane * 16, 0, AUX_SC1);
    }
    WG_BARRIER();
    return;
  }

  {
    const v4u s4 = {SENT, SENT, SENT, SENT};
    const int off = w < 4 ? p1own + w * 1024 : (w < 6 ? WS_S2 - 4096 : WS_F2 - 6144) + s2own + w * 1024;
    __builtin_amdgcn_raw_buffer_store_b128(s4, rs, off + lane * 16, 0, AUX_SC1);
    SB();
  }
  const int r8 = lane >> 3, c8 = lane & 7;
  const int krow = cg * 256 + w * 32 + r8 * 4;
  const v2u xr0 = *(const GAS v2u*)(p.x + krow), xr1 = *(const GAS v2u*)(p.x + D + krow);
  v4u wqr[4], wgr[4], wur[4];
#pragma unroll
  for (int i = 0; i < 4; ++i) wqr[i] = ldnt((const GAS v4u*)(p.wq + (size_t)(krow + i) * HD + rg * 64 + c8 * 8));
  SB();
  const int fbytes = has_ffn ? D * FF * 2 : 0;
  const __amdgpu_buffer_rsrc_t rwg = __builtin_amdgcn_make_buffer_rsrc((void*)p.wg, 0, fbytes, 0x00020000);
  const __amdgpu_buffer_rsrc_t rwu = __builtin_amdgcn_make_buffer_rsrc((void*)p.wu, 0, fbytes, 0x00020000);
  const __amdgpu_buffer_rsrc_t rwd = __builtin_amdgcn_make_buffer_rsrc((void*)p.wd, 0, fbytes, 0x00020000);
  const __amdgpu_buffer_rsrc_t rwo = __builtin_amdgcn_make_buffer_rsrc((void*)p.wo, 0, HD * D * 2, 0x00020000);
  const int guoff = (krow * FF + fi * 64 + c8 * 8) * 2;
#define LDWG() do { _Pragma("unroll") for (int i = 0; i < 4; ++i) wgr[i] = __builtin_amdgcn_raw_buffer_load_b128(rwg, guoff + i * FF * 2, 0, AUX_NT); SB(); } while (0)
#define LDWU() do { _Pragma("unroll") for (int i = 0; i < 4; ++i) wur[i] = __builtin_amdgcn_raw_buffer_load_b128(rwu, guoff + i * FF * 2, 0, AUX_NT); SB(); } while (0)
#define USEQ() do { const f32x4 r = gemv64(wqr, x0, x1); if ((lane & 8) == 0) *(LAS f32x4*)(L + L_RED + w * 512 + redoff) = r; \
    asm volatile("s_waitcnt vmcnt(4)" ::: "memory"); lds_signal(FL + 0); } while (0)
#define USEG() do { const f32x4 r = gemv64(wgr, x0, x1); if ((lane & 8) == 0) *(LAS f32x4*)(L + L_RED + 4096 + w * 512 + redoff) = r; SB(); } while (0)
#define USEU() do { const f32x4 r = gemv64(wur, x0, x1); if ((lane & 8) == 0) *(LAS f32x4*)(L + L_RED + 8192 + w * 512 + redoff) = r; lds_signal(FL + 1); } while (0)
  LDWG();
  const int i16 = lane & 15, g4 = lane >> 4;
  const __amdgpu_buffer_rsrc_t rk = __builtin_amdgcn_make_buffer_rsrc((void*)p.kc, 0, 8 * SK * 256, 0x00020000);
  const __amdgpu_buffer_rsrc_t rv = __builtin_amdgcn_make_buffer_rsrc((void*)p.vc, 0, 8 * SK * 256, 0x00020000);
  const int klane = (4 * (i16 & 3) + (i16 >> 2)) * 256 + g4 * 16, vlane = lane * 16;
  const int rsl = (((3 - (int)((size_t)p.wg >> 7)) & 7) + grp) & 7;
  const int xs = rsl < xl ? rsl : rsl + 1;
  const int nunit = rsl == 7 ? 64 : (!has_ffn ? 64 + MV : (xq == xs ? 64 - MV : 64));
  const int obase = ((bb * 4 + hkv) * 2048 + cg * 512) * 4096;
  const int ubase = obase + xq * 262144, xbase = obase + xs * 262144 - MV * 4096;
#define UOFF(t_) (ubase + (t_) * 4096)
#define UOFFX(t_) (((t_) < 64 ? ubase : xbase) + (t_) * 4096)
  v4u ka[4], va[4], kb[4], vb[4], kc[4], vc[4];
  WG_BARRIER();
  float x0[4], x1[4];
  {
    const f32x2v mm = *(LAS f32x2v*)(L + L_FL + 32);
    x0[0] = bflo(xr0.x) - mm.x; x0[1] = bfhi(xr0.x) - mm.x; x0[2] = bflo(xr0.y) - mm.x; x0[3] = bfhi(xr0.y) - mm.x;
    x1[0] = bflo(xr1.x) - mm.y; x1[1] = bfhi(xr1.x) - mm.y; x1[2] = bflo(xr1.y) - mm.y; x1[3] = bfhi(xr1.y) - mm.y;
  }
  const int redoff = ((g4 & 1) * 64 + c8 * 8 + (g4 >> 1) * 4) * 4;

  USEQ(); LDWU();
  USEG(); LDK(ka, UOFF(w));
  USEU(); LDV(va, UOFF(w));
  TOUCH4(ka);
  LDK(kb, UOFF(w + 8));
  TOUCH4(va);
  LDV(vb, UOFF(w + 8));
  TOUCH4(kb);
  LDK(kc, UOFF(w + 16));
  TOUCH4(vb);
  LDV(vc, UOFF(w + 16));
  lds_wait(FL + 3, 1);
  v4u qf[4];
#pragma unroll
  for (int kk = 0; kk < 4; ++kk) qf[kk] = *(LAS v4u*)(L + L_Q + i16 * 272 + kk * 64 + g4 * 16);

  AttnState st;
  st.m = -INFINITY;
  st.l = 0.f;
#pragma unroll
  for (int e = 0; e < 8; ++e) st.o[e] = f32x4{0.f, 0.f, 0.f, 0.f};
  attn_unit(st, ka, va, qf);
  attn_unit(st, kb, vb, qf);
  int t = w + 24;
  for (;;) {
    const int o0 = t < nunit ? UOFFX(t) : OOB;
    const int ak0 = klane + o0, av0 = vlane + o0;
    LDK2(kb, ak0);
    LDV2(vb, av0);
    asm volatile("" ::"v"(ak0), "v"(av0));
    attn_unit(st, kc, vc, qf);
    if (t >= nunit) break;
    const int o1 = t + 8 < nunit ? UOFFX(t + 8) : OOB;
    const int ak1 = klane + o1, av1 = vlane + o1;
    LDK2(kc, ak1);
    LDV2(vc, av1);
    asm volatile("" ::"v"(ak1), "v"(av1));
    attn_unit(st, kb, vb, qf);
    if (t + 8 >= nunit) break;
    t += 16;
  }
  lds_signal(FL + 2);
  const int sfoff = ((tid >> 2) & 1) * 1024 + ((tid & 127) >> 3) * 64 + (tid & 3) * 16;
  const int oidx = ((tid >> 4) & 1) * D + cg * 256 + (rg & 15) * 16 + (tid & 15);
  unsigned xo = 0;
  if (w == 0) xo = *(const GAS unsigned short*)(p.x + oidx);

  {
    const float l = x32_sum(x16_sum(st.l));
    if (i16 < 8) {
#pragma unroll
      for (int e = 0; e < 8; ++e) *(LAS f32x4*)(L + L_O + (w * 256 + (i16 * 4 + g4) * 8 + (e ^ i16)) * 16) = st.o[e];
      if (g4 == 0) *(LAS v2u*)(L + L_ML + (i16 * 8 + w) * 8) = v2u{f2u(st.m), f2u(l)};
    }
  }
  int poff = g4 * RSP + WS_P1 + (hkv * 8 + w) * 4096 + (rg & 15) * 256 + i16 * 16;
  int goff = (lane >> 3) * RSG + WS_GU + rg * 1024 + ((lane >> 2) & 1) * 512 + (w * 8 + (lane & 3) * 2) * 8;
  int roff = ((tid >> 3) & 7) * RSS + (cg * 8 + (tid >> 6)) * 2048 + (rg & 15) * 128 + (tid & 7) * 16;
  int p1st = p1own + tid * 16;
  int s2st = (w < 2 ? WS_S2 : WS_F2) + s2own + (tid & 127) * 16;
  __builtin_amdgcn_s_waitcnt(0x0F70);
  asm volatile("" : "+v"(poff), "+v"(goff), "+v"(roff), "+v"(p1st), "+v"(s2st));
  WG_BARRIER();
  if (w < 4) {
    const int j = tid >> 5;
    v4u ml[4];
    f32x4 ov[8];
#pragma unroll
    for (int k = 0; k < 4; ++k) ml[k] = *(LAS v4u*)(L + L_ML + j * 64 + k * 16);
#pragma unroll
    for (int ww = 0; ww < 8; ++ww) ov[ww] = *(LAS f32x4*)(L + L_O + (ww * 256 + (tid ^ j)) * 16);
    SB();
    float M = fmaxf(u2f(ml[0].x), u2f(ml[0].z));
#pragma unroll
    for (int k = 1; k < 4; ++k) M = fmaxf(M, fmaxf(u2f(ml[k].x), u2f(ml[k].z)));
    float Ls = 0.f;
    f32x4 acc = {0.f, 0.f, 0.f, 0.f};
#pragma unroll
    for (int ww = 0; ww < 8; ++ww) {
      const float wt = __builtin_amdgcn_exp2f(u2f(ml[ww >> 1][(ww & 1) * 2]) - M);
      Ls = fmaf(wt, u2f(ml[ww >> 1][(ww & 1) * 2 + 1]), Ls);
      acc += ov[ww] * wt;
    }
    acc *= rcp(Ls);
    const unsigned lse = f2u(M + __builtin_amdgcn_logf(Ls));
    v4u pk;
#pragma unroll
    for (int r = 0; r < 4; ++r) {
      const unsigned t = ((f2u(acc[r]) + 0x80u) & 0xFFFFFF00u) | ((lse >> (8 * r)) & 0xFFu);
      pk[r] = t == SENT ? 0x7FC000FFu : t;
    }
    __builtin_amdgcn_raw_buffer_store_b128(pk, rs, p1st, 0, AUX_SC1);
  }
  SB();

  f32x4 wof[8];
  {
    v4u gu = __builtin_amdgcn_raw_buffer_load_b128(rs, goff, 0, AUX_SC1);
    SB();
    v2u wt[8], wor[8];
#pragma unroll
    for (int k = 0; k < 8; ++k) wt[k] = __builtin_amdgcn_raw_buffer_load_b64(rwd, lane * 8, ((fi * 64 + w * 8 + k) * D + cg * 256) * 2, AUX_NT);
    SB();
#pragma unroll
    for (int k = 0; k < 8; ++k) wor[k] = __builtin_amdgcn_raw_buffer_load_b64(rwo, lane * 8, ((rg * 64 + w * 8 + k) * D + cg * 256) * 2, AUX_NT);
    SB();
    v4u pa = __builtin_amdgcn_raw_buffer_load_b128(rs, poff, 0, AUX_SC1);
    v4u pb = __builtin_amdgcn_raw_buffer_load_b128(rs, poff, 4 * RSP, AUX_SC1);
    SB();
    if (has_ffn) {
      const unsigned e_lo = FL[6], e_hi = FL[7];
      const unsigned tag_hi = (e_lo >> 8) | (e_hi << 24), tag_lo = e_lo & 0xFFu;
      if (!__all(tag2(gu, tag_lo, tag_hi))) {
#pragma nounroll
        for (unsigned spins = 0; spins < SPIN_CAP; ++spins) {
          asm volatile("" ::: "memory");
          __builtin_amdgcn_s_sleep(1);
          gu = __builtin_amdgcn_raw_buffer_load_b128(rs, goff, 0, AUX_SC1);
          if (__all(tag2(gu, tag_lo, tag_hi))) break;
        }
      }
      const float ga = x32_sum(x16_sum(u2f(gu.x << 8))), gb = x32_sum(x16_sum(u2f(gu.z << 8)));
      const float hx = silu(ga) * dpp<XOR8>(ga), hy = silu(gb) * dpp<XOR8>(gb);
      float h0[8], h1[8];
#pragma unroll
      for (int pr = 0; pr < 4; ++pr) {
        h0[2 * pr] = rdl(hx, pr); h0[2 * pr + 1] = rdl(hy, pr);
        h1[2 * pr] = rdl(hx, 4 + pr); h1[2 * pr + 1] = rdl(hy, 4 + pr);
      }
      f32x4 s0, s1;
      gemv256(wt, h0, h1, s0, s1);
      *(LAS f32x4*)(L + L_F + w * 2048 + lane * 16) = s0;
      *(LAS f32x4*)(L + L_F + w * 2048 + 1024 + lane * 16) = s1;
    }
#pragma unroll
    for (int k = 0; k < 8; ++k) {
      const v2u t = wor[k];
      wof[k] = f32x4{bflo(t.x), bfhi(t.x), bflo(t.y), bfhi(t.y)};
      asm volatile("" : "+v"(wof[k]));
    }
    bool ha = full4(pa), hb = full4(pb);
#pragma nounroll
    for (unsigned spins = 0; spins < SPIN_CAP; ++spins) {
      asm volatile("" ::: "memory");
      if (__all(ha & hb)) break;
      if (!ha) pa = __builtin_amdgcn_raw_buffer_load_b128(rs, poff, 0, AUX_SC1);
      if (!hb) pb = __builtin_amdgcn_raw_buffer_load_b128(rs, poff, 4 * RSP, AUX_SC1);
      ha = full4(pa);
      hb = full4(pb);
    }
    const float la = u2f(__builtin_amdgcn_perm(__builtin_amdgcn_perm(pa.w, pa.z, 0x0c0c0400u), __builtin_amdgcn_perm(pa.y, pa.x, 0x0c0c0400u), 0x05040100u));
    const float lb = u2f(__builtin_amdgcn_perm(__builtin_amdgcn_perm(pb.w, pb.z, 0x0c0c0400u), __builtin_amdgcn_perm(pb.y, pb.x, 0x0c0c0400u), 0x05040100u));
    const float Mw = x32_max(x16_max(fmaxf(la, lb)));
    const float wa = __builtin_amdgcn_exp2f(la - Mw), wb = __builtin_amdgcn_exp2f(lb - Mw);
    const float Ww = x32_sum(x16_sum(wa + wb));
    float a[4];
#pragma unroll
    for (int r = 0; r < 4; ++r) a[r] = fmaf(u2f(pa[r] & 0xFFFFFF00u), wa, u2f(pb[r] & 0xFFFFFF00u) * wb);
    const float v = tr32(tr16(a[0], a[1]), tr16(a[2], a[3]));
    *(LAS float*)(L + L_C + (((w >> 2) * 64 + (i16 >> 3) * 32 + g4 * 8 + (i16 & 7)) * 4 + (w & 3)) * 4) = v;
    if (lane == 0) *(LAS v2u*)(L + L_C + 2048 + w * 8) = v2u{f2u(Mw), f2u(Ww)};
  }
  WG_BARRIER();
  {
    const int b = (lane >> 3) & 1, k = lane & 7;
    const v4u m0 = *(LAS v4u*)(L + L_C + 2048 + b * 32), m1 = *(LAS v4u*)(L + L_C + 2048 + b * 32 + 16);
    const f32x4 av = *(LAS f32x4*)(L + L_C + (b * 64 + w * 8 + k) * 16);
    const float M = fmaxf(fmaxf(u2f(m0.x), u2f(m0.z)), fmaxf(u2f(m1.x), u2f(m1.z)));
    const float f0 = __builtin_amdgcn_exp2f(u2f(m0.x) - M), f1 = __builtin_amdgcn_exp2f(u2f(m0.z) - M);
    const float f2 = __builtin_amdgcn_exp2f(u2f(m1.x) - M), f3 = __builtin_amdgcn_exp2f(u2f(m1.z) - M);
    const float num = fmaf(f0, av.x, f1 * av.y) + fmaf(f2, av.z, f3 * av.w);
    const float den = fmaf(f0, u2f(m0.y), f1 * u2f(m0.w)) + fmaf(f2, u2f(m1.y), f3 * u2f(m1.w));
    const float ov = num * rcp(den);
    f32x4 s0 = {0.f, 0.f, 0.f, 0.f}, s1 = {0.f, 0.f, 0.f, 0.f};
#pragma unroll
    for (int kk = 0; kk < 8; ++kk) { s0 += wof[kk] * rdl(ov, kk); s1 += wof[kk] * rdl(ov, 8 + kk); }
    *(LAS f32x4*)(L + L_S + w * 2048 + lane * 16) = s0;
    *(LAS f32x4*)(L + L_S + w * 2048 + 1024 + lane * 16) = s1;
  }
  WG_BARRIER();
  if (w < 2 || (w < 4 && has_ffn)) {
    const int lb = (w < 2 ? L_S : L_F) + sfoff;
    f32x4 t[8];
#pragma unroll
    for (int ww = 0; ww < 8; ++ww) t[ww] = *(LAS f32x4*)(L + lb + ww * 2048);
    SB();
    const f32x4 s4 = ((t[0] + t[1]) + (t[2] + t[3])) + ((t[4] + t[5]) + (t[6] + t[7]));
    __builtin_amdgcn_raw_buffer_store_b128(clean4(s4), rs, s2st, 0, AUX_SC1);
  }

  if (reducer) {
    v4u a = __builtin_amdgcn_raw_buffer_load_b128(rs, roff, WS_S2, AUX_SC1);
    v4u c = {0u, 0u, 0u, 0u};
    if (((tid >> 3) & 7) != xl) c = __builtin_amdgcn_raw_buffer_load_b128(rs, roff, WS_F2, AUX_SC1);
    bool ha = full4(a), hc = full4(c);
#pragma nounroll
    for (unsigned spins = 0; spins < SPIN_CAP; ++spins) {
      asm volatile("" ::: "memory");
      if (__all(ha & hc)) break;
      if (!ha) a = __builtin_amdgcn_raw_buffer_load_b128(rs, roff, WS_S2, AUX_SC1);
      if (!hc) c = __builtin_amdgcn_raw_buffer_load_b128(rs, roff, WS_F2, AUX_SC1);
      ha = full4(a);
      hc = full4(c);
    }
    float vs = tr32(tr16(u2f(a.x), u2f(a.y)), tr16(u2f(a.z), u2f(a.w)));
    float vf = tr32(tr16(u2f(c.x), u2f(c.y)), tr16(u2f(c.z), u2f(c.w)));
    vs += dpp<XOR8>(vs);
    vf += dpp<XOR8>(vf);
    *(LAS float*)(L + L_R + ((lane >> 3) & 1) * 1024 + ((lane & 7) * 4 + (lane >> 4)) * 32 + w * 4) = (lane & 8) ? vs : vf;
    WG_BARRIER();
    if (tid < 32) {
      const f32x4 f0 = *(LAS f32x4*)(L + L_R + tid * 32), f1 = *(LAS f32x4*)(L + L_R + tid * 32 + 16);
      const f32x4 s0 = *(LAS f32x4*)(L + L_R + 1024 + tid * 32), s1 = *(LAS f32x4*)(L + L_R + 1024 + tid * 32 + 16);
      const f32x4 f4 = f0 + f1, s4 = s0 + s1;
      const float f = (f4.x + f4.y) + (f4.z + f4.w), sv = (s4.x + s4.y) + (s4.z + s4.w);
      const float r = fmaf(sv, silu(f), bflo(xo));
      p.out[oidx] = (unsigned short)(cvtpk(r, 0.f) & 0xFFFFu);
    }
    if (bid == NWG - 1 && tid == 0) {
      const v2u ne = {FL[6], FL[7]};
      __builtin_amdgcn_raw_buffer_store_b64(ne, rs, WS_E, 0, AUX_SC1);
    }
  }
}

}

extern "C" void kernel_launch(void* const* d_in, const int* in_sizes, int n_in, void* d_out, int out_size, void* d_ws,
                              size_t ws_size, hipStream_t stream) {
  Params p;
  p.x = (const unsigned short*)d_in[0];
  p.kc = (const unsigned short*)d_in[1];
  p.vc = (const unsigned short*)d_in[2];
  p.wq = (const unsigned short*)d_in[3];
  p.wo = (const unsigned short*)d_in[4];
  p.wg = (const unsigned short*)d_in[6];
  p.wu = (const unsigned short*)d_in[7];
  p.wd = (const unsigned short*)d_in[8];
  p.out = (unsigned short*)d_out;
  p.ws = (unsigned char*)d_ws;
  hipLaunchKernelGGL(fused_decode, dim3(NWG), dim3(NTHR), 0, stream, p);
}
```

```cpp
#include <hip/hip_runtime.h>
#include <hip/hip_bf16.h>
#include <stdint.h>

#define GAS __attribute__((address_space(1)))
#define LAS __attribute__((address_space(3)))
typedef unsigned v4u __attribute__((ext_vector_type(4)));
typedef unsigned v2u __attribute__((ext_vector_type(2)));
typedef float f32x4 __attribute__((ext_vector_type(4)));
typedef short bf16x8 __attribute__((ext_vector_type(8)));
typedef short bf16x4 __attribute__((ext_vector_type(4)));
typedef unsigned long long u64;

namespace {

constexpr int NWG = 256, NTHR = 576;
constexpr int D = 1024, HD = 4096, FF = 3584, SK = 32768;
constexpr int AUX_SC1 = 16;
constexpr unsigned SENT = 0xFFFFFFFFu;
constexpr unsigned SPIN_CAP = 1u << 22;

constexpr int RSG = 65536, RSP = 131072, RSS = 65536;
constexpr int WS_E = 0;
constexpr int WS_QP = 4096;
constexpr int WS_GU = WS_QP + 262144;
constexpr int WS_P1 = WS_GU + 8 * RSG;
constexpr int WS_S2 = WS_P1 + 8 * RSP;
constexpr int WS_F2 = WS_S2 + 8 * RSS;
constexpr int WS_ALL = WS_F2 + 8 * RSS;

constexpr int L_RED = 0;
constexpr int L_Q = 12288;
constexpr int L_F = 0;
constexpr int L_ML = L_Q + 16 * 272;
constexpr int L_O = L_ML + 512;
constexpr int L_S = L_O;
constexpr int L_C = L_O + 32768;
constexpr int L_R = L_C + 2048 + 64;
constexpr int L_FL = L_R + 2048;
constexpr int L_BYTES = L_FL + 64;
static_assert(L_F + 16384 <= L_ML, "lds");

struct Params {
  const unsigned short* x;
  const unsigned short* wq;
  unsigned char* ws;
  const unsigned short* wg;
  const unsigned short* wu;
  const unsigned short* wd;
  const unsigned short* wo;
  const unsigned short* kc;
  const unsigned short* vc;
  unsigned short* out;
};

__device__ __forceinline__ float bflo(unsigned u) { return __builtin_bit_cast(float, u << 16); }
__device__ __forceinline__ float bfhi(unsigned u) { return __builtin_bit_cast(float, u & 0xFFFF0000u); }
__device__ __forceinline__ unsigned f2u(float f) { return __builtin_bit_cast(unsigned, f); }
__device__ __forceinline__ float u2f(unsigned u) { return __builtin_bit_cast(float, u); }
typedef __bf16 bf16p __attribute__((ext_vector_type(2)));
typedef float f32x2 __attribute__((ext_vector_type(2)));
__device__ __forceinline__ unsigned cvtpk(float lo, float hi) {
  return __builtin_bit_cast(unsigned, __builtin_convertvector(f32x2{lo, hi}, bf16p));
}
template <int CTRL> __device__ __forceinline__ float dpp(float x) {
  return __builtin_bit_cast(float, __builtin_amdgcn_mov_dpp(__builtin_bit_cast(int, x), CTRL, 0xf, 0xf, true));
}
constexpr int XOR1 = 0xB1, XOR2 = 0x4E, XOR7 = 0x141, XOR8 = 0x128;
__device__ __forceinline__ float x16_sum(float x) {
  auto s = __builtin_amdgcn_permlane16_swap(f2u(x), f2u(x), false, false);
  return u2f(s[0]) + u2f(s[1]);
}
__device__ __forceinline__ float x32_sum(float x) {
  auto s = __builtin_amdgcn_permlane32_swap(f2u(x), f2u(x), false, false);
  return u2f(s[0]) + u2f(s[1]);
}
__device__ __forceinline__ float x16_max(float x) {
  auto s = __builtin_amdgcn_permlane16_swap(f2u(x), f2u(x), false, false);
  return fmaxf(u2f(s[0]), u2f(s[1]));
}
__device__ __forceinline__ float x32_max(float x) {
  auto s = __builtin_amdgcn_permlane32_swap(f2u(x), f2u(x), false, false);
  return fmaxf(u2f(s[0]), u2f(s[1]));
}
__device__ __forceinline__ float row16_sum(float x) {
  x += dpp<XOR1>(x);
  x += dpp<XOR2>(x);
  x += dpp<XOR7>(x);
  x += dpp<XOR8>(x);
  return x;
}
__device__ __forceinline__ float wave_sum(float x) { return x32_sum(x16_sum(row16_sum(x))); }
__device__ __forceinline__ float tr16(float a, float b) {
  auto s = __builtin_amdgcn_permlane16_swap(f2u(a), f2u(b), false, false);
  return u2f(s[0]) + u2f(s[1]);
}
__device__ __forceinline__ float tr32(float a, float b) {
  auto s = __builtin_amdgcn_permlane32_swap(f2u(a), f2u(b), false, false);
  return u2f(s[0]) + u2f(s[1]);
}
__device__ __forceinline__ v4u ldnt(const GAS v4u* p) { return __builtin_nontemporal_load(p); }
__device__ __forceinline__ unsigned clean(float f) {
  const unsigned u = f2u(f);
  return u == SENT ? 0x7FC00000u : u;
}
__device__ __forceinline__ v4u clean4(f32x4 v) { return v4u{clean(v.x), clean(v.y), clean(v.z), clean(v.w)}; }
__device__ __forceinline__ bool full4(v4u v) { return v.x != SENT && v.y != SENT && v.z != SENT && v.w != SENT; }
__device__ __forceinline__ float rdl(float v, int lane) { return u2f(__builtin_amdgcn_readlane(f2u(v), lane)); }
__device__ __forceinline__ float rcp(float v) { return __builtin_amdgcn_rcpf(v); }
__device__ __forceinline__ float silu(float v) { return v * rcp(1.0f + __builtin_amdgcn_exp2f(v * -1.4426950408889634f)); }

#define SB() __builtin_amdgcn_sched_barrier(0)
#define WG_BARRIER()                                        \
  do {                                                      \
    asm volatile("s_waitcnt lgkmcnt(0)" ::: "memory");      \
    __builtin_amdgcn_s_barrier();                           \
    asm volatile("" ::: "memory");                          \
  } while (0)

__device__ __forceinline__ f32x4 gemv64(const v4u (&w)[4], const float (&x0)[4], const float (&x1)[4]) {
  float a0[8], a1[8];
#pragma unroll
  for (int c = 0; c < 8; ++c) { a0[c] = 0.f; a1[c] = 0.f; }
#pragma unroll
  for (int i = 0; i < 4; ++i) {
#pragma unroll
    for (int k = 0; k < 4; ++k) {
      const float lo = bflo(w[i][k]), hi = bfhi(w[i][k]);
      a0[2 * k] = fmaf(lo, x0[i], a0[2 * k]);
      a0[2 * k + 1] = fmaf(hi, x0[i], a0[2 * k + 1]);
      a1[2 * k] = fmaf(lo, x1[i], a1[2 * k]);
      a1[2 * k + 1] = fmaf(hi, x1[i], a1[2 * k + 1]);
    }
  }
  float t[8];
#pragma unroll
  for (int c = 0; c < 8; ++c) t[c] = tr16(a0[c], a1[c]);
  f32x4 r;
#pragma unroll
  for (int c = 0; c < 4; ++c) {
    float v = tr32(t[c], t[c + 4]);
    v += dpp<XOR8>(v);
    r[c] = v;
  }
  return r;
}

__device__ __forceinline__ void gemv256(const v2u (&w)[8], const float (&c0)[8], const float (&c1)[8], f32x4& s0, f32x4& s1) {
  s0 = f32x4{0.f, 0.f, 0.f, 0.f};
  s1 = f32x4{0.f, 0.f, 0.f, 0.f};
#pragma unroll
  for (int k = 0; k < 8; ++k) {
    const f32x4 wf = {bflo(w[k].x), bfhi(w[k].x), bflo(w[k].y), bfhi(w[k].y)};
#pragma unroll
    for (int c = 0; c < 4; ++c) {
      s0[c] = fmaf(wf[c], c0[k], s0[c]);
      s1[c] = fmaf(wf[c], c1[k], s1[c]);
    }
  }
}

__device__ __forceinline__ v2u granule(float v, unsigned tag_lo, unsigned tag_hi) {
  return v2u{(tag_lo << 24) | ((f2u(v) + 0x80u) >> 8), tag_hi};
}
__device__ __forceinline__ bool tag2(v4u g, unsigned tag_lo, unsigned tag_hi) {
  return (g.y == tag_hi) & ((g.x >> 24) == tag_lo) & (g.w == tag_hi) & ((g.z >> 24) == tag_lo);
}
#define TOUCH4(a) asm volatile("" : "+v"(a[0]), "+v"(a[1]), "+v"(a[2]), "+v"(a[3]))
constexpr int AUX_NT = 2;
#define LDK(kb_, off_) do { _Pragma("unroll") for (int j = 0; j < 4; ++j) kb_[j] = __builtin_amdgcn_raw_buffer_load_b128(rk, (klane + (off_)) + j * 64, 0, AUX_NT); SB(); } while (0)
#define LDV(vb_, off_) do { _Pragma("unroll") for (int j = 0; j < 4; ++j) vb_[j] = __builtin_amdgcn_raw_buffer_load_b128(rv, (vlane + (off_)) + j * 1024, 0, AUX_NT); SB(); } while (0)
#define LDK2(kb_, a_) do { _Pragma("unroll") for (int j = 0; j < 4; ++j) kb_[j] = __builtin_amdgcn_raw_buffer_load_b128(rk, (a_) + j * 64, 0, AUX_NT); SB(); } while (0)
#define LDV2(vb_, a_) do { _Pragma("unroll") for (int j = 0; j < 4; ++j) vb_[j] = __builtin_amdgcn_raw_buffer_load_b128(rv, (a_) + j * 1024, 0, AUX_NT); SB(); } while (0)

struct AttnState {
  float m, l;
  f32x4 o[8];
};
__device__ __forceinline__ v2u attn_qk(AttnState& st, const v4u (&kc)[4], const v4u (&qf)[4]) {
  constexpr float C2 = 0.088388347648318440f * 1.4426950408889634f;
  f32x4 s = {0.f, 0.f, 0.f, 0.f};
#pragma unroll
  for (int kk = 0; kk < 4; ++kk)
    s = __builtin_amdgcn_mfma_f32_16x16x32_bf16(__builtin_bit_cast(bf16x8, kc[kk]), __builtin_bit_cast(bf16x8, qf[kk]), s, 0, 0, 0);
#pragma unroll
  for (int r = 0; r < 4; ++r) s[r] = (s[r] + dpp<XOR8>(s[r])) * C2;
  float mx = fmaxf(fmaxf(s[0], s[1]), fmaxf(s[2], s[3]));
  mx = x32_max(x16_max(mx));
  const float mn = fmaxf(st.m, mx);
  const float alpha = __builtin_amdgcn_exp2f(st.m - mn);
  st.m = mn;
  const float p0 = __builtin_amdgcn_exp2f(s[0] - mn), p1 = __builtin_amdgcn_exp2f(s[1] - mn);
  const float p2 = __builtin_amdgcn_exp2f(s[2] - mn), p3 = __builtin_amdgcn_exp2f(s[3] - mn);
  st.l = fmaf(st.l, alpha, (p0 + p1) + (p2 + p3));
  const v2u pb = {cvtpk(p0, p1), cvtpk(p2, p3)};
#pragma unroll
  for (int e = 0; e < 8; ++e) st.o[e] *= alpha;
  SB();
  return pb;
}
__device__ __forceinline__ void attn_pv(AttnState& st, const v4u (&vc)[4], v2u pb) {
#pragma unroll
  for (int e = 0; e < 8; ++e) {
    const unsigned sel = (e & 1) ? 0x07060302u : 0x05040100u;
    const v2u a = {__builtin_amdgcn_perm(vc[1][e >> 1], vc[0][e >> 1], sel), __builtin_amdgcn_perm(vc[3][e >> 1], vc[2][e >> 1], sel)};
    st.o[e] = __builtin_amdgcn_mfma_f32_16x16x16bf16_1k(__builtin_bit_cast(bf16x4, a), __builtin_bit_cast(bf16x4, pb), st.o[e], 0, 0, 0);
  }
  SB();
}
__device__ __forceinline__ void attn_unit(AttnState& st, const v4u (&kc)[4], const v4u (&vc)[4], const v4u (&qf)[4]) {
  attn_pv(st, vc, attn_qk(st, kc, qf));
}

#define DRAIN() asm volatile("s_waitcnt vmcnt(0)" ::: "memory")
constexpr int MV = 3;
typedef float f32x2v __attribute__((ext_vector_type(2)));
__device__ __forceinline__ void lds_signal(LAS unsigned* c) {
  asm volatile("s_waitcnt lgkmcnt(0)" ::: "memory");
  if ((threadIdx.x & 63) == 0) __hip_atomic_fetch_add(c, 1u, __ATOMIC_RELAXED, __HIP_MEMORY_SCOPE_WORKGROUP);
}
__device__ __forceinline__ void lds_wait(LAS unsigned* c, unsigned want) {
#pragma nounroll
  for (unsigned spins = 0; spins < (SPIN_CAP << 3); ++spins) {
    asm volatile("" ::: "memory");
    if (__builtin_amdgcn_readfirstlane(*(volatile LAS unsigned*)c) >= want) break;
    __builtin_amdgcn_s_sleep(1);
  }
  asm volatile("" ::: "memory");
}

__global__ void __launch_bounds__(NTHR) fused_decode(Params p) {
  __shared__ __attribute__((aligned(16))) unsigned char smem[L_BYTES];
  LAS unsigned char* L = (LAS unsigned char*)smem;
  LAS unsigned* FL = (LAS unsigned*)(L + L_FL);
  const int tid = (int)threadIdx.x - 64, lane = threadIdx.x & 63;
  const int w = __builtin_amdgcn_readfirstlane(threadIdx.x >> 6) - 1;
  const int bid = blockIdx.x;
  const int grp = bid >> 5, xq = bid & 7;
  const int cg = (bid >> 3) & 3, rg = grp * 8 + xq;
  const int hkv = bid >> 6, bb = grp & 1;
  const int xl = (3 - (int)((size_t)p.wq >> 7)) & 7;
  const bool has_ffn = xq != xl;
  const int fi = has_ffn ? grp * 7 + (xq < xl ? xq : xq - 1) : 0;
  const bool reducer = rg >= 48;
  constexpr int OOB = 0x7FFF0000;
  const __amdgpu_buffer_rsrc_t rs = __builtin_amdgcn_make_buffer_rsrc((void*)p.ws, 0, WS_ALL, 0x00020000);
  const int p1own = (bid & 7) * RSP + WS_P1 + (bid >> 3) * 4096;
  const int s2own = (rg & 7) * RSS + (cg * 8 + (rg >> 3)) * 2048;
  const GAS v4u* xg = (const GAS v4u*)p.x;

  if (w < 0) {
    __builtin_amdgcn_s_setprio(3);
    const v4u xa0 = xg[lane * 2], xa1 = xg[lane * 2 + 1], xb0 = xg[128 + lane * 2], xb1 = xg[128 + lane * 2 + 1];
    const v2u ev = __builtin_amdgcn_raw_buffer_load_b64(rs, WS_E, 0, AUX_SC1);
    if (lane < 6) FL[lane] = 0u;
    float rr;
    {
      float va_[16], vb_[16];
#pragma unroll
      for (int k = 0; k < 4; ++k) {
        va_[2 * k] = bflo(xa0[k]); va_[2 * k + 1] = bfhi(xa0[k]); va_[8 + 2 * k] = bflo(xa1[k]); va_[8 + 2 * k + 1] = bfhi(xa1[k]);
        vb_[2 * k] = bflo(xb0[k]); vb_[2 * k + 1] = bfhi(xb0[k]); vb_[8 + 2 * k] = bflo(xb1[k]); vb_[8 + 2 * k + 1] = bfhi(xb1[k]);
      }
      float sa = 0.f, sb = 0.f;
#pragma unroll
      for (int k = 0; k < 16; ++k) { sa += va_[k]; sb += vb_[k]; }
      const float ma = wave_sum(sa) * (1.0f / D), mb = wave_sum(sb) * (1.0f / D);
      if (lane == 0) *(LAS f32x2v*)(L + L_FL + 32) = f32x2v{ma, mb};
      WG_BARRIER();
      float qa = 0.f, qb = 0.f;
#pragma unroll
      for (int k = 0; k < 16; ++k) { const float da = va_[k] - ma, db = vb_[k] - mb; qa = fmaf(da, da, qa); qb = fmaf(db, db, qb); }
      const float ta = wave_sum(qa), tb = wave_sum(qb);
      rr = 1.0f / sqrtf((lane < 32 ? ta : tb) * (1.0f / D) + 1e-5f);
    }
    const u64 e1 = ((((u64)__builtin_amdgcn_readfirstlane(ev.y)) << 32) | (u64)(unsigned)__builtin_amdgcn_readfirstlane(ev.x)) + 1ull;
    const unsigned tag_hi = (unsigned)(e1 >> 8), tag_lo = (unsigned)(e1 & 0xFFull);
    const unsigned tq = (unsigned)e1 & 0xFFFFFFu;
    if (lane == 0) { FL[6] = (unsigned)e1; FL[7] = (unsigned)(e1 >> 32); }
    lds_wait(FL + 0, 8);
    {
      f32x2v a = {0.f, 0.f};
#pragma unroll
      for (int ww = 0; ww < 8; ++ww) a += *(LAS f32x2v*)(L + L_RED + ww * 512 + lane * 8);
      const unsigned v0 = (f2u(a.x * rr) + 0x800u) >> 12, v1 = (f2u(a.y * rr) + 0x800u) >> 12;
      __builtin_amdgcn_raw_buffer_store_b64(v2u{v0 | (v1 << 20), (v1 >> 12) | (tq << 8)}, rs,
          WS_QP + ((hkv * 2 + (lane >> 5)) * 4 + cg) * 4096 + (rg & 15) * 256 + (lane & 31) * 8, 0, AUX_SC1);
    }
    DRAIN();
    {
      const int qoff = WS_QP + (hkv * 2 + bb) * 16384 + lane * 16;
      v4u qg[16];
#pragma nounroll
      for (unsigned spins = 0; spins < SPIN_CAP; ++spins) {
        asm volatile("" ::: "memory");
        bool ok = true;
#pragma unroll
        for (int j = 0; j < 16; ++j) qg[j] = __builtin_amdgcn_raw_buffer_load_b128(rs, qoff + (j & 3) * 1024, (j >> 2) * 4096, AUX_SC1);
#pragma unroll
        for (int j = 0; j < 16; ++j) ok &= ((qg[j].y >> 8) == tq) & ((qg[j].w >> 8) == tq);
        if (__all(ok)) break;
        __builtin_amdgcn_s_sleep(4);
      }
#pragma unroll
      for (int j = 0; j < 4; ++j) {
        f32x4 q = {0.f, 0.f, 0.f, 0.f};
#pragma unroll
        for (int ks = 0; ks < 4; ++ks) {
          const v4u g = qg[ks * 4 + j];
          q += f32x4{u2f(g.x << 12), u2f((g.y << 24) | ((g.x >> 20) << 12)), u2f(g.z << 12), u2f((g.w << 24) | ((g.z >> 20) << 12))};
        }
        const v2u hi = {cvtpk(q.x, q.y), cvtpk(q.z, q.w)};
        const v2u lo = {cvtpk(q.x - bflo(hi.x), q.y - bfhi(hi.x)), cvtpk(q.z - bflo(hi.y), q.w - bfhi(hi.y))};
        *(LAS v2u*)(L + L_Q + (2 * j + (lane >> 5)) * 272 + (lane & 31) * 8) = hi;
        *(LAS v2u*)(L + L_Q + (2 * j + 8 + (lane >> 5)) * 272 + (lane & 31) * 8) = lo;
      }
      lds_signal(FL + 3);
    }
    const int coff = WS_QP + (((lane >> 4) * 2) * 4 + cg) * 4096 + (lane & 15) * 256;
    if (reducer) {
#pragma nounroll
      for (unsigned spins = 0; spins < SPIN_CAP; ++spins) {
        asm volatile("" ::: "memory");
        const v4u ck = __builtin_amdgcn_raw_buffer_load_b128(rs, coff, 0, AUX_SC1);
        if (__all(((ck.y >> 8) == tq) & ((ck.w >> 8) == tq))) break;
        __builtin_amdgcn_s_sleep(4);
      }
    }
    if (has_ffn) {
      lds_wait(FL + 1, 8);
      v4u gr[2];
#pragma unroll
      for (int m = 0; m < 2; ++m) {
        f32x2v a = {0.f, 0.f};
#pragma unroll
        for (int ww = 0; ww < 8; ++ww) a += *(LAS f32x2v*)(L + L_RED + 4096 + m * 4096 + ww * 512 + lane * 8);
        const v2u g0 = granule(a.x * rr, tag_lo, tag_hi), g1 = granule(a.y * rr, tag_lo, tag_hi);
        gr[m] = v4u{g0.x, g0.y, g1.x, g1.y};
      }
      lds_wait(FL + 2, 4);
#pragma unroll
      for (int m = 0; m < 2; ++m) __builtin_amdgcn_raw_buffer_store_b128(gr[m], rs, (cg * 2 + m) * RSG + WS_GU + rg * 1024 + lane * 16, 0, AUX_SC1);
    }
    WG_BARRIER();
    return;
  }

  {
    const v4u s4 = {SENT, SENT, SENT, SENT};
    const int off = w < 4 ? p1own + w * 1024 : (w < 6 ? WS_S2 - 4096 : WS_F2 - 6144) + s2own + w * 1024;
    __builtin_amdgcn_raw_buffer_store_b128(s4, rs, off + lane * 16, 0, AUX_SC1);
    SB();
  }
  const int r8 = lane >> 3, c8 = lane & 7;
  const int krow = cg * 256 + w * 32 + r8 * 4;
  const v2u xr0 = *(const GAS v2u*)(p.x + krow), xr1 = *(const GAS v2u*)(p.x + D + krow);
  v4u wqr[4], wgr[4], wur[4];
#pragma unroll
  for (int i = 0; i < 4; ++i) wqr[i] = ldnt((const GAS v4u*)(p.wq + (size_t)(krow + i) * HD + rg * 64 + c8 * 8));
  SB();
  const int fbytes = has_ffn ? D * FF * 2 : 0;
  const __amdgpu_buffer_rsrc_t rwg = __builtin_amdgcn_make_buffer_rsrc((void*)p.wg, 0, fbytes, 0x00020000);
  const __amdgpu_buffer_rsrc_t rwu = __builtin_amdgcn_make_buffer_rsrc((void*)p.wu, 0, fbytes, 0x00020000);
  const __amdgpu_buffer_rsrc_t rwd = __builtin_amdgcn_make_buffer_rsrc((void*)p.wd, 0, fbytes, 0x00020000);
  const __amdgpu_buffer_rsrc_t rwo = __builtin_amdgcn_make_buffer_rsrc((void*)p.wo, 0, HD * D * 2, 0x00020000);
  const int guoff = (krow * FF + fi * 64 + c8 * 8) * 2;
#define LDWG() do { _Pragma("unroll") for (int i = 0; i < 4; ++i) wgr[i] = __builtin_amdgcn_raw_buffer_load_b128(rwg, guoff + i * FF * 2, 0, AUX_NT); SB(); } while (0)
#define LDWU() do { _Pragma("unroll") for (int i = 0; i < 4; ++i) wur[i] = __builtin_amdgcn_raw_buffer_load_b128(rwu, guoff + i * FF * 2, 0, AUX_NT); SB(); } while (0)
#define USEQ() do { const f32x4 r = gemv64(wqr, x0, x1); if ((lane & 8) == 0) *(LAS f32x4*)(L + L_RED + w * 512 + redoff) = r; \
    asm volatile("s_waitcnt vmcnt(4)" ::: "memory"); lds_signal(FL + 0); } while (0)
#define USEG() do { const f32x4 r = gemv64(wgr, x0, x1); if ((lane & 8) == 0) *(LAS f32x4*)(L + L_RED + 4096 + w * 512 + redoff) = r; SB(); } while (0)
#define USEU() do { const f32x4 r = gemv64(wur, x0, x1); if ((lane & 8) == 0) *(LAS f32x4*)(L + L_RED + 8192 + w * 512 + redoff) = r; lds_signal(FL + 1); } while (0)
  LDWG();
  const int i16 = lane & 15, g4 = lane >> 4;
  const __amdgpu_buffer_rsrc_t rk = __builtin_amdgcn_make_buffer_rsrc((void*)p.kc, 0, 8 * SK * 256, 0x00020000);
  const __amdgpu_buffer_rsrc_t rv = __builtin_amdgcn_make_buffer_rsrc((void*)p.vc, 0, 8 * SK * 256, 0x00020000);
  const int klane = (4 * (i16 & 3) + (i16 >> 2)) * 256 + g4 * 16, vlane = lane * 16;
  const int rsl = (((3 - (int)((size_t)p.wg >> 7)) & 7) + grp) & 7;
  const int xs = rsl < xl ? rsl : rsl + 1;
  const int nunit = rsl == 7 ? 64 : (!has_ffn ? 64 + MV : (xq == xs ? 64 - MV : 64));
  const int obase = ((bb * 4 + hkv) * 2048 + cg * 512) * 4096;
  const int ubase = obase + xq * 262144, xbase = obase + xs * 262144 - MV * 4096;
#define UOFF(t_) (ubase + (t_) * 4096)
#define UOFFX(t_) (((t_) < 64 ? ubase : xbase) + (t_) * 4096)
  v4u ka[4], va[4], kb[4], vb[4], kc[4], vc[4];
  WG_BARRIER();
  float x0[4], x1[4];
  {
    const f32x2v mm = *(LAS f32x2v*)(L + L_FL + 32);
    x0[0] = bflo(xr0.x) - mm.x; x0[1] = bfhi(xr0.x) - mm.x; x0[2] = bflo(xr0.y) - mm.x; x0[3] = bfhi(xr0.y) - mm.x;
    x1[0] = bflo(xr1.x) - mm.y; x1[1] = bfhi(xr1.x) - mm.y; x1[2] = bflo(xr1.y) - mm.y; x1[3] = bfhi(xr1.y) - mm.y;
  }
  const int redoff = ((g4 & 1) * 64 + c8 * 8 + (g4 >> 1) * 4) * 4;

  USEQ(); LDWU();
  USEG(); LDK(ka, UOFF(w));
  USEU(); LDV(va, UOFF(w));
  TOUCH4(ka);
  LDK(kb, UOFF(w + 8));
  TOUCH4(va);
  LDV(vb, UOFF(w + 8));
  TOUCH4(kb);
  LDK(kc, UOFF(w + 16));
  TOUCH4(vb);
  LDV(vc, UOFF(w + 16));
  lds_wait(FL + 3, 1);
  v4u qf[4];
#pragma unroll
  for (int kk = 0; kk < 4; ++kk) qf[kk] = *(LAS v4u*)(L + L_Q + i16 * 272 + kk * 64 + g4 * 16);

  AttnState st;
  st.m = -INFINITY;
  st.l = 0.f;
#pragma unroll
  for (int e = 0; e < 8; ++e) st.o[e] = f32x4{0.f, 0.f, 0.f, 0.f};
  attn_unit(st, ka, va, qf);
  attn_unit(st, kb, vb, qf);
  int t = w + 24;
  for (;;) {
    const int o0 = t < nunit ? UOFFX(t) : OOB;
    const int ak0 = klane + o0, av0 = vlane + o0;
    LDK2(kb, ak0);
    LDV2(vb, av0);
    asm volatile("" ::"v"(ak0), "v"(av0));
    attn_unit(st, kc, vc, qf);
    if (t >= nunit) break;
    const int o1 = t + 8 < nunit ? UOFFX(t + 8) : OOB;
    const int ak1 = klane + o1, av1 = vlane + o1;
    LDK2(kc, ak1);
    LDV2(vc, av1);
    asm volatile("" ::"v"(ak1), "v"(av1));
    attn_unit(st, kb, vb, qf);
    if (t + 8 >= nunit) break;
    t += 16;
  }
  lds_signal(FL + 2);
  const int sfoff = ((tid >> 2) & 1) * 1024 + ((tid & 127) >> 3) * 64 + (tid & 3) * 16;
  const int oidx = ((tid >> 4) & 1) * D + cg * 256 + (rg & 15) * 16 + (tid & 15);
  unsigned xo = 0;
  if (w == 0) xo = *(const GAS unsigned short*)(p.x + oidx);

  {
    const float l = x32_sum(x16_sum(st.l));
    if (i16 < 8) {
#pragma unroll
      for (int e = 0; e < 8; ++e) *(LAS f32x4*)(L + L_O + (w * 256 + (i16 * 4 + g4) * 8 + (e ^ i16)) * 16) = st.o[e];
      if (g4 == 0) *(LAS v2u*)(L + L_ML + (i16 * 8 + w) * 8) = v2u{f2u(st.m), f2u(l)};
    }
  }
  int poff = g4 * RSP + WS_P1 + (hkv * 8 + w) * 4096 + (rg & 15) * 256 + i16 * 16;
  int goff = (lane >> 3) * RSG + WS_GU + rg * 1024 + ((lane >> 2) & 1) * 512 + (w * 8 + (lane & 3) * 2) * 8;
  int roff = ((tid >> 3) & 7) * RSS + (cg * 8 + (tid >> 6)) * 2048 + (rg & 15) * 128 + (tid & 7) * 16;
  int p1st = p1own + tid * 16;
  int s2st = (w < 2 ? WS_S2 : WS_F2) + s2own + (tid & 127) * 16;
  __builtin_amdgcn_s_waitcnt(0x0F70);
  asm volatile("" : "+v"(poff), "+v"(goff), "+v"(roff), "+v"(p1st), "+v"(s2st));
  WG_BARRIER();
  if (w < 4) {
    const int j = tid >> 5;
    v4u ml[4];
    f32x4 ov[8];
#pragma unroll
    for (int k = 0; k < 4; ++k) ml[k] = *(LAS v4u*)(L + L_ML + j * 64 + k * 16);
#pragma unroll
    for (int ww = 0; ww < 8; ++ww) ov[ww] = *(LAS f32x4*)(L + L_O + (ww * 256 + (tid ^ j)) * 16);
    SB();
    float M = fmaxf(u2f(ml[0].x), u2f(ml[0].z));
#pragma unroll
    for (int k = 1; k < 4; ++k) M = fmaxf(M, fmaxf(u2f(ml[k].x), u2f(ml[k].z)));
    float Ls = 0.f;
    f32x4 acc = {0.f, 0.f, 0.f, 0.f};
#pragma unroll
    for (int ww = 0; ww < 8; ++ww) {
      const float wt = __builtin_amdgcn_exp2f(u2f(ml[ww >> 1][(ww & 1) * 2]) - M);
      Ls = fmaf(wt, u2f(ml[ww >> 1][(ww & 1) * 2 + 1]), Ls);
      acc += ov[ww] * wt;
    }
    acc *= rcp(Ls);
    const unsigned lse = f2u(M + __builtin_amdgcn_logf(Ls));
    v4u pk;
#pragma unroll
    for (int r = 0; r < 4; ++r) {
      const unsigned t = ((f2u(acc[r]) + 0x80u) & 0xFFFFFF00u) | ((lse >> (8 * r)) & 0xFFu);
      pk[r] = t == SENT ? 0x7FC000FFu : t;
    }
    __builtin_amdgcn_raw_buffer_store_b128(pk, rs, p1st, 0, AUX_SC1);
  }
  SB();

  f32x4 wof[8];
  {
    v4u gu = __builtin_amdgcn_raw_buffer_load_b128(rs, goff, 0, AUX_SC1);
    SB();
    v2u wt[8], wor[8];
#pragma unroll
    for (int k = 0; k < 8; ++k) wt[k] = __builtin_amdgcn_raw_buffer_load_b64(rwd, lane * 8, ((fi * 64 + w * 8 + k) * D + cg * 256) * 2, AUX_NT);
    SB();
#pragma unroll
    for (int k = 0; k < 8; ++k) wor[k] = __builtin_amdgcn_raw_buffer_load_b64(rwo, lane * 8, ((rg * 64 + w * 8 + k) * D + cg * 256) * 2, AUX_NT);
    SB();
    v4u pa = __builtin_amdgcn_raw_buffer_load_b128(rs, poff, 0, AUX_SC1);
    v4u pb = __builtin_amdgcn_raw_buffer_load_b128(rs, poff, 4 * RSP, AUX_SC1);
    SB();
    if (has_ffn) {
      const unsigned e_lo = FL[6], e_hi = FL[7];
      const unsigned tag_hi = (e_lo >> 8) | (e_hi << 24), tag_lo = e_lo & 0xFFu;
      if (!__all(tag2(gu, tag_lo, tag_hi))) {
#pragma nounroll
        for (unsigned spins = 0; spins < SPIN_CAP; ++spins) {
          asm volatile("" ::: "memory");
          __builtin_amdgcn_s_sleep(1);
          gu = __builtin_amdgcn_raw_buffer_load_b128(rs, goff, 0, AUX_SC1);
          if (__all(tag2(gu, tag_lo, tag_hi))) break;
        }
      }
      const float ga = x32_sum(x16_sum(u2f(gu.x << 8))), gb = x32_sum(x16_sum(u2f(gu.z << 8)));
      const float hx = silu(ga) * dpp<XOR8>(ga), hy = silu(gb) * dpp<XOR8>(gb);
      float h0[8], h1[8];
#pragma unroll
      for (int pr = 0; pr < 4; ++pr) {
        h0[2 * pr] = rdl(hx, pr); h0[2 * pr + 1] = rdl(hy, pr);
        h1[2 * pr] = rdl(hx, 4 + pr); h1[2 * pr + 1] = rdl(hy, 4 + pr);
      }
      f32x4 s0, s1;
      gemv256(wt, h0, h1, s0, s1);
      *(LAS f32x4*)(L + L_F + w * 2048 + lane * 16) = s0;
      *(LAS f32x4*)(L + L_F + w * 2048 + 1024 + lane * 16) = s1;
    }
#pragma unroll
    for (int k = 0; k < 8; ++k) {
      const v2u t = wor[k];
      wof[k] = f32x4{bflo(t.x), bfhi(t.x), bflo(t.y), bfhi(t.y)};
      asm volatile("" : "+v"(wof[k]));
    }
    bool ha = full4(pa), hb = full4(pb);
#pragma nounroll
    for (unsigned spins = 0; spins < SPIN_CAP; ++spins) {
      asm volatile("" ::: "memory");
      if (__all(ha & hb)) break;
      if (!ha) pa = __builtin_amdgcn_raw_buffer_load_b128(rs, poff, 0, AUX_SC1);
      if (!hb) pb = __builtin_amdgcn_raw_buffer_load_b128(rs, poff, 4 * RSP, AUX_SC1);
      ha = full4(pa);
      hb = full4(pb);
    }
    const float la = u2f(__builtin_amdgcn_perm(__builtin_amdgcn_perm(pa.w, pa.z, 0x0c0c0400u), __builtin_amdgcn_perm(pa.y, pa.x, 0x0c0c0400u), 0x05040100u));
    const float lb = u2f(__builtin_amdgcn_perm(__builtin_amdgcn_perm(pb.w, pb.z, 0x0c0c0400u), __builtin_amdgcn_perm(pb.y, pb.x, 0x0c0c0400u), 0x05040100u));
    const float Mw = x32_max(x16_max(fmaxf(la, lb)));
    const float wa = __builtin_amdgcn_exp2f(la - Mw), wb = __builtin_amdgcn_exp2f(lb - Mw);
    const float Ww = x32_sum(x16_sum(wa + wb));
    float a[4];
#pragma unroll
    for (int r = 0; r < 4; ++r) a[r] = fmaf(u2f(pa[r] & 0xFFFFFF00u), wa, u2f(pb[r] & 0xFFFFFF00u) * wb);
    const float v = tr32(tr16(a[0], a[1]), tr16(a[2], a[3]));
    *(LAS float*)(L + L_C + (((w >> 2) * 64 + (i16 >> 3) * 32 + g4 * 8 + (i16 & 7)) * 4 + (w & 3)) * 4) = v;
    if (lane == 0) *(LAS v2u*)(L + L_C + 2048 + w * 8) = v2u{f2u(Mw), f2u(Ww)};
  }
  WG_BARRIER();
  {
    const int b = (lane >> 3) & 1, k = lane & 7;
    const v4u m0 = *(LAS v4u*)(L + L_C + 2048 + b * 32), m1 = *(LAS v4u*)(L + L_C + 2048 + b * 32 + 16);
    const f32x4 av = *(LAS f32x4*)(L + L_C + (b * 64 + w * 8 + k) * 16);
    const float M = fmaxf(fmaxf(u2f(m0.x), u2f(m0.z)), fmaxf(u2f(m1.x), u2f(m1.z)));
    const float f0 = __builtin_amdgcn_exp2f(u2f(m0.x) - M), f1 = __builtin_amdgcn_exp2f(u2f(m0.z) - M);
    const float f2 = __builtin_amdgcn_exp2f(u2f(m1.x) - M), f3 = __builtin_amdgcn_exp2f(u2f(m1.z) - M);
    const float num = fmaf(f0, av.x, f1 * av.y) + fmaf(f2, av.z, f3 * av.w);
    const float den = fmaf(f0, u2f(m0.y), f1 * u2f(m0.w)) + fmaf(f2, u2f(m1.y), f3 * u2f(m1.w));
    const float ov = num * rcp(den);
    f32x4 s0 = {0.f, 0.f, 0.f, 0.f}, s1 = {0.f, 0.f, 0.f, 0.f};
#pragma unroll
    for (int kk = 0; kk < 8; ++kk) { s0 += wof[kk] * rdl(ov, kk); s1 += wof[kk] * rdl(ov, 8 + kk); }
    *(LAS f32x4*)(L + L_S + w * 2048 + lane * 16) = s0;
    *(LAS f32x4*)(L + L_S + w * 2048 + 1024 + lane * 16) = s1;
  }
  WG_BARRIER();
  if (w < 2 || (w < 4 && has_ffn)) {
    const int lb = (w < 2 ? L_S : L_F) + sfoff;
    f32x4 t[8];
#pragma unroll
    for (int ww = 0; ww < 8; ++ww) t[ww] = *(LAS f32x4*)(L + lb + ww * 2048);
    SB();
    const f32x4 s4 = ((t[0] + t[1]) + (t[2] + t[3])) + ((t[4] + t[5]) + (t[6] + t[7]));
    __builtin_amdgcn_raw_buffer_store_b128(clean4(s4), rs, s2st, 0, AUX_SC1);
  }

  if (reducer) {
    v4u a = __builtin_amdgcn_raw_buffer_load_b128(rs, roff, WS_S2, AUX_SC1);
    v4u c = {0u, 0u, 0u, 0u};
    if (((tid >> 3) & 7) != xl) c = __builtin_amdgcn_raw_buffer_load_b128(rs, roff, WS_F2, AUX_SC1);
    bool ha = full4(a), hc = full4(c);
#pragma nounroll
    for (unsigned spins = 0; spins < SPIN_CAP; ++spins) {
      asm volatile("" ::: "memory");
      if (__all(ha & hc)) break;
      if (!ha) a = __builtin_amdgcn_raw_buffer_load_b128(rs, roff, WS_S2, AUX_SC1);
      if (!hc) c = __builtin_amdgcn_raw_buffer_load_b128(rs, roff, WS_F2, AUX_SC1);
      ha = full4(a);
      hc = full4(c);
    }
    float vs = tr32(tr16(u2f(a.x), u2f(a.y)), tr16(u2f(a.z), u2f(a.w)));
    float vf = tr32(tr16(u2f(c.x), u2f(c.y)), tr16(u2f(c.z), u2f(c.w)));
    vs += dpp<XOR8>(vs);
    vf += dpp<XOR8>(vf);
    *(LAS float*)(L + L_R + ((lane >> 3) & 1) * 1024 + ((lane & 7) * 4 + (lane >> 4)) * 32 + w * 4) = (lane & 8) ? vs : vf;
    WG_BARRIER();
    if (tid < 32) {
      const f32x4 f0 = *(LAS f32x4*)(L + L_R + tid * 32), f1 = *(LAS f32x4*)(L + L_R + tid * 32 + 16);
      const f32x4 s0 = *(LAS f32x4*)(L + L_R + 1024 + tid * 32), s1 = *(LAS f32x4*)(L + L_R + 1024 + tid * 32 + 16);
      const f32x4 f4 = f0 + f1, s4 = s0 + s1;
      const float f = (f4.x + f4.y) + (f4.z + f4.w), sv = (s4.x + s4.y) + (s4.z + s4.w);
      const float r = fmaf(sv, silu(f), bflo(xo));
      p.out[oidx] = (unsigned short)(cvtpk(r, 0.f) & 0xFFFFu);
    }
    if (bid == NWG - 1 && tid == 0) {
      const v2u ne = {FL[6], FL[7]};
      __builtin_amdgcn_raw_buffer_store_b64(ne, rs, WS_E, 0, AUX_SC1);
    }
  }
}

}

extern "C" void kernel_launch(void* const* d_in, const int* in_sizes, int n_in, void* d_out, int out_size, void* d_ws,
                              size_t ws_size, hipStream_t stream) {
  Params p;
  p.x = (const unsigned short*)d_in[0];
  p.kc = (const unsigned short*)d_in[1];
  p.vc = (const unsigned short*)d_in[2];
  p.wq = (const unsigned short*)d_in[3];
  p.wo = (const unsigned short*)d_in[4];
  p.wg = (const unsigned short*)d_in[6];
  p.wu = (const unsigned short*)d_in[7];
  p.wd = (const unsigned short*)d_in[8];
  p.out = (unsigned short*)d_out;
  p.ws = (unsigned char*)d_ws;
  hipLaunchKernelGGL(fused_decode, dim3(NWG), dim3(NTHR), 0, stream, p);
}
```
